```python
import torch

def _softmax(x, axis=-1):
    return torch.softmax(x, dim=axis)
def _gelu(x):
    return torch.nn.functional.gelu(x, approximate="tanh")
def _silu(x):
    return torch.nn.functional.silu(x)
def _sigmoid(x):
    return torch.sigmoid(x)
def _layernorm(x, axis=-1, eps=1e-5):
    m = x.mean(dim=axis, keepdim=True)
    v = x.var(dim=axis, keepdim=True, unbiased=False)
    return (x - m) / torch.sqrt(v + eps)
def _scatter_add(tgt, idx, src, axis=0):
    return tgt.clone().index_add_(axis, idx, src)
def _segment_sum(src, seg, n):
    out = torch.zeros((n,) + src.shape[1:], dtype=src.dtype)
    return out.index_add_(0, seg, src)
def _pad(x, pw):
    flat = tuple(v for pair in reversed(pw) for v in pair)
    return torch.nn.functional.pad(x, flat)

kernel_name = 'standalone__mha_block_cached__near_ridge'

def setup_inputs():

    _w = (lambda a: (lambda t: t.to(torch.bfloat16) if t.is_floating_point() else t)(torch.as_tensor(a)))
    B, Sq, D = 16, 192, 1024
    H, Sk, Dh = 32, 1024, 128
    return {
        "x": _w((lambda *s: torch.randn(*s, dtype=torch.bfloat16) * 0.8)(B, Sq, D)),
        "k_cache": _w((lambda *s: torch.randn(*s, dtype=torch.bfloat16) * 0.8)(B, H, Sk, Dh)),
        "v_cache": _w((lambda *s: torch.randn(*s, dtype=torch.bfloat16) * 0.05)(B, H, Sk, Dh)),
        "wq": _w((lambda *s: torch.randn(*s, dtype=torch.bfloat16) * 0.8)(D, H * Dh) * (D ** -0.5)),
        "wo": _w((lambda *s: torch.randn(*s, dtype=torch.bfloat16) * 0.05)(H * Dh, D) * ((H * Dh) ** -0.5)),
    }

def reference(x, k_cache, v_cache, wq, wo):
    B, Sq, D = x.shape
    H, Dh = 32, 128
    q = (lambda x, *d: x.permute(*d))((x @ wq).reshape(B, Sq, H, Dh), 0, 2, 1, 3)
    scores = torch.einsum("bhqd,bhkd->bhqk", q, k_cache) * (Dh ** -0.5)
    p = _softmax(scores, axis=-1)
    o = torch.einsum("bhqk,bhkd->bhqd", p, v_cache)
    return (lambda x, *d: x.permute(*d))(o, 0, 2, 1, 3).reshape(B, Sq, H * Dh) @ wo
```

```cpp
#include <hip/hip_runtime.h>
#include <hip/hip_bf16.h>
typedef unsigned short u16;
typedef short bf16x8 __attribute__((ext_vector_type(8)));
typedef float f32x4 __attribute__((ext_vector_type(4)));
typedef float f32x16 __attribute__((ext_vector_type(16)));
namespace base {
constexpr int B=16, SQ=192, D=1024, H=32, SK=1024, DH=128;
__device__ __forceinline__ float bf2f(u16 v){ return __uint_as_float(((unsigned)v)<<16); }
__device__ __forceinline__ u16 f2bf(float f){ unsigned u=__float_as_uint(f); u += 0x7fffu + ((u>>16)&1u); return (u16)(u>>16); }

__global__ __launch_bounds__(256) void gemm_nn(const u16* __restrict__ A, const u16* __restrict__ Bm, u16* __restrict__ C, int M, int N, int K, float scale){
  __shared__ u16 As[64][40];
  __shared__ u16 Bs[64][40];
  int tid=threadIdx.x, lane=tid&63, w=tid>>6, wr=w>>1, wc=w&1;
  int m0=blockIdx.y*64, n0=blockIdx.x*64;
  f32x16 acc={};
  for(int k0=0;k0<K;k0+=32){
    { int r=tid>>2, c=(tid&3)*8; *(bf16x8*)&As[r][c] = *(const bf16x8*)&A[(size_t)(m0+r)*K+k0+c]; }
    { int kk=tid>>3, c=(tid&7)*8; bf16x8 v=*(const bf16x8*)&Bm[(size_t)(k0+kk)*N+n0+c]; for(int j=0;j<8;j++) Bs[c+j][kk]=(u16)v[j]; }
    __syncthreads();
    for(int s=0;s<2;s++){
      bf16x8 a=*(bf16x8*)&As[wr*32+(lane&31)][s*16+(lane>>5)*8];
      bf16x8 b=*(bf16x8*)&Bs[wc*32+(lane&31)][s*16+(lane>>5)*8];
      acc=__builtin_amdgcn_mfma_f32_32x32x16_bf16(a,b,acc,0,0,0);
    }
    __syncthreads();
  }
  for(int r=0;r<16;r++){ int row=(r&3)+8*(r>>2)+4*(lane>>5); C[(size_t)(m0+wr*32+row)*N+n0+wc*32+(lane&31)]=f2bf(acc[r]*scale); }
}
__global__ __launch_bounds__(256) void attn_naive(const u16* __restrict__ Q, const u16* __restrict__ Kc, const u16* __restrict__ Vc, u16* __restrict__ O){
  __shared__ float qs[8][DH];
  __shared__ float ps[8][SK];
  __shared__ float red[8][4];
  int tid=threadIdx.x; int qg=blockIdx.x, h=blockIdx.y, b=blockIdx.z;
  for(int i=tid;i<8*DH;i+=256){ int r=i/DH,d=i%DH; qs[r][d]=bf2f(Q[(size_t)(b*SQ+qg*8+r)*(H*DH)+h*DH+d]); }
  __syncthreads();
  const u16* Kh=Kc+(size_t)(b*H+h)*SK*DH; const u16* Vh=Vc+(size_t)(b*H+h)*SK*DH;
  for(int k=tid;k<SK;k+=256){
    float s[8]={0,0,0,0,0,0,0,0};
    for(int d=0;d<DH;d+=8){ bf16x8 kv=*(const bf16x8*)&Kh[(size_t)k*DH+d]; for(int j=0;j<8;j++){ float kk=bf2f((u16)kv[j]); for(int r=0;r<8;r++) s[r]+=qs[r][d+j]*kk; } }
    for(int r=0;r<8;r++) ps[r][k]=s[r]*0.088388347648318440f;
  }
  __syncthreads();
  int w=tid>>6, lane=tid&63;
  for(int r=w*2;r<w*2+2;r++){
    float m=-INFINITY; for(int k=lane;k<SK;k+=64) m=fmaxf(m,ps[r][k]);
    for(int o=32;o;o>>=1) m=fmaxf(m,__shfl_xor(m,o));
    float l=0; for(int k=lane;k<SK;k+=64){ float e=__expf(ps[r][k]-m); ps[r][k]=e; l+=e; }
    for(int o=32;o;o>>=1) l+=__shfl_xor(l,o);
    if(lane==0) red[r][0]=1.f/l;
  }
  __syncthreads();
  { int d=tid&127, rh=tid>>7; float o[4]={0,0,0,0};
    for(int k=0;k<SK;k++){ float v=bf2f(Vh[(size_t)k*DH+d]); for(int r=0;r<4;r++) o[r]+=ps[rh*4+r][k]*v; }
    for(int r=0;r<4;r++) O[(size_t)(b*SQ+qg*8+rh*4+r)*(H*DH)+h*DH+d]=f2bf(o[r]*red[rh*4+r][0]); }
}
static void launch(void* const* d_in, void* d_out, void* d_ws, hipStream_t stream){
  const u16* x=(const u16*)d_in[0]; const u16* kc=(const u16*)d_in[1]; const u16* vc=(const u16*)d_in[2];
  const u16* wq=(const u16*)d_in[3]; const u16* wo=(const u16*)d_in[4];
  u16* q=(u16*)d_ws; u16* o=q+(size_t)B*SQ*H*DH;
  gemm_nn<<<dim3(H*DH/64,B*SQ/64),256,0,stream>>>(x,wq,q,B*SQ,H*DH,D,1.f);
  attn_naive<<<dim3(SQ/8,H,B),256,0,stream>>>(q,kc,vc,o);
  gemm_nn<<<dim3(D/64,B*SQ/64),256,0,stream>>>(o,wo,(u16*)d_out,B*SQ,D,H*DH,1.f);
}
}
extern "C" void kernel_launch(void* const* d_in, const int* in_sizes, int n_in, void* d_out, int out_size, void* d_ws, size_t ws_size, hipStream_t stream){
  base::launch(d_in,d_out,d_ws,stream);
}
```

```cpp
#include <hip/hip_runtime.h>
#include <hip/hip_bf16.h>
__global__ __launch_bounds__(256, 1) void mha_fused(const char* x, const char* kc, const char* vc, const char* wq, const char* wo, char* out, char* ws) {
  __shared__ __attribute__((aligned(16))) char lds[163840];
  const unsigned w = (unsigned)__builtin_amdgcn_readfirstlane((int)(threadIdx.x >> 6));
  asm volatile(
"K1_START:\n"
"s_load_dwordx8 s[8:15], %[ka], 0x0\n"
"s_load_dwordx4 s[16:19], %[ka], 0x20\n"
"s_load_dwordx2 s[20:21], %[ka], 0x30\n"
"v_mbcnt_lo_u32_b32 v57, -1, 0\n"
"v_mbcnt_hi_u32_b32 v57, -1, v57\n"
"s_getpc_b64 s[72:73]\n"
"K1_PC:\n"
"s_lshl_b32 s93, %[w], 13\n"
"v_lshl_add_u32 v52, v57, 7, s93\n"
"global_load_dword v37, v52, s[72:73]\n"
"s_bfe_u32 s22, %[wg], 0x30003\n"
"s_and_b32 s31, %[wg], 4\n"
"s_lshl_b32 s31, s31, 1\n"
"s_mov_b32 s27, s22\n"
"s_or_b32 s22, s22, s31\n"
"s_and_b32 s23, %[wg], 3\n"
"s_lshl_b32 s23, s23, 2\n"
"s_lshr_b32 s31, %[wg], 6\n"
"s_add_u32 s23, s23, s31\n"
"s_and_b32 s25, %[wg], 7\n"
"s_lshl_b32 s25, s25, 1\n"
"s_and_b32 s30, s27, 1\n"
"s_lshl_b32 s31, s23, 9\n"
"s_sub_u32 s24, 0x800000, s31\n"
"s_mul_i32 s26, s22, 0x60000\n"
"s_mov_b32 s52, %[w]\n"
"s_lshl_b32 s80, s25, 7\n"
"s_lshl_b32 s81, s25, 19\n"
"s_mov_b32 s94, s30\n"
"s_and_b32 s69, s52, 1\n"
"s_lshr_b32 s70, s52, 1\n"
"v_bfe_u32 v48, v57, 2, 3\n"
"v_lshrrev_b32 v49, 5, v57\n"
"v_lshlrev_b32 v49, 6, v49\n"
"v_and_b32 v50, 3, v57\n"
"s_lshl_b32 s65, s70, 8\n"
"s_lshl_b32 s66, s69, 18\n"
"s_add_u32 s65, s65, s66\n"
"s_mov_b32 s40, s65\n"
"s_sub_u32 s42, s24, s65\n"
"s_mov_b32 s43, 0x20000\n"
"s_mul_i32 s68, s70, 40960\n"
"s_lshl_b32 s66, s69, 12\n"
"s_add_u32 s68, s68, s66\n"
"v_lshlrev_b32 v52, 13, v48\n"
"v_add_u32 v52, v52, v49\n"
"v_lshl_add_u32 v52, v50, 4, v52\n"
"v_add_u32 v24, 0, v52\n"
"v_add_u32 v25, 64512, v52\n"
"v_add_u32 v26, 129024, v52\n"
"v_add_u32 v27, 193536, v52\n"
"s_mul_i32 s65, s52, 98304\n"
"s_mov_b32 s36, s65\n"
"s_mov_b32 s38, 98304\n"
"s_mov_b32 s39, 0x20000\n"
"s_mul_i32 s67, s52, 6144\n"
"s_add_u32 s67, s67, 81920\n"
"v_lshl_add_u32 v52, v48, 11, v49\n"
"v_lshl_add_u32 v53, v50, 4, v52\n"
"v_xor_b32 v54, 32, v53\n"
"v_add_u32 v28, 0, v53\n"
"v_add_u32 v29, 15360, v54\n"
"v_add_u32 v30, 30720, v53\n"
"v_add_u32 v31, 49152, v54\n"
"v_add_u32 v32, 64512, v53\n"
"v_add_u32 v33, 79872, v54\n"
"s_waitcnt lgkmcnt(0)\n"
"s_add_u32 s14, s14, s31\n"
"s_addc_u32 s15, s15, 0\n"
"s_add_u32 s8, s8, s26\n"
"s_addc_u32 s9, s9, 0\n"
"s_add_u32 s40, s14, s40\n"
"s_addc_u32 s41, s15, 0\n"
"s_and_b32 s41, s41, 0xffff\n"
"s_add_u32 s36, s8, s36\n"
"s_addc_u32 s37, s9, 0\n"
"s_and_b32 s37, s37, 0xffff\n"
"s_add_u32 s65, s81, 0\n"
"s_add_u32 m0, s68, 0\n"
"s_and_b32 s65, s65, 0x7fffff\n"
"buffer_load_dwordx4 v24, s[40:43], s65 offen offset:0 lds\n"
"buffer_load_dwordx4 v25, s[40:43], s65 offen offset:1024 lds\n"
"buffer_load_dwordx4 v26, s[40:43], s65 offen offset:2048 lds\n"
"buffer_load_dwordx4 v27, s[40:43], s65 offen offset:3072 lds\n"
"s_add_u32 s66, s80, 0\n"
"s_add_u32 m0, s67, 24576\n"
"s_and_b32 s66, s66, 0x7ff\n"
"buffer_load_dwordx4 v28, s[36:39], s66 offen offset:0 lds\n"
"buffer_load_dwordx4 v29, s[36:39], s66 offen offset:1024 lds\n"
"buffer_load_dwordx4 v30, s[36:39], s66 offen offset:2048 lds\n"
"s_add_u32 s66, s80, 0\n"
"s_add_u32 m0, s67, 27648\n"
"s_and_b32 s66, s66, 0x7ff\n"
"buffer_load_dwordx4 v31, s[36:39], s66 offen offset:0 lds\n"
"buffer_load_dwordx4 v32, s[36:39], s66 offen offset:1024 lds\n"
"buffer_load_dwordx4 v33, s[36:39], s66 offen offset:2048 lds\n"
"s_add_u32 s65, s81, 128\n"
"s_add_u32 m0, s68, 8192\n"
"s_and_b32 s65, s65, 0x7fffff\n"
"buffer_load_dwordx4 v24, s[40:43], s65 offen offset:0 lds\n"
"buffer_load_dwordx4 v25, s[40:43], s65 offen offset:1024 lds\n"
"buffer_load_dwordx4 v26, s[40:43], s65 offen offset:2048 lds\n"
"buffer_load_dwordx4 v27, s[40:43], s65 offen offset:3072 lds\n"
"s_add_u32 s66, s80, 128\n"
"s_add_u32 m0, s67, 49152\n"
"s_and_b32 s66, s66, 0x7ff\n"
"buffer_load_dwordx4 v28, s[36:39], s66 offen offset:0 lds\n"
"buffer_load_dwordx4 v29, s[36:39], s66 offen offset:1024 lds\n"
"buffer_load_dwordx4 v30, s[36:39], s66 offen offset:2048 lds\n"
"s_add_u32 s66, s80, 128\n"
"s_add_u32 m0, s67, 52224\n"
"s_and_b32 s66, s66, 0x7ff\n"
"buffer_load_dwordx4 v31, s[36:39], s66 offen offset:0 lds\n"
"buffer_load_dwordx4 v32, s[36:39], s66 offen offset:1024 lds\n"
"buffer_load_dwordx4 v33, s[36:39], s66 offen offset:2048 lds\n"
"s_add_u32 s65, s81, 524288\n"
"s_add_u32 m0, s68, 16384\n"
"s_and_b32 s65, s65, 0x7fffff\n"
"buffer_load_dwordx4 v24, s[40:43], s65 offen offset:0 lds\n"
"buffer_load_dwordx4 v25, s[40:43], s65 offen offset:1024 lds\n"
"buffer_load_dwordx4 v26, s[40:43], s65 offen offset:2048 lds\n"
"buffer_load_dwordx4 v27, s[40:43], s65 offen offset:3072 lds\n"
"s_add_u32 s65, s81, 524416\n"
"s_add_u32 m0, s68, 24576\n"
"s_and_b32 s65, s65, 0x7fffff\n"
"buffer_load_dwordx4 v24, s[40:43], s65 offen offset:0 lds\n"
"buffer_load_dwordx4 v25, s[40:43], s65 offen offset:1024 lds\n"
"buffer_load_dwordx4 v26, s[40:43], s65 offen offset:2048 lds\n"
"buffer_load_dwordx4 v27, s[40:43], s65 offen offset:3072 lds\n"
"s_add_u32 s66, s80, 256\n"
"s_add_u32 m0, s67, 0\n"
"s_and_b32 s66, s66, 0x7ff\n"
"buffer_load_dwordx4 v28, s[36:39], s66 offen offset:0 lds\n"
"buffer_load_dwordx4 v29, s[36:39], s66 offen offset:1024 lds\n"
"buffer_load_dwordx4 v30, s[36:39], s66 offen offset:2048 lds\n"
"s_add_u32 s66, s80, 256\n"
"s_add_u32 m0, s67, 3072\n"
"s_and_b32 s66, s66, 0x7ff\n"
"buffer_load_dwordx4 v31, s[36:39], s66 offen offset:0 lds\n"
"buffer_load_dwordx4 v32, s[36:39], s66 offen offset:1024 lds\n"
"buffer_load_dwordx4 v33, s[36:39], s66 offen offset:2048 lds\n"
"s_add_u32 s18, s18, s26\n"
"s_addc_u32 s19, s19, 0\n"
"s_lshl_b32 s31, s23, 1\n"
"s_add_u32 s31, s31, s70\n"
"s_cmp_eq_u32 s69, 0\n"
"s_cselect_b32 s10, s10, s12\n"
"s_cselect_b32 s11, s11, s13\n"
"s_lshl_b32 s26, s22, 5\n"
"s_add_u32 s26, s26, s31\n"
"s_lshl_b32 s12, s26, 18\n"
"s_sub_u32 s12, s12, 1024\n"
"s_ashr_i32 s13, s12, 31\n"
"s_add_u32 s10, s10, s12\n"
"s_addc_u32 s11, s11, s13\n"
"s_mul_i32 s26, s26, 37\n"
"s_and_b32 s26, s26, 63\n"
"s_lshl_b32 s26, s26, 12\n"
"s_mov_b32 s82, s26\n"
"s_lshl_b32 s12, s22, 1\n"
"s_add_u32 s13, s12, s69\n"
"s_lshl_b32 s13, s13, 7\n"
"s_lshl_b32 s31, s31, 2\n"
"s_add_u32 s13, s13, s31\n"
"s_mul_i32 s13, s13, 6144\n"
"s_add_u32 s33, s12, s70\n"
"s_lshl_b32 s33, s33, 7\n"
"s_add_u32 s33, s33, s69\n"
"s_mul_i32 s33, s33, 6144\n"
"s_add_u32 s12, s20, s13\n"
"s_addc_u32 s13, s21, 0\n"
"s_add_u32 s33, s20, s33\n"
"s_addc_u32 s71, s21, 0\n"
"s_lshl_b32 s31, %[wg], 2\n"
"s_add_u32 s31, s31, s52\n"
"s_lshl_b32 s31, s31, 13\n"
"s_add_u32 s28, s16, s31\n"
"s_addc_u32 s29, s17, 0\n"
"s_and_b32 s34, s23, 3\n"
"s_lshl_b32 s31, s34, 9\n"
"s_lshr_b32 s35, s23, 2\n"
"s_lshl_b32 s35, s35, 6\n"
"s_add_u32 s35, s35, s31\n"
"s_add_u32 s18, s18, s35\n"
"s_addc_u32 s19, s19, 0\n"
"s_lshl_b32 s35, s69, 16\n"
"s_add_u32 s31, s31, s35\n"
"s_lshl_b32 s35, s70, 8\n"
"s_add_u32 s31, s31, s35\n"
"s_add_u32 s16, s16, s31\n"
"s_addc_u32 s17, s17, 0\n"
"s_lshl_b32 s31, s22, 2\n"
"s_add_u32 s34, s34, s31\n"
"s_lshl_b32 s34, s34, 6\n"
"s_add_u32 s34, s34, s52\n"
"s_mul_i32 s34, s34, 6272\n"
"s_add_u32 s34, s34, 33554432\n"
"s_add_u32 s34, s20, s34\n"
"s_addc_u32 s35, s21, 0\n"
"s_add_u32 s20, s20, 62914560\n"
"s_addc_u32 s21, s21, 0\n"
"v_mov_b32 v52, 0\n"
"global_load_dwordx2 v[20:21], v52, s[20:21] sc1\n"
"v_lshlrev_b32 v56, 4, v57\n"
"s_mul_i32 s61, s52, 40960\n"
"s_mov_b32 s48, s10\n"
"s_and_b32 s49, s11, 0xffff\n"
"s_mov_b32 s50, 263168\n"
"s_mov_b32 s51, 0x20000\n"
"s_mov_b32 s44, s12\n"
"s_and_b32 s45, s13, 0xffff\n"
"s_mov_b32 s46, 0x6000\n"
"s_mov_b32 s47, 0x20000\n"
"s_mov_b32 s54, 0x3e0293ee\n"
"s_mov_b32 s55, 0x3e0293ee\n"
"s_mov_b32 s53, 1149239296\n"
"v_lshl_add_u32 v52, v48, 8, v49\n"
"v_lshl_add_u32 v52, v50, 4, v52\n"
"v_bfe_u32 v53, v57, 4, 1\n"
"v_lshlrev_b32 v53, 5, v53\n"
"s_cmp_eq_u32 s69, 0\n"
"s_cselect_b32 s65, 0, -1\n"
"s_cselect_b32 s66, 32, 0\n"
"v_and_b32 v53, s65, v53\n"
"v_xor_b32 v52, v52, v53\n"
"v_xor_b32 v53, s66, v52\n"
"v_add_u32 v44, 1024, v52\n"
"v_add_u32 v45, 128, v52\n"
"v_add_u32 v46, 1024, v53\n"
"v_add_u32 v47, 128, v53\n"
"v_add_u32 v23, s61, v56\n"
"v_bfe_u32 v52, v57, 3, 1\n"
"v_lshrrev_b32 v53, 4, v57\n"
"v_lshlrev_b32 v54, 1, v52\n"
"v_xor_b32 v54, v54, v53\n"
"v_and_b32 v55, 7, v57\n"
"v_lshlrev_b32 v55, 6, v55\n"
"v_lshl_add_u32 v54, v54, 4, v55\n"
"s_mul_i32 s65, s69, 12288\n"
"s_add_u32 s65, s65, 81920\n"
"v_lshl_add_u32 v34, v52, 10, v54\n"
"v_add_u32 v34, s65, v34\n"
"s_mul_i32 s66, s70, 81920\n"
"v_lshl_add_u32 v38, v52, 11, v54\n"
"v_add_u32 v38, s66, v38\n"
"v_bfe_u32 v52, v57, 2, 2\n"
"v_lshlrev_b32 v52, 6, v52\n"
"s_mul_i32 s65, s70, 40960\n"
"v_lshl_add_u32 v54, v50, 4, v52\n"
"v_lshl_add_u32 v35, v53, 10, v54\n"
"v_add_u32 v35, s65, v35\n"
"v_lshl_add_u32 v54, v50, 3, v52\n"
"v_lshrrev_b32 v55, 1, v53\n"
"v_lshl_add_u32 v54, v55, 11, v54\n"
"v_and_b32 v55, 1, v53\n"
"v_mul_u32_u24 v55, 288, v55\n"
"v_add_u32 v54, v54, v55\n"
"s_add_u32 s66, s66, 40960\n"
"v_add_u32 v39, s66, v54\n"
"v_xor_b32 v40, 32, v39\n"
"v_mov_b32 v58, 0x3f803f80\n"
"v_mov_b32 v59, 0x3f803f80\n"
"v_mov_b32 v60, 0x3f803f80\n"
"v_mov_b32 v61, 0x3f803f80\n"
"s_lshl_b32 s95, s27, 13\n"
"s_add_u32 s95, s95, s82\n"
"s_waitcnt vmcnt(25)\n"
"s_barrier\n"
"ds_read_b64_tr_b16 v[0:1], v35 offset:0\n"
"ds_read_b64_tr_b16 v[2:3], v35 offset:256\n"
"ds_read_b64_tr_b16 v[4:5], v35 offset:4096\n"
"ds_read_b64_tr_b16 v[6:7], v35 offset:4352\n"
"ds_read_b128 a[208:211], v34 offset:24576\n"
"ds_read_b128 a[212:215], v34 offset:25088\n"
"ds_read_b128 a[216:219], v34 offset:26624\n"
"ds_read_b128 a[220:223], v34 offset:27136\n"
"ds_read_b128 a[224:227], v34 offset:28672\n"
"ds_read_b128 a[228:231], v34 offset:29184\n"
"ds_read_b128 a[232:235], v34 offset:30720\n"
"ds_read_b128 a[236:239], v34 offset:31232\n"
"ds_read_b128 a[240:243], v34 offset:32768\n"
"ds_read_b128 a[244:247], v34 offset:33280\n"
"ds_read_b128 a[248:251], v34 offset:34816\n"
"ds_read_b128 a[252:255], v34 offset:35328\n"
"s_waitcnt lgkmcnt(0)\n"
"s_barrier\n"
"v_mfma_f32_16x16x32_bf16 v[64:67], v[0:3], a[208:211], 0\n"
"ds_read_b64_tr_b16 v[8:9], v35 offset:8\n"
"v_mfma_f32_16x16x32_bf16 v[64:67], v[4:7], a[212:215], v[64:67]\n"
"ds_read_b64_tr_b16 v[10:11], v35 offset:264\n"
"v_mfma_f32_16x16x32_bf16 v[72:75], v[0:3], a[216:219], 0\n"
"ds_read_b64_tr_b16 v[12:13], v35 offset:4104\n"
"v_mfma_f32_16x16x32_bf16 v[72:75], v[4:7], a[220:223], v[72:75]\n"
"ds_read_b64_tr_b16 v[14:15], v35 offset:4360\n"
"v_mfma_f32_16x16x32_bf16 v[80:83], v[0:3], a[224:227], 0\n"
"v_mfma_f32_16x16x32_bf16 v[80:83], v[4:7], a[228:231], v[80:83]\n"
"s_add_u32 s65, s81, 1048576\n"
"v_mfma_f32_16x16x32_bf16 v[88:91], v[0:3], a[232:235], 0\n"
"s_add_u32 m0, s68, 32768\n"
"s_and_b32 s65, s65, 0x7fffff\n"
"v_mfma_f32_16x16x32_bf16 v[88:91], v[4:7], a[236:239], v[88:91]\n"
"buffer_load_dwordx4 v24, s[40:43], s65 offen offset:0 lds\n"
"v_mfma_f32_16x16x32_bf16 v[96:99], v[0:3], a[240:243], 0\n"
"buffer_load_dwordx4 v25, s[40:43], s65 offen offset:1024 lds\n"
"v_mfma_f32_16x16x32_bf16 v[96:99], v[4:7], a[244:247], v[96:99]\n"
"v_mfma_f32_16x16x32_bf16 v[104:107], v[0:3], a[248:251], 0\n"
"buffer_load_dwordx4 v26, s[40:43], s65 offen offset:2048 lds\n"
"v_mfma_f32_16x16x32_bf16 v[104:107], v[4:7], a[252:255], v[104:107]\n"
"s_waitcnt lgkmcnt(0)\n"
"v_mfma_f32_16x16x32_bf16 v[68:71], v[8:11], a[208:211], 0\n"
"ds_read_b64_tr_b16 v[0:1], v35 offset:512\n"
"v_mfma_f32_16x16x32_bf16 v[68:71], v[12:15], a[212:215], v[68:71]\n"
"ds_read_b64_tr_b16 v[2:3], v35 offset:768\n"
"v_mfma_f32_16x16x32_bf16 v[76:79], v[8:11], a[216:219], 0\n"
"ds_read_b64_tr_b16 v[4:5], v35 offset:4608\n"
"v_mfma_f32_16x16x32_bf16 v[76:79], v[12:15], a[220:223], v[76:79]\n"
"ds_read_b64_tr_b16 v[6:7], v35 offset:4864\n"
"v_mfma_f32_16x16x32_bf16 v[84:87], v[8:11], a[224:227], 0\n"
"v_mfma_f32_16x16x32_bf16 v[84:87], v[12:15], a[228:231], v[84:87]\n"
"v_mfma_f32_16x16x32_bf16 v[92:95], v[8:11], a[232:235], 0\n"
"v_mfma_f32_16x16x32_bf16 v[92:95], v[12:15], a[236:239], v[92:95]\n"
"v_mfma_f32_16x16x32_bf16 v[100:103], v[8:11], a[240:243], 0\n"
"buffer_load_dwordx4 v27, s[40:43], s65 offen offset:3072 lds\n"
"v_mfma_f32_16x16x32_bf16 v[100:103], v[12:15], a[244:247], v[100:103]\n"
"s_add_u32 s66, s80, 384\n"
"s_add_u32 m0, s67, 24576\n"
"s_and_b32 s66, s66, 0x7ff\n"
"buffer_load_dwordx4 v28, s[36:39], s66 offen offset:0 lds\n"
"v_mfma_f32_16x16x32_bf16 v[108:111], v[8:11], a[248:251], 0\n"
"v_mfma_f32_16x16x32_bf16 v[108:111], v[12:15], a[252:255], v[108:111]\n"
"s_waitcnt lgkmcnt(0)\n"
"v_mfma_f32_16x16x32_bf16 v[112:115], v[0:3], a[208:211], 0\n"
"ds_read_b64_tr_b16 v[8:9], v35 offset:520\n"
"v_mfma_f32_16x16x32_bf16 v[112:115], v[4:7], a[212:215], v[112:115]\n"
"ds_read_b64_tr_b16 v[10:11], v35 offset:776\n"
"v_mfma_f32_16x16x32_bf16 v[120:123], v[0:3], a[216:219], 0\n"
"ds_read_b64_tr_b16 v[12:13], v35 offset:4616\n"
"v_mfma_f32_16x16x32_bf16 v[120:123], v[4:7], a[220:223], v[120:123]\n"
"ds_read_b64_tr_b16 v[14:15], v35 offset:4872\n"
"v_mfma_f32_16x16x32_bf16 v[128:131], v[0:3], a[224:227], 0\n"
"v_mfma_f32_16x16x32_bf16 v[128:131], v[4:7], a[228:231], v[128:131]\n"
"v_mfma_f32_16x16x32_bf16 v[136:139], v[0:3], a[232:235], 0\n"
"v_mfma_f32_16x16x32_bf16 v[136:139], v[4:7], a[236:239], v[136:139]\n"
"buffer_load_dwordx4 v29, s[36:39], s66 offen offset:1024 lds\n"
"v_mfma_f32_16x16x32_bf16 v[144:147], v[0:3], a[240:243], 0\n"
"v_mfma_f32_16x16x32_bf16 v[144:147], v[4:7], a[244:247], v[144:147]\n"
"buffer_load_dwordx4 v30, s[36:39], s66 offen offset:2048 lds\n"
"v_mfma_f32_16x16x32_bf16 v[152:155], v[0:3], a[248:251], 0\n"
"v_mfma_f32_16x16x32_bf16 v[152:155], v[4:7], a[252:255], v[152:155]\n"
"s_waitcnt vmcnt(22) lgkmcnt(0)\n"
"s_barrier\n"
"v_mfma_f32_16x16x32_bf16 v[116:119], v[8:11], a[208:211], 0\n"
"ds_read_b64_tr_b16 v[0:1], v35 offset:8192\n"
"v_mfma_f32_16x16x32_bf16 v[116:119], v[12:15], a[212:215], v[116:119]\n"
"ds_read_b64_tr_b16 v[2:3], v35 offset:8448\n"
"v_mfma_f32_16x16x32_bf16 v[124:127], v[8:11], a[216:219], 0\n"
"ds_read_b64_tr_b16 v[4:5], v35 offset:12288\n"
"v_mfma_f32_16x16x32_bf16 v[124:127], v[12:15], a[220:223], v[124:127]\n"
"ds_read_b64_tr_b16 v[6:7], v35 offset:12544\n"
"v_mfma_f32_16x16x32_bf16 v[132:135], v[8:11], a[224:227], 0\n"
"ds_read_b128 a[160:163], v34 offset:49152\n"
"v_mfma_f32_16x16x32_bf16 v[132:135], v[12:15], a[228:231], v[132:135]\n"
"s_add_u32 s65, s81, 1048704\n"
"ds_read_b128 a[164:167], v34 offset:49664\n"
"v_mfma_f32_16x16x32_bf16 v[140:143], v[8:11], a[232:235], 0\n"
"s_add_u32 m0, s68, 0\n"
"s_and_b32 s65, s65, 0x7fffff\n"
"ds_read_b128 a[168:171], v34 offset:51200\n"
"v_mfma_f32_16x16x32_bf16 v[140:143], v[12:15], a[236:239], v[140:143]\n"
"buffer_load_dwordx4 v24, s[40:43], s65 offen offset:0 lds\n"
"v_mfma_f32_16x16x32_bf16 v[148:151], v[8:11], a[240:243], 0\n"
"v_mfma_f32_16x16x32_bf16 v[148:151], v[12:15], a[244:247], v[148:151]\n"
"buffer_load_dwordx4 v25, s[40:43], s65 offen offset:1024 lds\n"
"v_mfma_f32_16x16x32_bf16 v[156:159], v[8:11], a[248:251], 0\n"
"v_mfma_f32_16x16x32_bf16 v[156:159], v[12:15], a[252:255], v[156:159]\n"
"s_waitcnt lgkmcnt(3)\n"
"v_mfma_f32_16x16x32_bf16 v[160:163], v[0:3], a[208:211], 0\n"
"ds_read_b64_tr_b16 v[8:9], v35 offset:8200\n"
"v_mfma_f32_16x16x32_bf16 v[160:163], v[4:7], a[212:215], v[160:163]\n"
"ds_read_b64_tr_b16 v[10:11], v35 offset:8456\n"
"v_mfma_f32_16x16x32_bf16 v[168:171], v[0:3], a[216:219], 0\n"
"ds_read_b64_tr_b16 v[12:13], v35 offset:12296\n"
"v_mfma_f32_16x16x32_bf16 v[168:171], v[4:7], a[220:223], v[168:171]\n"
"ds_read_b64_tr_b16 v[14:15], v35 offset:12552\n"
"v_mfma_f32_16x16x32_bf16 v[176:179], v[0:3], a[224:227], 0\n"
"ds_read_b128 a[172:175], v34 offset:51712\n"
"v_mfma_f32_16x16x32_bf16 v[176:179], v[4:7], a[228:231], v[176:179]\n"
"ds_read_b128 a[176:179], v34 offset:53248\n"
"v_mfma_f32_16x16x32_bf16 v[184:187], v[0:3], a[232:235], 0\n"
"ds_read_b128 a[180:183], v34 offset:53760\n"
"v_mfma_f32_16x16x32_bf16 v[184:187], v[4:7], a[236:239], v[184:187]\n"
"buffer_load_dwordx4 v26, s[40:43], s65 offen offset:2048 lds\n"
"v_mfma_f32_16x16x32_bf16 v[192:195], v[0:3], a[240:243], 0\n"
"v_mfma_f32_16x16x32_bf16 v[192:195], v[4:7], a[244:247], v[192:195]\n"
"buffer_load_dwordx4 v27, s[40:43], s65 offen offset:3072 lds\n"
"v_mfma_f32_16x16x32_bf16 v[200:203], v[0:3], a[248:251], 0\n"
"v_mfma_f32_16x16x32_bf16 v[200:203], v[4:7], a[252:255], v[200:203]\n"
"s_waitcnt lgkmcnt(3)\n"
"v_mfma_f32_16x16x32_bf16 v[164:167], v[8:11], a[208:211], 0\n"
"ds_read_b64_tr_b16 v[0:1], v35 offset:8704\n"
"v_mfma_f32_16x16x32_bf16 v[164:167], v[12:15], a[212:215], v[164:167]\n"
"ds_read_b64_tr_b16 v[2:3], v35 offset:8960\n"
"v_mfma_f32_16x16x32_bf16 v[172:175], v[8:11], a[216:219], 0\n"
"ds_read_b64_tr_b16 v[4:5], v35 offset:12800\n"
"v_mfma_f32_16x16x32_bf16 v[172:175], v[12:15], a[220:223], v[172:175]\n"
"ds_read_b64_tr_b16 v[6:7], v35 offset:13056\n"
"v_mfma_f32_16x16x32_bf16 v[180:183], v[8:11], a[224:227], 0\n"
"ds_read_b128 a[184:187], v34 offset:55296\n"
"v_mfma_f32_16x16x32_bf16 v[180:183], v[12:15], a[228:231], v[180:183]\n"
"ds_read_b128 a[188:191], v34 offset:55808\n"
"v_mfma_f32_16x16x32_bf16 v[188:191], v[8:11], a[232:235], 0\n"
"s_add_u32 s66, s80, 384\n"
"ds_read_b128 a[192:195], v34 offset:57344\n"
"v_mfma_f32_16x16x32_bf16 v[188:191], v[12:15], a[236:239], v[188:191]\n"
"s_add_u32 m0, s67, 27648\n"
"s_and_b32 s66, s66, 0x7ff\n"
"v_mfma_f32_16x16x32_bf16 v[196:199], v[8:11], a[240:243], 0\n"
"buffer_load_dwordx4 v31, s[36:39], s66 offen offset:0 lds\n"
"v_mfma_f32_16x16x32_bf16 v[196:199], v[12:15], a[244:247], v[196:199]\n"
"v_mfma_f32_16x16x32_bf16 v[204:207], v[8:11], a[248:251], 0\n"
"buffer_load_dwordx4 v32, s[36:39], s66 offen offset:1024 lds\n"
"v_mfma_f32_16x16x32_bf16 v[204:207], v[12:15], a[252:255], v[204:207]\n"
"s_waitcnt lgkmcnt(3)\n"
"v_mfma_f32_16x16x32_bf16 v[208:211], v[0:3], a[208:211], 0\n"
"ds_read_b64_tr_b16 v[8:9], v35 offset:8712\n"
"v_mfma_f32_16x16x32_bf16 v[208:211], v[4:7], a[212:215], v[208:211]\n"
"ds_read_b64_tr_b16 v[10:11], v35 offset:8968\n"
"v_mfma_f32_16x16x32_bf16 v[216:219], v[0:3], a[216:219], 0\n"
"ds_read_b64_tr_b16 v[12:13], v35 offset:12808\n"
"v_mfma_f32_16x16x32_bf16 v[216:219], v[4:7], a[220:223], v[216:219]\n"
"ds_read_b64_tr_b16 v[14:15], v35 offset:13064\n"
"v_mfma_f32_16x16x32_bf16 v[224:227], v[0:3], a[224:227], 0\n"
"ds_read_b128 a[196:199], v34 offset:57856\n"
"v_mfma_f32_16x16x32_bf16 v[224:227], v[4:7], a[228:231], v[224:227]\n"
"ds_read_b128 a[200:203], v34 offset:59392\n"
"v_mfma_f32_16x16x32_bf16 v[232:235], v[0:3], a[232:235], 0\n"
"ds_read_b128 a[204:207], v34 offset:59904\n"
"v_mfma_f32_16x16x32_bf16 v[232:235], v[4:7], a[236:239], v[232:235]\n"
"v_mfma_f32_16x16x32_bf16 v[240:243], v[0:3], a[240:243], 0\n"
"buffer_load_dwordx4 v33, s[36:39], s66 offen offset:2048 lds\n"
"v_mfma_f32_16x16x32_bf16 v[240:243], v[4:7], a[244:247], v[240:243]\n"
"v_mfma_f32_16x16x32_bf16 v[248:251], v[0:3], a[248:251], 0\n"
"v_mfma_f32_16x16x32_bf16 v[248:251], v[4:7], a[252:255], v[248:251]\n"
"s_waitcnt vmcnt(25) lgkmcnt(0)\n"
"s_barrier\n"
"v_mfma_f32_16x16x32_bf16 v[212:215], v[8:11], a[208:211], 0\n"
"ds_read_b64_tr_b16 v[0:1], v35 offset:16384\n"
"v_mfma_f32_16x16x32_bf16 v[212:215], v[12:15], a[212:215], v[212:215]\n"
"ds_read_b64_tr_b16 v[2:3], v35 offset:16640\n"
"v_mfma_f32_16x16x32_bf16 v[220:223], v[8:11], a[216:219], 0\n"
"ds_read_b64_tr_b16 v[4:5], v35 offset:20480\n"
"v_mfma_f32_16x16x32_bf16 v[220:223], v[12:15], a[220:223], v[220:223]\n"
"ds_read_b64_tr_b16 v[6:7], v35 offset:20736\n"
"v_mfma_f32_16x16x32_bf16 v[228:231], v[8:11], a[224:227], 0\n"
"v_mfma_f32_16x16x32_bf16 v[228:231], v[12:15], a[228:231], v[228:231]\n"
"s_add_u32 s65, s81, 1572864\n"
"v_mfma_f32_16x16x32_bf16 v[236:239], v[8:11], a[232:235], 0\n"
"s_add_u32 m0, s68, 8192\n"
"s_and_b32 s65, s65, 0x7fffff\n"
"v_mfma_f32_16x16x32_bf16 v[236:239], v[12:15], a[236:239], v[236:239]\n"
"buffer_load_dwordx4 v24, s[40:43], s65 offen offset:0 lds\n"
"v_mfma_f32_16x16x32_bf16 v[244:247], v[8:11], a[240:243], 0\n"
"v_mfma_f32_16x16x32_bf16 v[244:247], v[12:15], a[244:247], v[244:247]\n"
"buffer_load_dwordx4 v25, s[40:43], s65 offen offset:1024 lds\n"
"v_mfma_f32_16x16x32_bf16 v[252:255], v[8:11], a[248:251], 0\n"
"v_mfma_f32_16x16x32_bf16 v[252:255], v[12:15], a[252:255], v[252:255]\n"
"s_waitcnt lgkmcnt(0)\n"
"v_mfma_f32_16x16x32_bf16 v[64:67], v[0:3], a[160:163], v[64:67]\n"
"ds_read_b64_tr_b16 v[8:9], v35 offset:16392\n"
"v_mfma_f32_16x16x32_bf16 v[64:67], v[4:7], a[164:167], v[64:67]\n"
"ds_read_b64_tr_b16 v[10:11], v35 offset:16648\n"
"v_mfma_f32_16x16x32_bf16 v[72:75], v[0:3], a[168:171], v[72:75]\n"
"ds_read_b64_tr_b16 v[12:13], v35 offset:20488\n"
"v_mfma_f32_16x16x32_bf16 v[72:75], v[4:7], a[172:175], v[72:75]\n"
"ds_read_b64_tr_b16 v[14:15], v35 offset:20744\n"
"v_mfma_f32_16x16x32_bf16 v[80:83], v[0:3], a[176:179], v[80:83]\n"
"v_mfma_f32_16x16x32_bf16 v[80:83], v[4:7], a[180:183], v[80:83]\n"
"v_mfma_f32_16x16x32_bf16 v[88:91], v[0:3], a[184:187], v[88:91]\n"
"v_mfma_f32_16x16x32_bf16 v[88:91], v[4:7], a[188:191], v[88:91]\n"
"buffer_load_dwordx4 v26, s[40:43], s65 offen offset:2048 lds\n"
"v_mfma_f32_16x16x32_bf16 v[96:99], v[0:3], a[192:195], v[96:99]\n"
"v_mfma_f32_16x16x32_bf16 v[96:99], v[4:7], a[196:199], v[96:99]\n"
"buffer_load_dwordx4 v27, s[40:43], s65 offen offset:3072 lds\n"
"v_mfma_f32_16x16x32_bf16 v[104:107], v[0:3], a[200:203], v[104:107]\n"
"v_mfma_f32_16x16x32_bf16 v[104:107], v[4:7], a[204:207], v[104:107]\n"
"s_waitcnt lgkmcnt(0)\n"
"v_mfma_f32_16x16x32_bf16 v[68:71], v[8:11], a[160:163], v[68:71]\n"
"ds_read_b64_tr_b16 v[0:1], v35 offset:16896\n"
"v_mfma_f32_16x16x32_bf16 v[68:71], v[12:15], a[164:167], v[68:71]\n"
"ds_read_b64_tr_b16 v[2:3], v35 offset:17152\n"
"v_mfma_f32_16x16x32_bf16 v[76:79], v[8:11], a[168:171], v[76:79]\n"
"ds_read_b64_tr_b16 v[4:5], v35 offset:20992\n"
"v_mfma_f32_16x16x32_bf16 v[76:79], v[12:15], a[172:175], v[76:79]\n"
"ds_read_b64_tr_b16 v[6:7], v35 offset:21248\n"
"v_mfma_f32_16x16x32_bf16 v[84:87], v[8:11], a[176:179], v[84:87]\n"
"v_mfma_f32_16x16x32_bf16 v[84:87], v[12:15], a[180:183], v[84:87]\n"
"v_mfma_f32_16x16x32_bf16 v[92:95], v[8:11], a[184:187], v[92:95]\n"
"s_add_u32 s66, s80, 512\n"
"v_mfma_f32_16x16x32_bf16 v[92:95], v[12:15], a[188:191], v[92:95]\n"
"s_add_u32 m0, s67, 49152\n"
"s_and_b32 s66, s66, 0x7ff\n"
"v_mfma_f32_16x16x32_bf16 v[100:103], v[8:11], a[192:195], v[100:103]\n"
"buffer_load_dwordx4 v28, s[36:39], s66 offen offset:0 lds\n"
"v_mfma_f32_16x16x32_bf16 v[100:103], v[12:15], a[196:199], v[100:103]\n"
"v_mfma_f32_16x16x32_bf16 v[108:111], v[8:11], a[200:203], v[108:111]\n"
"buffer_load_dwordx4 v29, s[36:39], s66 offen offset:1024 lds\n"
"v_mfma_f32_16x16x32_bf16 v[108:111], v[12:15], a[204:207], v[108:111]\n"
"s_waitcnt lgkmcnt(0)\n"
"v_mfma_f32_16x16x32_bf16 v[112:115], v[0:3], a[160:163], v[112:115]\n"
"ds_read_b64_tr_b16 v[8:9], v35 offset:16904\n"
"v_mfma_f32_16x16x32_bf16 v[112:115], v[4:7], a[164:167], v[112:115]\n"
"ds_read_b64_tr_b16 v[10:11], v35 offset:17160\n"
"v_mfma_f32_16x16x32_bf16 v[120:123], v[0:3], a[168:171], v[120:123]\n"
"ds_read_b64_tr_b16 v[12:13], v35 offset:21000\n"
"v_mfma_f32_16x16x32_bf16 v[120:123], v[4:7], a[172:175], v[120:123]\n"
"ds_read_b64_tr_b16 v[14:15], v35 offset:21256\n"
"v_mfma_f32_16x16x32_bf16 v[128:131], v[0:3], a[176:179], v[128:131]\n"
"v_mfma_f32_16x16x32_bf16 v[128:131], v[4:7], a[180:183], v[128:131]\n"
"v_mfma_f32_16x16x32_bf16 v[136:139], v[0:3], a[184:187], v[136:139]\n"
"v_mfma_f32_16x16x32_bf16 v[136:139], v[4:7], a[188:191], v[136:139]\n"
"v_mfma_f32_16x16x32_bf16 v[144:147], v[0:3], a[192:195], v[144:147]\n"
"buffer_load_dwordx4 v30, s[36:39], s66 offen offset:2048 lds\n"
"v_mfma_f32_16x16x32_bf16 v[144:147], v[4:7], a[196:199], v[144:147]\n"
"v_mfma_f32_16x16x32_bf16 v[152:155], v[0:3], a[200:203], v[152:155]\n"
"v_mfma_f32_16x16x32_bf16 v[152:155], v[4:7], a[204:207], v[152:155]\n"
"s_waitcnt vmcnt(22) lgkmcnt(0)\n"
"s_barrier\n"
"v_mfma_f32_16x16x32_bf16 v[116:119], v[8:11], a[160:163], v[116:119]\n"
"ds_read_b64_tr_b16 v[0:1], v35 offset:24576\n"
"v_mfma_f32_16x16x32_bf16 v[116:119], v[12:15], a[164:167], v[116:119]\n"
"ds_read_b64_tr_b16 v[2:3], v35 offset:24832\n"
"v_mfma_f32_16x16x32_bf16 v[124:127], v[8:11], a[168:171], v[124:127]\n"
"ds_read_b64_tr_b16 v[4:5], v35 offset:28672\n"
"v_mfma_f32_16x16x32_bf16 v[124:127], v[12:15], a[172:175], v[124:127]\n"
"ds_read_b64_tr_b16 v[6:7], v35 offset:28928\n"
"v_mfma_f32_16x16x32_bf16 v[132:135], v[8:11], a[176:179], v[132:135]\n"
"ds_read_b128 a[208:211], v34 offset:0\n"
"v_mfma_f32_16x16x32_bf16 v[132:135], v[12:15], a[180:183], v[132:135]\n"
"s_add_u32 s65, s81, 1572992\n"
"ds_read_b128 a[212:215], v34 offset:512\n"
"v_mfma_f32_16x16x32_bf16 v[140:143], v[8:11], a[184:187], v[140:143]\n"
"s_add_u32 m0, s68, 16384\n"
"s_and_b32 s65, s65, 0x7fffff\n"
"ds_read_b128 a[216:219], v34 offset:2048\n"
"v_mfma_f32_16x16x32_bf16 v[140:143], v[12:15], a[188:191], v[140:143]\n"
"buffer_load_dwordx4 v24, s[40:43], s65 offen offset:0 lds\n"
"v_mfma_f32_16x16x32_bf16 v[148:151], v[8:11], a[192:195], v[148:151]\n"
"buffer_load_dwordx4 v25, s[40:43], s65 offen offset:1024 lds\n"
"v_mfma_f32_16x16x32_bf16 v[148:151], v[12:15], a[196:199], v[148:151]\n"
"v_mfma_f32_16x16x32_bf16 v[156:159], v[8:11], a[200:203], v[156:159]\n"
"buffer_load_dwordx4 v26, s[40:43], s65 offen offset:2048 lds\n"
"v_mfma_f32_16x16x32_bf16 v[156:159], v[12:15], a[204:207], v[156:159]\n"
"s_waitcnt lgkmcnt(3)\n"
"v_mfma_f32_16x16x32_bf16 v[160:163], v[0:3], a[160:163], v[160:163]\n"
"ds_read_b64_tr_b16 v[8:9], v35 offset:24584\n"
"v_mfma_f32_16x16x32_bf16 v[160:163], v[4:7], a[164:167], v[160:163]\n"
"ds_read_b64_tr_b16 v[10:11], v35 offset:24840\n"
"v_mfma_f32_16x16x32_bf16 v[168:171], v[0:3], a[168:171], v[168:171]\n"
"ds_read_b64_tr_b16 v[12:13], v35 offset:28680\n"
"v_mfma_f32_16x16x32_bf16 v[168:171], v[4:7], a[172:175], v[168:171]\n"
"ds_read_b64_tr_b16 v[14:15], v35 offset:28936\n"
"v_mfma_f32_16x16x32_bf16 v[176:179], v[0:3], a[176:179], v[176:179]\n"
"ds_read_b128 a[220:223], v34 offset:2560\n"
"v_mfma_f32_16x16x32_bf16 v[176:179], v[4:7], a[180:183], v[176:179]\n"
"ds_read_b128 a[224:227], v34 offset:4096\n"
"v_mfma_f32_16x16x32_bf16 v[184:187], v[0:3], a[184:187], v[184:187]\n"
"ds_read_b128 a[228:231], v34 offset:4608\n"
"v_mfma_f32_16x16x32_bf16 v[184:187], v[4:7], a[188:191], v[184:187]\n"
"v_mfma_f32_16x16x32_bf16 v[192:195], v[0:3], a[192:195], v[192:195]\n"
"buffer_load_dwordx4 v27, s[40:43], s65 offen offset:3072 lds\n"
"v_mfma_f32_16x16x32_bf16 v[192:195], v[4:7], a[196:199], v[192:195]\n"
"s_add_u32 s66, s80, 512\n"
"s_add_u32 m0, s67, 52224\n"
"s_and_b32 s66, s66, 0x7ff\n"
"v_mfma_f32_16x16x32_bf16 v[200:203], v[0:3], a[200:203], v[200:203]\n"
"buffer_load_dwordx4 v31, s[36:39], s66 offen offset:0 lds\n"
"v_mfma_f32_16x16x32_bf16 v[200:203], v[4:7], a[204:207], v[200:203]\n"
"s_waitcnt lgkmcnt(3)\n"
"v_mfma_f32_16x16x32_bf16 v[164:167], v[8:11], a[160:163], v[164:167]\n"
"ds_read_b64_tr_b16 v[0:1], v35 offset:25088\n"
"v_mfma_f32_16x16x32_bf16 v[164:167], v[12:15], a[164:167], v[164:167]\n"
"ds_read_b64_tr_b16 v[2:3], v35 offset:25344\n"
"v_mfma_f32_16x16x32_bf16 v[172:175], v[8:11], a[168:171], v[172:175]\n"
"ds_read_b64_tr_b16 v[4:5], v35 offset:29184\n"
"v_mfma_f32_16x16x32_bf16 v[172:175], v[12:15], a[172:175], v[172:175]\n"
"ds_read_b64_tr_b16 v[6:7], v35 offset:29440\n"
"v_mfma_f32_16x16x32_bf16 v[180:183], v[8:11], a[176:179], v[180:183]\n"
"ds_read_b128 a[232:235], v34 offset:6144\n"
"v_mfma_f32_16x16x32_bf16 v[180:183], v[12:15], a[180:183], v[180:183]\n"
"ds_read_b128 a[236:239], v34 offset:6656\n"
"v_mfma_f32_16x16x32_bf16 v[188:191], v[8:11], a[184:187], v[188:191]\n"
"ds_read_b128 a[240:243], v34 offset:8192\n"
"v_mfma_f32_16x16x32_bf16 v[188:191], v[12:15], a[188:191], v[188:191]\n"
"buffer_load_dwordx4 v32, s[36:39], s66 offen offset:1024 lds\n"
"v_mfma_f32_16x16x32_bf16 v[196:199], v[8:11], a[192:195], v[196:199]\n"
"v_mfma_f32_16x16x32_bf16 v[196:199], v[12:15], a[196:199], v[196:199]\n"
"buffer_load_dwordx4 v33, s[36:39], s66 offen offset:2048 lds\n"
"v_mfma_f32_16x16x32_bf16 v[204:207], v[8:11], a[200:203], v[204:207]\n"
"v_mfma_f32_16x16x32_bf16 v[204:207], v[12:15], a[204:207], v[204:207]\n"
"s_waitcnt lgkmcnt(3)\n"
"v_mfma_f32_16x16x32_bf16 v[208:211], v[0:3], a[160:163], v[208:211]\n"
"ds_read_b64_tr_b16 v[8:9], v35 offset:25096\n"
"v_mfma_f32_16x16x32_bf16 v[208:211], v[4:7], a[164:167], v[208:211]\n"
"ds_read_b64_tr_b16 v[10:11], v35 offset:25352\n"
"v_mfma_f32_16x16x32_bf16 v[216:219], v[0:3], a[168:171], v[216:219]\n"
"ds_read_b64_tr_b16 v[12:13], v35 offset:29192\n"
"v_mfma_f32_16x16x32_bf16 v[216:219], v[4:7], a[172:175], v[216:219]\n"
"ds_read_b64_tr_b16 v[14:15], v35 offset:29448\n"
"v_mfma_f32_16x16x32_bf16 v[224:227], v[0:3], a[176:179], v[224:227]\n"
"ds_read_b128 a[244:247], v34 offset:8704\n"
"v_mfma_f32_16x16x32_bf16 v[224:227], v[4:7], a[180:183], v[224:227]\n"
"ds_read_b128 a[248:251], v34 offset:10240\n"
"v_mfma_f32_16x16x32_bf16 v[232:235], v[0:3], a[184:187], v[232:235]\n"
"ds_read_b128 a[252:255], v34 offset:10752\n"
"v_mfma_f32_16x16x32_bf16 v[232:235], v[4:7], a[188:191], v[232:235]\n"
"s_cmp_eq_u32 s94, 0\n"
"s_cselect_b64 vcc, -1, 0\n"
"s_add_u32 s56, s82, 0\n"
"s_and_b32 s56, s56, 0x3ffff\n"
"s_cbranch_vccz K1_KS0_0_0\n"
"buffer_load_dwordx4 a[0:3], v44, s[48:51], s56 offen offset:0 nt\n"
"buffer_load_dwordx4 a[4:7], v45, s[48:51], s56 offen offset:1024 nt\n"
"buffer_load_dwordx4 a[8:11], v46, s[48:51], s56 offen offset:2048 nt\n"
"buffer_load_dwordx4 a[12:15], v47, s[48:51], s56 offen offset:3072 nt\n"
"K1_KS0_0_0:\n"
"v_mfma_f32_16x16x32_bf16 v[240:243], v[0:3], a[192:195], v[240:243]\n"
"v_mfma_f32_16x16x32_bf16 v[240:243], v[4:7], a[196:199], v[240:243]\n"
"s_add_u32 s56, s82, 4096\n"
"s_and_b32 s56, s56, 0x3ffff\n"
"s_cbranch_vccz K1_KS0_0_4\n"
"buffer_load_dwordx4 a[16:19], v44, s[48:51], s56 offen offset:0 nt\n"
"buffer_load_dwordx4 a[20:23], v45, s[48:51], s56 offen offset:1024 nt\n"
"buffer_load_dwordx4 a[24:27], v46, s[48:51], s56 offen offset:2048 nt\n"
"buffer_load_dwordx4 a[28:31], v47, s[48:51], s56 offen offset:3072 nt\n"
"K1_KS0_0_4:\n"
"v_mfma_f32_16x16x32_bf16 v[248:251], v[0:3], a[200:203], v[248:251]\n"
"v_mfma_f32_16x16x32_bf16 v[248:251], v[4:7], a[204:207], v[248:251]\n"
"s_bitcmp1_b32 2, s94\n"
"s_cbranch_scc0 K1_W1\n"
"s_waitcnt vmcnt(24)\n"
"K1_W1:\n"
"s_waitcnt vmcnt(32) lgkmcnt(0)\n"
"s_barrier\n"
"v_mfma_f32_16x16x32_bf16 v[212:215], v[8:11], a[160:163], v[212:215]\n"
"ds_read_b64_tr_b16 v[0:1], v35 offset:32768\n"
"v_mfma_f32_16x16x32_bf16 v[212:215], v[12:15], a[164:167], v[212:215]\n"
"ds_read_b64_tr_b16 v[2:3], v35 offset:33024\n"
"v_mfma_f32_16x16x32_bf16 v[220:223], v[8:11], a[168:171], v[220:223]\n"
"ds_read_b64_tr_b16 v[4:5], v35 offset:36864\n"
"v_mfma_f32_16x16x32_bf16 v[220:223], v[12:15], a[172:175], v[220:223]\n"
"ds_read_b64_tr_b16 v[6:7], v35 offset:37120\n"
"v_mfma_f32_16x16x32_bf16 v[228:231], v[8:11], a[176:179], v[228:231]\n"
"v_mfma_f32_16x16x32_bf16 v[228:231], v[12:15], a[180:183], v[228:231]\n"
"s_add_u32 s65, s81, 2097152\n"
"v_mfma_f32_16x16x32_bf16 v[236:239], v[8:11], a[184:187], v[236:239]\n"
"s_add_u32 m0, s68, 24576\n"
"s_and_b32 s65, s65, 0x7fffff\n"
"v_mfma_f32_16x16x32_bf16 v[236:239], v[12:15], a[188:191], v[236:239]\n"
"buffer_load_dwordx4 v24, s[40:43], s65 offen offset:0 lds\n"
"v_mfma_f32_16x16x32_bf16 v[244:247], v[8:11], a[192:195], v[244:247]\n"
"v_mfma_f32_16x16x32_bf16 v[244:247], v[12:15], a[196:199], v[244:247]\n"
"buffer_load_dwordx4 v25, s[40:43], s65 offen offset:1024 lds\n"
"v_mfma_f32_16x16x32_bf16 v[252:255], v[8:11], a[200:203], v[252:255]\n"
"v_mfma_f32_16x16x32_bf16 v[252:255], v[12:15], a[204:207], v[252:255]\n"
"s_waitcnt lgkmcnt(0)\n"
"v_mfma_f32_16x16x32_bf16 v[64:67], v[0:3], a[208:211], v[64:67]\n"
"ds_read_b64_tr_b16 v[8:9], v35 offset:32776\n"
"v_mfma_f32_16x16x32_bf16 v[64:67], v[4:7], a[212:215], v[64:67]\n"
"ds_read_b64_tr_b16 v[10:11], v35 offset:33032\n"
"v_mfma_f32_16x16x32_bf16 v[72:75], v[0:3], a[216:219], v[72:75]\n"
"ds_read_b64_tr_b16 v[12:13], v35 offset:36872\n"
"v_mfma_f32_16x16x32_bf16 v[72:75], v[4:7], a[220:223], v[72:75]\n"
"ds_read_b64_tr_b16 v[14:15], v35 offset:37128\n"
"v_mfma_f32_16x16x32_bf16 v[80:83], v[0:3], a[224:227], v[80:83]\n"
"v_mfma_f32_16x16x32_bf16 v[80:83], v[4:7], a[228:231], v[80:83]\n"
"v_mfma_f32_16x16x32_bf16 v[88:91], v[0:3], a[232:235], v[88:91]\n"
"v_mfma_f32_16x16x32_bf16 v[88:91], v[4:7], a[236:239], v[88:91]\n"
"buffer_load_dwordx4 v26, s[40:43], s65 offen offset:2048 lds\n"
"v_mfma_f32_16x16x32_bf16 v[96:99], v[0:3], a[240:243], v[96:99]\n"
"v_mfma_f32_16x16x32_bf16 v[96:99], v[4:7], a[244:247], v[96:99]\n"
"buffer_load_dwordx4 v27, s[40:43], s65 offen offset:3072 lds\n"
"v_mfma_f32_16x16x32_bf16 v[104:107], v[0:3], a[248:251], v[104:107]\n"
"v_mfma_f32_16x16x32_bf16 v[104:107], v[4:7], a[252:255], v[104:107]\n"
"s_waitcnt lgkmcnt(0)\n"
"v_mfma_f32_16x16x32_bf16 v[68:71], v[8:11], a[208:211], v[68:71]\n"
"ds_read_b64_tr_b16 v[0:1], v35 offset:33280\n"
"v_mfma_f32_16x16x32_bf16 v[68:71], v[12:15], a[212:215], v[68:71]\n"
"ds_read_b64_tr_b16 v[2:3], v35 offset:33536\n"
"v_mfma_f32_16x16x32_bf16 v[76:79], v[8:11], a[216:219], v[76:79]\n"
"ds_read_b64_tr_b16 v[4:5], v35 offset:37376\n"
"v_mfma_f32_16x16x32_bf16 v[76:79], v[12:15], a[220:223], v[76:79]\n"
"ds_read_b64_tr_b16 v[6:7], v35 offset:37632\n"
"v_mfma_f32_16x16x32_bf16 v[84:87], v[8:11], a[224:227], v[84:87]\n"
"v_mfma_f32_16x16x32_bf16 v[84:87], v[12:15], a[228:231], v[84:87]\n"
"v_mfma_f32_16x16x32_bf16 v[92:95], v[8:11], a[232:235], v[92:95]\n"
"s_add_u32 s66, s80, 640\n"
"v_mfma_f32_16x16x32_bf16 v[92:95], v[12:15], a[236:239], v[92:95]\n"
"s_add_u32 m0, s67, 0\n"
"s_and_b32 s66, s66, 0x7ff\n"
"v_mfma_f32_16x16x32_bf16 v[100:103], v[8:11], a[240:243], v[100:103]\n"
"buffer_load_dwordx4 v28, s[36:39], s66 offen offset:0 lds\n"
"v_mfma_f32_16x16x32_bf16 v[100:103], v[12:15], a[244:247], v[100:103]\n"
"v_mfma_f32_16x16x32_bf16 v[108:111], v[8:11], a[248:251], v[108:111]\n"
"buffer_load_dwordx4 v29, s[36:39], s66 offen offset:1024 lds\n"
"v_mfma_f32_16x16x32_bf16 v[108:111], v[12:15], a[252:255], v[108:111]\n"
"s_waitcnt lgkmcnt(0)\n"
"v_mfma_f32_16x16x32_bf16 v[112:115], v[0:3], a[208:211], v[112:115]\n"
"ds_read_b64_tr_b16 v[8:9], v35 offset:33288\n"
"v_mfma_f32_16x16x32_bf16 v[112:115], v[4:7], a[212:215], v[112:115]\n"
"ds_read_b64_tr_b16 v[10:11], v35 offset:33544\n"
"v_mfma_f32_16x16x32_bf16 v[120:123], v[0:3], a[216:219], v[120:123]\n"
"ds_read_b64_tr_b16 v[12:13], v35 offset:37384\n"
"v_mfma_f32_16x16x32_bf16 v[120:123], v[4:7], a[220:223], v[120:123]\n"
"ds_read_b64_tr_b16 v[14:15], v35 offset:37640\n"
"v_mfma_f32_16x16x32_bf16 v[128:131], v[0:3], a[224:227], v[128:131]\n"
"v_mfma_f32_16x16x32_bf16 v[128:131], v[4:7], a[228:231], v[128:131]\n"
"v_mfma_f32_16x16x32_bf16 v[136:139], v[0:3], a[232:235], v[136:139]\n"
"v_mfma_f32_16x16x32_bf16 v[136:139], v[4:7], a[236:239], v[136:139]\n"
"v_mfma_f32_16x16x32_bf16 v[144:147], v[0:3], a[240:243], v[144:147]\n"
"buffer_load_dwordx4 v30, s[36:39], s66 offen offset:2048 lds\n"
"v_mfma_f32_16x16x32_bf16 v[144:147], v[4:7], a[244:247], v[144:147]\n"
"v_mfma_f32_16x16x32_bf16 v[152:155], v[0:3], a[248:251], v[152:155]\n"
"v_mfma_f32_16x16x32_bf16 v[152:155], v[4:7], a[252:255], v[152:155]\n"
"s_bitcmp1_b32 2, s94\n"
"s_cbranch_scc0 K1_W2\n"
"s_waitcnt vmcnt(21)\n"
"K1_W2:\n"
"s_waitcnt vmcnt(29) lgkmcnt(0)\n"
"s_barrier\n"
"v_mfma_f32_16x16x32_bf16 v[116:119], v[8:11], a[208:211], v[116:119]\n"
"ds_read_b64_tr_b16 v[0:1], v35 offset:0\n"
"v_mfma_f32_16x16x32_bf16 v[116:119], v[12:15], a[212:215], v[116:119]\n"
"ds_read_b64_tr_b16 v[2:3], v35 offset:256\n"
"v_mfma_f32_16x16x32_bf16 v[124:127], v[8:11], a[216:219], v[124:127]\n"
"ds_read_b64_tr_b16 v[4:5], v35 offset:4096\n"
"v_mfma_f32_16x16x32_bf16 v[124:127], v[12:15], a[220:223], v[124:127]\n"
"ds_read_b64_tr_b16 v[6:7], v35 offset:4352\n"
"v_mfma_f32_16x16x32_bf16 v[132:135], v[8:11], a[224:227], v[132:135]\n"
"ds_read_b128 a[160:163], v34 offset:24576\n"
"v_mfma_f32_16x16x32_bf16 v[132:135], v[12:15], a[228:231], v[132:135]\n"
"s_add_u32 s65, s81, 2097280\n"
"ds_read_b128 a[164:167], v34 offset:25088\n"
"v_mfma_f32_16x16x32_bf16 v[140:143], v[8:11], a[232:235], v[140:143]\n"
"s_add_u32 m0, s68, 32768\n"
"s_and_b32 s65, s65, 0x7fffff\n"
"ds_read_b128 a[168:171], v34 offset:26624\n"
"v_mfma_f32_16x16x32_bf16 v[140:143], v[12:15], a[236:239], v[140:143]\n"
"buffer_load_dwordx4 v24, s[40:43], s65 offen offset:0 lds\n"
"v_mfma_f32_16x16x32_bf16 v[148:151], v[8:11], a[240:243], v[148:151]\n"
"v_mfma_f32_16x16x32_bf16 v[148:151], v[12:15], a[244:247], v[148:151]\n"
"buffer_load_dwordx4 v25, s[40:43], s65 offen offset:1024 lds\n"
"v_mfma_f32_16x16x32_bf16 v[156:159], v[8:11], a[248:251], v[156:159]\n"
"v_mfma_f32_16x16x32_bf16 v[156:159], v[12:15], a[252:255], v[156:159]\n"
"s_waitcnt lgkmcnt(3)\n"
"v_mfma_f32_16x16x32_bf16 v[160:163], v[0:3], a[208:211], v[160:163]\n"
"ds_read_b64_tr_b16 v[8:9], v35 offset:8\n"
"v_mfma_f32_16x16x32_bf16 v[160:163], v[4:7], a[212:215], v[160:163]\n"
"ds_read_b64_tr_b16 v[10:11], v35 offset:264\n"
"v_mfma_f32_16x16x32_bf16 v[168:171], v[0:3], a[216:219], v[168:171]\n"
"ds_read_b64_tr_b16 v[12:13], v35 offset:4104\n"
"v_mfma_f32_16x16x32_bf16 v[168:171], v[4:7], a[220:223], v[168:171]\n"
"ds_read_b64_tr_b16 v[14:15], v35 offset:4360\n"
"v_mfma_f32_16x16x32_bf16 v[176:179], v[0:3], a[224:227], v[176:179]\n"
"ds_read_b128 a[172:175], v34 offset:27136\n"
"v_mfma_f32_16x16x32_bf16 v[176:179], v[4:7], a[228:231], v[176:179]\n"
"ds_read_b128 a[176:179], v34 offset:28672\n"
"v_mfma_f32_16x16x32_bf16 v[184:187], v[0:3], a[232:235], v[184:187]\n"
"ds_read_b128 a[180:183], v34 offset:29184\n"
"v_mfma_f32_16x16x32_bf16 v[184:187], v[4:7], a[236:239], v[184:187]\n"
"buffer_load_dwordx4 v26, s[40:43], s65 offen offset:2048 lds\n"
"v_mfma_f32_16x16x32_bf16 v[192:195], v[0:3], a[240:243], v[192:195]\n"
"v_mfma_f32_16x16x32_bf16 v[192:195], v[4:7], a[244:247], v[192:195]\n"
"buffer_load_dwordx4 v27, s[40:43], s65 offen offset:3072 lds\n"
"v_mfma_f32_16x16x32_bf16 v[200:203], v[0:3], a[248:251], v[200:203]\n"
"v_mfma_f32_16x16x32_bf16 v[200:203], v[4:7], a[252:255], v[200:203]\n"
"s_waitcnt lgkmcnt(3)\n"
"v_mfma_f32_16x16x32_bf16 v[164:167], v[8:11], a[208:211], v[164:167]\n"
"ds_read_b64_tr_b16 v[0:1], v35 offset:512\n"
"v_mfma_f32_16x16x32_bf16 v[164:167], v[12:15], a[212:215], v[164:167]\n"
"ds_read_b64_tr_b16 v[2:3], v35 offset:768\n"
"v_mfma_f32_16x16x32_bf16 v[172:175], v[8:11], a[216:219], v[172:175]\n"
"ds_read_b64_tr_b16 v[4:5], v35 offset:4608\n"
"v_mfma_f32_16x16x32_bf16 v[172:175], v[12:15], a[220:223], v[172:175]\n"
"ds_read_b64_tr_b16 v[6:7], v35 offset:4864\n"
"v_mfma_f32_16x16x32_bf16 v[180:183], v[8:11], a[224:227], v[180:183]\n"
"ds_read_b128 a[184:187], v34 offset:30720\n"
"v_mfma_f32_16x16x32_bf16 v[180:183], v[12:15], a[228:231], v[180:183]\n"
"ds_read_b128 a[188:191], v34 offset:31232\n"
"v_mfma_f32_16x16x32_bf16 v[188:191], v[8:11], a[232:235], v[188:191]\n"
"s_add_u32 s66, s80, 640\n"
"ds_read_b128 a[192:195], v34 offset:32768\n"
"v_mfma_f32_16x16x32_bf16 v[188:191], v[12:15], a[236:239], v[188:191]\n"
"s_add_u32 m0, s67, 3072\n"
"s_and_b32 s66, s66, 0x7ff\n"
"v_mfma_f32_16x16x32_bf16 v[196:199], v[8:11], a[240:243], v[196:199]\n"
"buffer_load_dwordx4 v31, s[36:39], s66 offen offset:0 lds\n"
"v_mfma_f32_16x16x32_bf16 v[196:199], v[12:15], a[244:247], v[196:199]\n"
"v_mfma_f32_16x16x32_bf16 v[204:207], v[8:11], a[248:251], v[204:207]\n"
"buffer_load_dwordx4 v32, s[36:39], s66 offen offset:1024 lds\n"
"v_mfma_f32_16x16x32_bf16 v[204:207], v[12:15], a[252:255], v[204:207]\n"
"s_waitcnt lgkmcnt(3)\n"
"v_mfma_f32_16x16x32_bf16 v[208:211], v[0:3], a[208:211], v[208:211]\n"
"ds_read_b64_tr_b16 v[8:9], v35 offset:520\n"
"v_mfma_f32_16x16x32_bf16 v[208:211], v[4:7], a[212:215], v[208:211]\n"
"ds_read_b64_tr_b16 v[10:11], v35 offset:776\n"
"v_mfma_f32_16x16x32_bf16 v[216:219], v[0:3], a[216:219], v[216:219]\n"
"ds_read_b64_tr_b16 v[12:13], v35 offset:4616\n"
"v_mfma_f32_16x16x32_bf16 v[216:219], v[4:7], a[220:223], v[216:219]\n"
"ds_read_b64_tr_b16 v[14:15], v35 offset:4872\n"
"v_mfma_f32_16x16x32_bf16 v[224:227], v[0:3], a[224:227], v[224:227]\n"
"ds_read_b128 a[196:199], v34 offset:33280\n"
"v_mfma_f32_16x16x32_bf16 v[224:227], v[4:7], a[228:231], v[224:227]\n"
"ds_read_b128 a[200:203], v34 offset:34816\n"
"v_mfma_f32_16x16x32_bf16 v[232:235], v[0:3], a[232:235], v[232:235]\n"
"ds_read_b128 a[204:207], v34 offset:35328\n"
"v_mfma_f32_16x16x32_bf16 v[232:235], v[4:7], a[236:239], v[232:235]\n"
"v_mfma_f32_16x16x32_bf16 v[240:243], v[0:3], a[240:243], v[240:243]\n"
"buffer_load_dwordx4 v33, s[36:39], s66 offen offset:2048 lds\n"
"v_mfma_f32_16x16x32_bf16 v[240:243], v[4:7], a[244:247], v[240:243]\n"
"v_mfma_f32_16x16x32_bf16 v[248:251], v[0:3], a[248:251], v[248:251]\n"
"v_mfma_f32_16x16x32_bf16 v[248:251], v[4:7], a[252:255], v[248:251]\n"
"s_bitcmp1_b32 2, s94\n"
"s_cbranch_scc0 K1_W3\n"
"s_waitcnt vmcnt(24)\n"
"K1_W3:\n"
"s_waitcnt vmcnt(32) lgkmcnt(0)\n"
"s_barrier\n"
"v_mfma_f32_16x16x32_bf16 v[212:215], v[8:11], a[208:211], v[212:215]\n"
"ds_read_b64_tr_b16 v[0:1], v35 offset:8192\n"
"v_mfma_f32_16x16x32_bf16 v[212:215], v[12:15], a[212:215], v[212:215]\n"
"ds_read_b64_tr_b16 v[2:3], v35 offset:8448\n"
"v_mfma_f32_16x16x32_bf16 v[220:223], v[8:11], a[216:219], v[220:223]\n"
"ds_read_b64_tr_b16 v[4:5], v35 offset:12288\n"
"v_mfma_f32_16x16x32_bf16 v[220:223], v[12:15], a[220:223], v[220:223]\n"
"ds_read_b64_tr_b16 v[6:7], v35 offset:12544\n"
"v_mfma_f32_16x16x32_bf16 v[228:231], v[8:11], a[224:227], v[228:231]\n"
"v_mfma_f32_16x16x32_bf16 v[228:231], v[12:15], a[228:231], v[228:231]\n"
"s_add_u32 s65, s81, 2621440\n"
"v_mfma_f32_16x16x32_bf16 v[236:239], v[8:11], a[232:235], v[236:239]\n"
"s_add_u32 m0, s68, 0\n"
"s_and_b32 s65, s65, 0x7fffff\n"
"v_mfma_f32_16x16x32_bf16 v[236:239], v[12:15], a[236:239], v[236:239]\n"
"buffer_load_dwordx4 v24, s[40:43], s65 offen offset:0 lds\n"
"v_mfma_f32_16x16x32_bf16 v[244:247], v[8:11], a[240:243], v[244:247]\n"
"buffer_load_dwordx4 v25, s[40:43], s65 offen offset:1024 lds\n"
"v_mfma_f32_16x16x32_bf16 v[244:247], v[12:15], a[244:247], v[244:247]\n"
"v_mfma_f32_16x16x32_bf16 v[252:255], v[8:11], a[248:251], v[252:255]\n"
"buffer_load_dwordx4 v26, s[40:43], s65 offen offset:2048 lds\n"
"v_mfma_f32_16x16x32_bf16 v[252:255], v[12:15], a[252:255], v[252:255]\n"
"s_waitcnt lgkmcnt(0)\n"
"v_mfma_f32_16x16x32_bf16 v[64:67], v[0:3], a[160:163], v[64:67]\n"
"ds_read_b64_tr_b16 v[8:9], v35 offset:8200\n"
"v_mfma_f32_16x16x32_bf16 v[64:67], v[4:7], a[164:167], v[64:67]\n"
"ds_read_b64_tr_b16 v[10:11], v35 offset:8456\n"
"v_mfma_f32_16x16x32_bf16 v[72:75], v[0:3], a[168:171], v[72:75]\n"
"ds_read_b64_tr_b16 v[12:13], v35 offset:12296\n"
"v_mfma_f32_16x16x32_bf16 v[72:75], v[4:7], a[172:175], v[72:75]\n"
"ds_read_b64_tr_b16 v[14:15], v35 offset:12552\n"
"v_mfma_f32_16x16x32_bf16 v[80:83], v[0:3], a[176:179], v[80:83]\n"
"v_mfma_f32_16x16x32_bf16 v[80:83], v[4:7], a[180:183], v[80:83]\n"
"v_mfma_f32_16x16x32_bf16 v[88:91], v[0:3], a[184:187], v[88:91]\n"
"v_mfma_f32_16x16x32_bf16 v[88:91], v[4:7], a[188:191], v[88:91]\n"
"v_mfma_f32_16x16x32_bf16 v[96:99], v[0:3], a[192:195], v[96:99]\n"
"buffer_load_dwordx4 v27, s[40:43], s65 offen offset:3072 lds\n"
"v_mfma_f32_16x16x32_bf16 v[96:99], v[4:7], a[196:199], v[96:99]\n"
"s_add_u32 s66, s80, 768\n"
"s_add_u32 m0, s67, 24576\n"
"s_and_b32 s66, s66, 0x7ff\n"
"v_mfma_f32_16x16x32_bf16 v[104:107], v[0:3], a[200:203], v[104:107]\n"
"buffer_load_dwordx4 v28, s[36:39], s66 offen offset:0 lds\n"
"v_mfma_f32_16x16x32_bf16 v[104:107], v[4:7], a[204:207], v[104:107]\n"
"s_waitcnt lgkmcnt(0)\n"
"v_mfma_f32_16x16x32_bf16 v[68:71], v[8:11], a[160:163], v[68:71]\n"
"ds_read_b64_tr_b16 v[0:1], v35 offset:8704\n"
"v_mfma_f32_16x16x32_bf16 v[68:71], v[12:15], a[164:167], v[68:71]\n"
"ds_read_b64_tr_b16 v[2:3], v35 offset:8960\n"
"v_mfma_f32_16x16x32_bf16 v[76:79], v[8:11], a[168:171], v[76:79]\n"
"ds_read_b64_tr_b16 v[4:5], v35 offset:12800\n"
"v_mfma_f32_16x16x32_bf16 v[76:79], v[12:15], a[172:175], v[76:79]\n"
"ds_read_b64_tr_b16 v[6:7], v35 offset:13056\n"
"v_mfma_f32_16x16x32_bf16 v[84:87], v[8:11], a[176:179], v[84:87]\n"
"v_mfma_f32_16x16x32_bf16 v[84:87], v[12:15], a[180:183], v[84:87]\n"
"v_mfma_f32_16x16x32_bf16 v[92:95], v[8:11], a[184:187], v[92:95]\n"
"v_mfma_f32_16x16x32_bf16 v[92:95], v[12:15], a[188:191], v[92:95]\n"
"buffer_load_dwordx4 v29, s[36:39], s66 offen offset:1024 lds\n"
"v_mfma_f32_16x16x32_bf16 v[100:103], v[8:11], a[192:195], v[100:103]\n"
"v_mfma_f32_16x16x32_bf16 v[100:103], v[12:15], a[196:199], v[100:103]\n"
"buffer_load_dwordx4 v30, s[36:39], s66 offen offset:2048 lds\n"
"v_mfma_f32_16x16x32_bf16 v[108:111], v[8:11], a[200:203], v[108:111]\n"
"v_mfma_f32_16x16x32_bf16 v[108:111], v[12:15], a[204:207], v[108:111]\n"
"s_waitcnt lgkmcnt(0)\n"
"v_mfma_f32_16x16x32_bf16 v[112:115], v[0:3], a[160:163], v[112:115]\n"
"ds_read_b64_tr_b16 v[8:9], v35 offset:8712\n"
"v_mfma_f32_16x16x32_bf16 v[112:115], v[4:7], a[164:167], v[112:115]\n"
"ds_read_b64_tr_b16 v[10:11], v35 offset:8968\n"
"v_mfma_f32_16x16x32_bf16 v[120:123], v[0:3], a[168:171], v[120:123]\n"
"ds_read_b64_tr_b16 v[12:13], v35 offset:12808\n"
"v_mfma_f32_16x16x32_bf16 v[120:123], v[4:7], a[172:175], v[120:123]\n"
"ds_read_b64_tr_b16 v[14:15], v35 offset:13064\n"
"v_mfma_f32_16x16x32_bf16 v[128:131], v[0:3], a[176:179], v[128:131]\n"
"v_mfma_f32_16x16x32_bf16 v[128:131], v[4:7], a[180:183], v[128:131]\n"
"v_mfma_f32_16x16x32_bf16 v[136:139], v[0:3], a[184:187], v[136:139]\n"
"v_mfma_f32_16x16x32_bf16 v[136:139], v[4:7], a[188:191], v[136:139]\n"
"s_cmp_eq_u32 s94, 1\n"
"s_cselect_b64 vcc, -1, 0\n"
"s_add_u32 s56, s82, 0\n"
"s_and_b32 s56, s56, 0x3ffff\n"
"s_cbranch_vccz K1_KS0_1_0\n"
"buffer_load_dwordx4 a[0:3], v44, s[48:51], s56 offen offset:0 nt\n"
"buffer_load_dwordx4 a[4:7], v45, s[48:51], s56 offen offset:1024 nt\n"
"buffer_load_dwordx4 a[8:11], v46, s[48:51], s56 offen offset:2048 nt\n"
"buffer_load_dwordx4 a[12:15], v47, s[48:51], s56 offen offset:3072 nt\n"
"K1_KS0_1_0:\n"
"v_mfma_f32_16x16x32_bf16 v[144:147], v[0:3], a[192:195], v[144:147]\n"
"v_mfma_f32_16x16x32_bf16 v[144:147], v[4:7], a[196:199], v[144:147]\n"
"s_add_u32 s56, s82, 4096\n"
"s_and_b32 s56, s56, 0x3ffff\n"
"s_cbranch_vccz K1_KS0_1_4\n"
"buffer_load_dwordx4 a[16:19], v44, s[48:51], s56 offen offset:0 nt\n"
"buffer_load_dwordx4 a[20:23], v45, s[48:51], s56 offen offset:1024 nt\n"
"buffer_load_dwordx4 a[24:27], v46, s[48:51], s56 offen offset:2048 nt\n"
"buffer_load_dwordx4 a[28:31], v47, s[48:51], s56 offen offset:3072 nt\n"
"K1_KS0_1_4:\n"
"v_mfma_f32_16x16x32_bf16 v[152:155], v[0:3], a[200:203], v[152:155]\n"
"v_mfma_f32_16x16x32_bf16 v[152:155], v[4:7], a[204:207], v[152:155]\n"
"s_waitcnt vmcnt(29) lgkmcnt(0)\n"
"s_barrier\n"
"v_mfma_f32_16x16x32_bf16 v[116:119], v[8:11], a[160:163], v[116:119]\n"
"ds_read_b64_tr_b16 v[0:1], v35 offset:16384\n"
"v_mfma_f32_16x16x32_bf16 v[116:119], v[12:15], a[164:167], v[116:119]\n"
"ds_read_b64_tr_b16 v[2:3], v35 offset:16640\n"
"v_mfma_f32_16x16x32_bf16 v[124:127], v[8:11], a[168:171], v[124:127]\n"
"ds_read_b64_tr_b16 v[4:5], v35 offset:20480\n"
"v_mfma_f32_16x16x32_bf16 v[124:127], v[12:15], a[172:175], v[124:127]\n"
"ds_read_b64_tr_b16 v[6:7], v35 offset:20736\n"
"v_mfma_f32_16x16x32_bf16 v[132:135], v[8:11], a[176:179], v[132:135]\n"
"ds_read_b128 a[208:211], v34 offset:49152\n"
"v_mfma_f32_16x16x32_bf16 v[132:135], v[12:15], a[180:183], v[132:135]\n"
"s_add_u32 s65, s81, 2621568\n"
"ds_read_b128 a[212:215], v34 offset:49664\n"
"v_mfma_f32_16x16x32_bf16 v[140:143], v[8:11], a[184:187], v[140:143]\n"
"s_add_u32 m0, s68, 8192\n"
"s_and_b32 s65, s65, 0x7fffff\n"
"ds_read_b128 a[216:219], v34 offset:51200\n"
"v_mfma_f32_16x16x32_bf16 v[140:143], v[12:15], a[188:191], v[140:143]\n"
"buffer_load_dwordx4 v24, s[40:43], s65 offen offset:0 lds\n"
"v_mfma_f32_16x16x32_bf16 v[148:151], v[8:11], a[192:195], v[148:151]\n"
"v_mfma_f32_16x16x32_bf16 v[148:151], v[12:15], a[196:199], v[148:151]\n"
"buffer_load_dwordx4 v25, s[40:43], s65 offen offset:1024 lds\n"
"v_mfma_f32_16x16x32_bf16 v[156:159], v[8:11], a[200:203], v[156:159]\n"
"v_mfma_f32_16x16x32_bf16 v[156:159], v[12:15], a[204:207], v[156:159]\n"
"s_waitcnt lgkmcnt(3)\n"
"v_mfma_f32_16x16x32_bf16 v[160:163], v[0:3], a[160:163], v[160:163]\n"
"ds_read_b64_tr_b16 v[8:9], v35 offset:16392\n"
"v_mfma_f32_16x16x32_bf16 v[160:163], v[4:7], a[164:167], v[160:163]\n"
"ds_read_b64_tr_b16 v[10:11], v35 offset:16648\n"
"v_mfma_f32_16x16x32_bf16 v[168:171], v[0:3], a[168:171], v[168:171]\n"
"ds_read_b64_tr_b16 v[12:13], v35 offset:20488\n"
"v_mfma_f32_16x16x32_bf16 v[168:171], v[4:7], a[172:175], v[168:171]\n"
"ds_read_b64_tr_b16 v[14:15], v35 offset:20744\n"
"v_mfma_f32_16x16x32_bf16 v[176:179], v[0:3], a[176:179], v[176:179]\n"
"ds_read_b128 a[220:223], v34 offset:51712\n"
"v_mfma_f32_16x16x32_bf16 v[176:179], v[4:7], a[180:183], v[176:179]\n"
"ds_read_b128 a[224:227], v34 offset:53248\n"
"v_mfma_f32_16x16x32_bf16 v[184:187], v[0:3], a[184:187], v[184:187]\n"
"ds_read_b128 a[228:231], v34 offset:53760\n"
"v_mfma_f32_16x16x32_bf16 v[184:187], v[4:7], a[188:191], v[184:187]\n"
"buffer_load_dwordx4 v26, s[40:43], s65 offen offset:2048 lds\n"
"v_mfma_f32_16x16x32_bf16 v[192:195], v[0:3], a[192:195], v[192:195]\n"
"v_mfma_f32_16x16x32_bf16 v[192:195], v[4:7], a[196:199], v[192:195]\n"
"buffer_load_dwordx4 v27, s[40:43], s65 offen offset:3072 lds\n"
"v_mfma_f32_16x16x32_bf16 v[200:203], v[0:3], a[200:203], v[200:203]\n"
"v_mfma_f32_16x16x32_bf16 v[200:203], v[4:7], a[204:207], v[200:203]\n"
"s_waitcnt lgkmcnt(3)\n"
"v_mfma_f32_16x16x32_bf16 v[164:167], v[8:11], a[160:163], v[164:167]\n"
"ds_read_b64_tr_b16 v[0:1], v35 offset:16896\n"
"v_mfma_f32_16x16x32_bf16 v[164:167], v[12:15], a[164:167], v[164:167]\n"
"ds_read_b64_tr_b16 v[2:3], v35 offset:17152\n"
"v_mfma_f32_16x16x32_bf16 v[172:175], v[8:11], a[168:171], v[172:175]\n"
"ds_read_b64_tr_b16 v[4:5], v35 offset:20992\n"
"v_mfma_f32_16x16x32_bf16 v[172:175], v[12:15], a[172:175], v[172:175]\n"
"ds_read_b64_tr_b16 v[6:7], v35 offset:21248\n"
"v_mfma_f32_16x16x32_bf16 v[180:183], v[8:11], a[176:179], v[180:183]\n"
"ds_read_b128 a[232:235], v34 offset:55296\n"
"v_mfma_f32_16x16x32_bf16 v[180:183], v[12:15], a[180:183], v[180:183]\n"
"ds_read_b128 a[236:239], v34 offset:55808\n"
"v_mfma_f32_16x16x32_bf16 v[188:191], v[8:11], a[184:187], v[188:191]\n"
"s_add_u32 s66, s80, 768\n"
"ds_read_b128 a[240:243], v34 offset:57344\n"
"v_mfma_f32_16x16x32_bf16 v[188:191], v[12:15], a[188:191], v[188:191]\n"
"s_add_u32 m0, s67, 27648\n"
"s_and_b32 s66, s66, 0x7ff\n"
"v_mfma_f32_16x16x32_bf16 v[196:199], v[8:11], a[192:195], v[196:199]\n"
"buffer_load_dwordx4 v31, s[36:39], s66 offen offset:0 lds\n"
"v_mfma_f32_16x16x32_bf16 v[196:199], v[12:15], a[196:199], v[196:199]\n"
"v_mfma_f32_16x16x32_bf16 v[204:207], v[8:11], a[200:203], v[204:207]\n"
"buffer_load_dwordx4 v32, s[36:39], s66 offen offset:1024 lds\n"
"v_mfma_f32_16x16x32_bf16 v[204:207], v[12:15], a[204:207], v[204:207]\n"
"s_waitcnt lgkmcnt(3)\n"
"v_mfma_f32_16x16x32_bf16 v[208:211], v[0:3], a[160:163], v[208:211]\n"
"ds_read_b64_tr_b16 v[8:9], v35 offset:16904\n"
"v_mfma_f32_16x16x32_bf16 v[208:211], v[4:7], a[164:167], v[208:211]\n"
"ds_read_b64_tr_b16 v[10:11], v35 offset:17160\n"
"v_mfma_f32_16x16x32_bf16 v[216:219], v[0:3], a[168:171], v[216:219]\n"
"ds_read_b64_tr_b16 v[12:13], v35 offset:21000\n"
"v_mfma_f32_16x16x32_bf16 v[216:219], v[4:7], a[172:175], v[216:219]\n"
"ds_read_b64_tr_b16 v[14:15], v35 offset:21256\n"
"v_mfma_f32_16x16x32_bf16 v[224:227], v[0:3], a[176:179], v[224:227]\n"
"ds_read_b128 a[244:247], v34 offset:57856\n"
"v_mfma_f32_16x16x32_bf16 v[224:227], v[4:7], a[180:183], v[224:227]\n"
"ds_read_b128 a[248:251], v34 offset:59392\n"
"v_mfma_f32_16x16x32_bf16 v[232:235], v[0:3], a[184:187], v[232:235]\n"
"ds_read_b128 a[252:255], v34 offset:59904\n"
"v_mfma_f32_16x16x32_bf16 v[232:235], v[4:7], a[188:191], v[232:235]\n"
"v_mfma_f32_16x16x32_bf16 v[240:243], v[0:3], a[192:195], v[240:243]\n"
"buffer_load_dwordx4 v33, s[36:39], s66 offen offset:2048 lds\n"
"v_mfma_f32_16x16x32_bf16 v[240:243], v[4:7], a[196:199], v[240:243]\n"
"v_mfma_f32_16x16x32_bf16 v[248:251], v[0:3], a[200:203], v[248:251]\n"
"v_mfma_f32_16x16x32_bf16 v[248:251], v[4:7], a[204:207], v[248:251]\n"
"s_bitcmp1_b32 1, s94\n"
"s_cbranch_scc0 K1_W4\n"
"s_waitcnt vmcnt(24)\n"
"K1_W4:\n"
"s_waitcnt vmcnt(32) lgkmcnt(0)\n"
"s_barrier\n"
"v_mfma_f32_16x16x32_bf16 v[212:215], v[8:11], a[160:163], v[212:215]\n"
"ds_read_b64_tr_b16 v[0:1], v35 offset:24576\n"
"v_mfma_f32_16x16x32_bf16 v[212:215], v[12:15], a[164:167], v[212:215]\n"
"ds_read_b64_tr_b16 v[2:3], v35 offset:24832\n"
"v_mfma_f32_16x16x32_bf16 v[220:223], v[8:11], a[168:171], v[220:223]\n"
"ds_read_b64_tr_b16 v[4:5], v35 offset:28672\n"
"v_mfma_f32_16x16x32_bf16 v[220:223], v[12:15], a[172:175], v[220:223]\n"
"ds_read_b64_tr_b16 v[6:7], v35 offset:28928\n"
"v_mfma_f32_16x16x32_bf16 v[228:231], v[8:11], a[176:179], v[228:231]\n"
"v_mfma_f32_16x16x32_bf16 v[228:231], v[12:15], a[180:183], v[228:231]\n"
"s_add_u32 s65, s81, 3145728\n"
"v_readfirstlane_b32 s88, v20\n"
"v_mfma_f32_16x16x32_bf16 v[236:239], v[8:11], a[184:187], v[236:239]\n"
"s_add_u32 m0, s68, 16384\n"
"s_and_b32 s65, s65, 0x7fffff\n"
"v_readfirstlane_b32 s89, v21\n"
"v_mfma_f32_16x16x32_bf16 v[236:239], v[12:15], a[188:191], v[236:239]\n"
"buffer_load_dwordx4 v24, s[40:43], s65 offen offset:0 lds\n"
"v_mfma_f32_16x16x32_bf16 v[244:247], v[8:11], a[192:195], v[244:247]\n"
"buffer_load_dwordx4 v25, s[40:43], s65 offen offset:1024 lds\n"
"v_mfma_f32_16x16x32_bf16 v[244:247], v[12:15], a[196:199], v[244:247]\n"
"v_mfma_f32_16x16x32_bf16 v[252:255], v[8:11], a[200:203], v[252:255]\n"
"buffer_load_dwordx4 v26, s[40:43], s65 offen offset:2048 lds\n"
"v_mfma_f32_16x16x32_bf16 v[252:255], v[12:15], a[204:207], v[252:255]\n"
"s_waitcnt lgkmcnt(0)\n"
"v_mfma_f32_16x16x32_bf16 v[64:67], v[0:3], a[208:211], v[64:67]\n"
"ds_read_b64_tr_b16 v[8:9], v35 offset:24584\n"
"v_mfma_f32_16x16x32_bf16 v[64:67], v[4:7], a[212:215], v[64:67]\n"
"ds_read_b64_tr_b16 v[10:11], v35 offset:24840\n"
"v_mfma_f32_16x16x32_bf16 v[72:75], v[0:3], a[216:219], v[72:75]\n"
"ds_read_b64_tr_b16 v[12:13], v35 offset:28680\n"
"v_mfma_f32_16x16x32_bf16 v[72:75], v[4:7], a[220:223], v[72:75]\n"
"ds_read_b64_tr_b16 v[14:15], v35 offset:28936\n"
"v_mfma_f32_16x16x32_bf16 v[80:83], v[0:3], a[224:227], v[80:83]\n"
"v_mfma_f32_16x16x32_bf16 v[80:83], v[4:7], a[228:231], v[80:83]\n"
"s_add_u32 s88, s88, 0x7f4a7c15\n"
"s_addc_u32 s89, s89, 0x9e3779b9\n"
"v_mfma_f32_16x16x32_bf16 v[88:91], v[0:3], a[232:235], v[88:91]\n"
"v_mfma_f32_16x16x32_bf16 v[88:91], v[4:7], a[236:239], v[88:91]\n"
"v_mfma_f32_16x16x32_bf16 v[96:99], v[0:3], a[240:243], v[96:99]\n"
"buffer_load_dwordx4 v27, s[40:43], s65 offen offset:3072 lds\n"
"v_mfma_f32_16x16x32_bf16 v[96:99], v[4:7], a[244:247], v[96:99]\n"
"s_add_u32 s66, s80, 896\n"
"s_add_u32 m0, s67, 49152\n"
"s_and_b32 s66, s66, 0x7ff\n"
"v_mfma_f32_16x16x32_bf16 v[104:107], v[0:3], a[248:251], v[104:107]\n"
"buffer_load_dwordx4 v28, s[36:39], s66 offen offset:0 lds\n"
"v_mfma_f32_16x16x32_bf16 v[104:107], v[4:7], a[252:255], v[104:107]\n"
"s_waitcnt lgkmcnt(0)\n"
"v_mfma_f32_16x16x32_bf16 v[68:71], v[8:11], a[208:211], v[68:71]\n"
"ds_read_b64_tr_b16 v[0:1], v35 offset:25088\n"
"v_mfma_f32_16x16x32_bf16 v[68:71], v[12:15], a[212:215], v[68:71]\n"
"ds_read_b64_tr_b16 v[2:3], v35 offset:25344\n"
"v_mfma_f32_16x16x32_bf16 v[76:79], v[8:11], a[216:219], v[76:79]\n"
"ds_read_b64_tr_b16 v[4:5], v35 offset:29184\n"
"v_mfma_f32_16x16x32_bf16 v[76:79], v[12:15], a[220:223], v[76:79]\n"
"ds_read_b64_tr_b16 v[6:7], v35 offset:29440\n"
"v_mfma_f32_16x16x32_bf16 v[84:87], v[8:11], a[224:227], v[84:87]\n"
"v_mfma_f32_16x16x32_bf16 v[84:87], v[12:15], a[228:231], v[84:87]\n"
"v_mfma_f32_16x16x32_bf16 v[92:95], v[8:11], a[232:235], v[92:95]\n"
"v_mfma_f32_16x16x32_bf16 v[92:95], v[12:15], a[236:239], v[92:95]\n"
"buffer_load_dwordx4 v29, s[36:39], s66 offen offset:1024 lds\n"
"v_mfma_f32_16x16x32_bf16 v[100:103], v[8:11], a[240:243], v[100:103]\n"
"v_mfma_f32_16x16x32_bf16 v[100:103], v[12:15], a[244:247], v[100:103]\n"
"buffer_load_dwordx4 v30, s[36:39], s66 offen offset:2048 lds\n"
"v_mfma_f32_16x16x32_bf16 v[108:111], v[8:11], a[248:251], v[108:111]\n"
"v_mfma_f32_16x16x32_bf16 v[108:111], v[12:15], a[252:255], v[108:111]\n"
"s_waitcnt lgkmcnt(0)\n"
"v_mfma_f32_16x16x32_bf16 v[112:115], v[0:3], a[208:211], v[112:115]\n"
"ds_read_b64_tr_b16 v[8:9], v35 offset:25096\n"
"v_mfma_f32_16x16x32_bf16 v[112:115], v[4:7], a[212:215], v[112:115]\n"
"ds_read_b64_tr_b16 v[10:11], v35 offset:25352\n"
"v_mfma_f32_16x16x32_bf16 v[120:123], v[0:3], a[216:219], v[120:123]\n"
"ds_read_b64_tr_b16 v[12:13], v35 offset:29192\n"
"v_mfma_f32_16x16x32_bf16 v[120:123], v[4:7], a[220:223], v[120:123]\n"
"ds_read_b64_tr_b16 v[14:15], v35 offset:29448\n"
"v_mfma_f32_16x16x32_bf16 v[128:131], v[0:3], a[224:227], v[128:131]\n"
"v_mfma_f32_16x16x32_bf16 v[128:131], v[4:7], a[228:231], v[128:131]\n"
"v_mfma_f32_16x16x32_bf16 v[136:139], v[0:3], a[232:235], v[136:139]\n"
"v_mfma_f32_16x16x32_bf16 v[136:139], v[4:7], a[236:239], v[136:139]\n"
"s_cmp_eq_u32 s94, 0\n"
"s_cselect_b64 vcc, -1, 0\n"
"s_add_u32 s56, s82, 8192\n"
"s_and_b32 s56, s56, 0x3ffff\n"
"s_cbranch_vccz K1_KS1_0_0\n"
"buffer_load_dwordx4 a[32:35], v44, s[48:51], s56 offen offset:0 nt\n"
"buffer_load_dwordx4 a[36:39], v45, s[48:51], s56 offen offset:1024 nt\n"
"buffer_load_dwordx4 a[40:43], v46, s[48:51], s56 offen offset:2048 nt\n"
"buffer_load_dwordx4 a[44:47], v47, s[48:51], s56 offen offset:3072 nt\n"
"K1_KS1_0_0:\n"
"v_mfma_f32_16x16x32_bf16 v[144:147], v[0:3], a[240:243], v[144:147]\n"
"v_mfma_f32_16x16x32_bf16 v[144:147], v[4:7], a[244:247], v[144:147]\n"
"s_add_u32 s56, s82, 12288\n"
"s_and_b32 s56, s56, 0x3ffff\n"
"s_cbranch_vccz K1_KS1_0_4\n"
"buffer_load_dwordx4 a[48:51], v44, s[48:51], s56 offen offset:0 nt\n"
"buffer_load_dwordx4 a[52:55], v45, s[48:51], s56 offen offset:1024 nt\n"
"buffer_load_dwordx4 a[56:59], v46, s[48:51], s56 offen offset:2048 nt\n"
"buffer_load_dwordx4 a[60:63], v47, s[48:51], s56 offen offset:3072 nt\n"
"K1_KS1_0_4:\n"
"v_mfma_f32_16x16x32_bf16 v[152:155], v[0:3], a[248:251], v[152:155]\n"
"v_mfma_f32_16x16x32_bf16 v[152:155], v[4:7], a[252:255], v[152:155]\n"
"s_waitcnt vmcnt(29) lgkmcnt(0)\n"
"s_barrier\n"
"v_mfma_f32_16x16x32_bf16 v[116:119], v[8:11], a[208:211], v[116:119]\n"
"ds_read_b64_tr_b16 v[0:1], v35 offset:32768\n"
"v_mfma_f32_16x16x32_bf16 v[116:119], v[12:15], a[212:215], v[116:119]\n"
"ds_read_b64_tr_b16 v[2:3], v35 offset:33024\n"
"v_mfma_f32_16x16x32_bf16 v[124:127], v[8:11], a[216:219], v[124:127]\n"
"ds_read_b64_tr_b16 v[4:5], v35 offset:36864\n"
"v_mfma_f32_16x16x32_bf16 v[124:127], v[12:15], a[220:223], v[124:127]\n"
"ds_read_b64_tr_b16 v[6:7], v35 offset:37120\n"
"v_mfma_f32_16x16x32_bf16 v[132:135], v[8:11], a[224:227], v[132:135]\n"
"ds_read_b128 a[160:163], v34 offset:0\n"
"v_mfma_f32_16x16x32_bf16 v[132:135], v[12:15], a[228:231], v[132:135]\n"
"s_add_u32 s65, s81, 3145856\n"
"ds_read_b128 a[164:167], v34 offset:512\n"
"v_mfma_f32_16x16x32_bf16 v[140:143], v[8:11], a[232:235], v[140:143]\n"
"s_add_u32 m0, s68, 24576\n"
"s_and_b32 s65, s65, 0x7fffff\n"
"ds_read_b128 a[168:171], v34 offset:2048\n"
"v_mfma_f32_16x16x32_bf16 v[140:143], v[12:15], a[236:239], v[140:143]\n"
"buffer_load_dwordx4 v24, s[40:43], s65 offen offset:0 lds\n"
"v_mfma_f32_16x16x32_bf16 v[148:151], v[8:11], a[240:243], v[148:151]\n"
"v_mfma_f32_16x16x32_bf16 v[148:151], v[12:15], a[244:247], v[148:151]\n"
"buffer_load_dwordx4 v25, s[40:43], s65 offen offset:1024 lds\n"
"v_mfma_f32_16x16x32_bf16 v[156:159], v[8:11], a[248:251], v[156:159]\n"
"v_mfma_f32_16x16x32_bf16 v[156:159], v[12:15], a[252:255], v[156:159]\n"
"s_waitcnt lgkmcnt(3)\n"
"v_mfma_f32_16x16x32_bf16 v[160:163], v[0:3], a[208:211], v[160:163]\n"
"ds_read_b64_tr_b16 v[8:9], v35 offset:32776\n"
"v_mfma_f32_16x16x32_bf16 v[160:163], v[4:7], a[212:215], v[160:163]\n"
"ds_read_b64_tr_b16 v[10:11], v35 offset:33032\n"
"v_mfma_f32_16x16x32_bf16 v[168:171], v[0:3], a[216:219], v[168:171]\n"
"ds_read_b64_tr_b16 v[12:13], v35 offset:36872\n"
"v_mfma_f32_16x16x32_bf16 v[168:171], v[4:7], a[220:223], v[168:171]\n"
"ds_read_b64_tr_b16 v[14:15], v35 offset:37128\n"
"v_mfma_f32_16x16x32_bf16 v[176:179], v[0:3], a[224:227], v[176:179]\n"
"ds_read_b128 a[172:175], v34 offset:2560\n"
"v_mfma_f32_16x16x32_bf16 v[176:179], v[4:7], a[228:231], v[176:179]\n"
"ds_read_b128 a[176:179], v34 offset:4096\n"
"v_mfma_f32_16x16x32_bf16 v[184:187], v[0:3], a[232:235], v[184:187]\n"
"ds_read_b128 a[180:183], v34 offset:4608\n"
"v_mfma_f32_16x16x32_bf16 v[184:187], v[4:7], a[236:239], v[184:187]\n"
"buffer_load_dwordx4 v26, s[40:43], s65 offen offset:2048 lds\n"
"v_mfma_f32_16x16x32_bf16 v[192:195], v[0:3], a[240:243], v[192:195]\n"
"v_mfma_f32_16x16x32_bf16 v[192:195], v[4:7], a[244:247], v[192:195]\n"
"buffer_load_dwordx4 v27, s[40:43], s65 offen offset:3072 lds\n"
"v_mfma_f32_16x16x32_bf16 v[200:203], v[0:3], a[248:251], v[200:203]\n"
"v_mfma_f32_16x16x32_bf16 v[200:203], v[4:7], a[252:255], v[200:203]\n"
"s_waitcnt lgkmcnt(3)\n"
"v_mfma_f32_16x16x32_bf16 v[164:167], v[8:11], a[208:211], v[164:167]\n"
"ds_read_b64_tr_b16 v[0:1], v35 offset:33280\n"
"v_mfma_f32_16x16x32_bf16 v[164:167], v[12:15], a[212:215], v[164:167]\n"
"ds_read_b64_tr_b16 v[2:3], v35 offset:33536\n"
"v_mfma_f32_16x16x32_bf16 v[172:175], v[8:11], a[216:219], v[172:175]\n"
"ds_read_b64_tr_b16 v[4:5], v35 offset:37376\n"
"v_mfma_f32_16x16x32_bf16 v[172:175], v[12:15], a[220:223], v[172:175]\n"
"ds_read_b64_tr_b16 v[6:7], v35 offset:37632\n"
"v_mfma_f32_16x16x32_bf16 v[180:183], v[8:11], a[224:227], v[180:183]\n"
"ds_read_b128 a[184:187], v34 offset:6144\n"
"v_mfma_f32_16x16x32_bf16 v[180:183], v[12:15], a[228:231], v[180:183]\n"
"ds_read_b128 a[188:191], v34 offset:6656\n"
"v_mfma_f32_16x16x32_bf16 v[188:191], v[8:11], a[232:235], v[188:191]\n"
"s_add_u32 s66, s80, 896\n"
"ds_read_b128 a[192:195], v34 offset:8192\n"
"v_mfma_f32_16x16x32_bf16 v[188:191], v[12:15], a[236:239], v[188:191]\n"
"s_add_u32 m0, s67, 52224\n"
"s_and_b32 s66, s66, 0x7ff\n"
"v_mfma_f32_16x16x32_bf16 v[196:199], v[8:11], a[240:243], v[196:199]\n"
"buffer_load_dwordx4 v31, s[36:39], s66 offen offset:0 lds\n"
"v_mfma_f32_16x16x32_bf16 v[196:199], v[12:15], a[244:247], v[196:199]\n"
"v_mfma_f32_16x16x32_bf16 v[204:207], v[8:11], a[248:251], v[204:207]\n"
"buffer_load_dwordx4 v32, s[36:39], s66 offen offset:1024 lds\n"
"v_mfma_f32_16x16x32_bf16 v[204:207], v[12:15], a[252:255], v[204:207]\n"
"s_waitcnt lgkmcnt(3)\n"
"v_mfma_f32_16x16x32_bf16 v[208:211], v[0:3], a[208:211], v[208:211]\n"
"ds_read_b64_tr_b16 v[8:9], v35 offset:33288\n"
"v_mfma_f32_16x16x32_bf16 v[208:211], v[4:7], a[212:215], v[208:211]\n"
"ds_read_b64_tr_b16 v[10:11], v35 offset:33544\n"
"v_mfma_f32_16x16x32_bf16 v[216:219], v[0:3], a[216:219], v[216:219]\n"
"ds_read_b64_tr_b16 v[12:13], v35 offset:37384\n"
"v_mfma_f32_16x16x32_bf16 v[216:219], v[4:7], a[220:223], v[216:219]\n"
"ds_read_b64_tr_b16 v[14:15], v35 offset:37640\n"
"v_mfma_f32_16x16x32_bf16 v[224:227], v[0:3], a[224:227], v[224:227]\n"
"ds_read_b128 a[196:199], v34 offset:8704\n"
"v_mfma_f32_16x16x32_bf16 v[224:227], v[4:7], a[228:231], v[224:227]\n"
"ds_read_b128 a[200:203], v34 offset:10240\n"
"v_mfma_f32_16x16x32_bf16 v[232:235], v[0:3], a[232:235], v[232:235]\n"
"ds_read_b128 a[204:207], v34 offset:10752\n"
"v_mfma_f32_16x16x32_bf16 v[232:235], v[4:7], a[236:239], v[232:235]\n"
"v_mfma_f32_16x16x32_bf16 v[240:243], v[0:3], a[240:243], v[240:243]\n"
"buffer_load_dwordx4 v33, s[36:39], s66 offen offset:2048 lds\n"
"v_mfma_f32_16x16x32_bf16 v[240:243], v[4:7], a[244:247], v[240:243]\n"
"v_mfma_f32_16x16x32_bf16 v[248:251], v[0:3], a[248:251], v[248:251]\n"
"v_mfma_f32_16x16x32_bf16 v[248:251], v[4:7], a[252:255], v[248:251]\n"
"s_waitcnt vmcnt(32) lgkmcnt(0)\n"
"s_barrier\n"
"v_mfma_f32_16x16x32_bf16 v[212:215], v[8:11], a[208:211], v[212:215]\n"
"ds_read_b64_tr_b16 v[0:1], v35 offset:0\n"
"v_mfma_f32_16x16x32_bf16 v[212:215], v[12:15], a[212:215], v[212:215]\n"
"ds_read_b64_tr_b16 v[2:3], v35 offset:256\n"
"v_mfma_f32_16x16x32_bf16 v[220:223], v[8:11], a[216:219], v[220:223]\n"
"ds_read_b64_tr_b16 v[4:5], v35 offset:4096\n"
"v_mfma_f32_16x16x32_bf16 v[220:223], v[12:15], a[220:223], v[220:223]\n"
"ds_read_b64_tr_b16 v[6:7], v35 offset:4352\n"
"v_mfma_f32_16x16x32_bf16 v[228:231], v[8:11], a[224:227], v[228:231]\n"
"v_mfma_f32_16x16x32_bf16 v[228:231], v[12:15], a[228:231], v[228:231]\n"
"s_add_u32 s65, s81, 3670016\n"
"v_mfma_f32_16x16x32_bf16 v[236:239], v[8:11], a[232:235], v[236:239]\n"
"s_add_u32 m0, s68, 32768\n"
"s_and_b32 s65, s65, 0x7fffff\n"
"v_mfma_f32_16x16x32_bf16 v[236:239], v[12:15], a[236:239], v[236:239]\n"
"buffer_load_dwordx4 v24, s[40:43], s65 offen offset:0 lds\n"
"v_mfma_f32_16x16x32_bf16 v[244:247], v[8:11], a[240:243], v[244:247]\n"
"v_mfma_f32_16x16x32_bf16 v[244:247], v[12:15], a[244:247], v[244:247]\n"
"buffer_load_dwordx4 v25, s[40:43], s65 offen offset:1024 lds\n"
"v_mfma_f32_16x16x32_bf16 v[252:255], v[8:11], a[248:251], v[252:255]\n"
"v_mfma_f32_16x16x32_bf16 v[252:255], v[12:15], a[252:255], v[252:255]\n"
"s_waitcnt lgkmcnt(0)\n"
"v_mfma_f32_16x16x32_bf16 v[64:67], v[0:3], a[160:163], v[64:67]\n"
"ds_read_b64_tr_b16 v[8:9], v35 offset:8\n"
"v_mfma_f32_16x16x32_bf16 v[64:67], v[4:7], a[164:167], v[64:67]\n"
"ds_read_b64_tr_b16 v[10:11], v35 offset:264\n"
"v_mfma_f32_16x16x32_bf16 v[72:75], v[0:3], a[168:171], v[72:75]\n"
"ds_read_b64_tr_b16 v[12:13], v35 offset:4104\n"
"v_mfma_f32_16x16x32_bf16 v[72:75], v[4:7], a[172:175], v[72:75]\n"
"ds_read_b64_tr_b16 v[14:15], v35 offset:4360\n"
"v_mfma_f32_16x16x32_bf16 v[80:83], v[0:3], a[176:179], v[80:83]\n"
"v_mfma_f32_16x16x32_bf16 v[80:83], v[4:7], a[180:183], v[80:83]\n"
"v_mfma_f32_16x16x32_bf16 v[88:91], v[0:3], a[184:187], v[88:91]\n"
"v_mfma_f32_16x16x32_bf16 v[88:91], v[4:7], a[188:191], v[88:91]\n"
"buffer_load_dwordx4 v26, s[40:43], s65 offen offset:2048 lds\n"
"v_mfma_f32_16x16x32_bf16 v[96:99], v[0:3], a[192:195], v[96:99]\n"
"v_mfma_f32_16x16x32_bf16 v[96:99], v[4:7], a[196:199], v[96:99]\n"
"buffer_load_dwordx4 v27, s[40:43], s65 offen offset:3072 lds\n"
"v_mfma_f32_16x16x32_bf16 v[104:107], v[0:3], a[200:203], v[104:107]\n"
"v_mfma_f32_16x16x32_bf16 v[104:107], v[4:7], a[204:207], v[104:107]\n"
"s_waitcnt lgkmcnt(0)\n"
"v_mfma_f32_16x16x32_bf16 v[68:71], v[8:11], a[160:163], v[68:71]\n"
"ds_read_b64_tr_b16 v[0:1], v35 offset:512\n"
"v_mfma_f32_16x16x32_bf16 v[68:71], v[12:15], a[164:167], v[68:71]\n"
"ds_read_b64_tr_b16 v[2:3], v35 offset:768\n"
"v_mfma_f32_16x16x32_bf16 v[76:79], v[8:11], a[168:171], v[76:79]\n"
"ds_read_b64_tr_b16 v[4:5], v35 offset:4608\n"
"v_mfma_f32_16x16x32_bf16 v[76:79], v[12:15], a[172:175], v[76:79]\n"
"ds_read_b64_tr_b16 v[6:7], v35 offset:4864\n"
"v_mfma_f32_16x16x32_bf16 v[84:87], v[8:11], a[176:179], v[84:87]\n"
"v_mfma_f32_16x16x32_bf16 v[84:87], v[12:15], a[180:183], v[84:87]\n"
"v_mfma_f32_16x16x32_bf16 v[92:95], v[8:11], a[184:187], v[92:95]\n"
"s_add_u32 s66, s80, 1024\n"
"v_mfma_f32_16x16x32_bf16 v[92:95], v[12:15], a[188:191], v[92:95]\n"
"s_add_u32 m0, s67, 0\n"
"s_and_b32 s66, s66, 0x7ff\n"
"v_mfma_f32_16x16x32_bf16 v[100:103], v[8:11], a[192:195], v[100:103]\n"
"buffer_load_dwordx4 v28, s[36:39], s66 offen offset:0 lds\n"
"v_mfma_f32_16x16x32_bf16 v[100:103], v[12:15], a[196:199], v[100:103]\n"
"v_mfma_f32_16x16x32_bf16 v[108:111], v[8:11], a[200:203], v[108:111]\n"
"buffer_load_dwordx4 v29, s[36:39], s66 offen offset:1024 lds\n"
"v_mfma_f32_16x16x32_bf16 v[108:111], v[12:15], a[204:207], v[108:111]\n"
"s_waitcnt lgkmcnt(0)\n"
"v_mfma_f32_16x16x32_bf16 v[112:115], v[0:3], a[160:163], v[112:115]\n"
"ds_read_b64_tr_b16 v[8:9], v35 offset:520\n"
"v_mfma_f32_16x16x32_bf16 v[112:115], v[4:7], a[164:167], v[112:115]\n"
"ds_read_b64_tr_b16 v[10:11], v35 offset:776\n"
"v_mfma_f32_16x16x32_bf16 v[120:123], v[0:3], a[168:171], v[120:123]\n"
"ds_read_b64_tr_b16 v[12:13], v35 offset:4616\n"
"v_mfma_f32_16x16x32_bf16 v[120:123], v[4:7], a[172:175], v[120:123]\n"
"ds_read_b64_tr_b16 v[14:15], v35 offset:4872\n"
"v_mfma_f32_16x16x32_bf16 v[128:131], v[0:3], a[176:179], v[128:131]\n"
"v_mfma_f32_16x16x32_bf16 v[128:131], v[4:7], a[180:183], v[128:131]\n"
"v_mfma_f32_16x16x32_bf16 v[136:139], v[0:3], a[184:187], v[136:139]\n"
"v_mfma_f32_16x16x32_bf16 v[136:139], v[4:7], a[188:191], v[136:139]\n"
"v_mfma_f32_16x16x32_bf16 v[144:147], v[0:3], a[192:195], v[144:147]\n"
"buffer_load_dwordx4 v30, s[36:39], s66 offen offset:2048 lds\n"
"v_mfma_f32_16x16x32_bf16 v[144:147], v[4:7], a[196:199], v[144:147]\n"
"v_mfma_f32_16x16x32_bf16 v[152:155], v[0:3], a[200:203], v[152:155]\n"
"v_mfma_f32_16x16x32_bf16 v[152:155], v[4:7], a[204:207], v[152:155]\n"
"s_bitcmp1_b32 2, s94\n"
"s_cbranch_scc0 K1_W5\n"
"s_waitcnt vmcnt(21)\n"
"K1_W5:\n"
"s_waitcnt vmcnt(29) lgkmcnt(0)\n"
"s_barrier\n"
"v_mfma_f32_16x16x32_bf16 v[116:119], v[8:11], a[160:163], v[116:119]\n"
"ds_read_b64_tr_b16 v[0:1], v35 offset:8192\n"
"v_mfma_f32_16x16x32_bf16 v[116:119], v[12:15], a[164:167], v[116:119]\n"
"ds_read_b64_tr_b16 v[2:3], v35 offset:8448\n"
"v_mfma_f32_16x16x32_bf16 v[124:127], v[8:11], a[168:171], v[124:127]\n"
"ds_read_b64_tr_b16 v[4:5], v35 offset:12288\n"
"v_mfma_f32_16x16x32_bf16 v[124:127], v[12:15], a[172:175], v[124:127]\n"
"ds_read_b64_tr_b16 v[6:7], v35 offset:12544\n"
"v_mfma_f32_16x16x32_bf16 v[132:135], v[8:11], a[176:179], v[132:135]\n"
"ds_read_b128 a[208:211], v34 offset:24576\n"
"v_mfma_f32_16x16x32_bf16 v[132:135], v[12:15], a[180:183], v[132:135]\n"
"s_add_u32 s65, s81, 3670144\n"
"ds_read_b128 a[212:215], v34 offset:25088\n"
"v_mfma_f32_16x16x32_bf16 v[140:143], v[8:11], a[184:187], v[140:143]\n"
"s_add_u32 m0, s68, 0\n"
"s_and_b32 s65, s65, 0x7fffff\n"
"ds_read_b128 a[216:219], v34 offset:26624\n"
"v_mfma_f32_16x16x32_bf16 v[140:143], v[12:15], a[188:191], v[140:143]\n"
"buffer_load_dwordx4 v24, s[40:43], s65 offen offset:0 lds\n"
"v_mfma_f32_16x16x32_bf16 v[148:151], v[8:11], a[192:195], v[148:151]\n"
"buffer_load_dwordx4 v25, s[40:43], s65 offen offset:1024 lds\n"
"v_mfma_f32_16x16x32_bf16 v[148:151], v[12:15], a[196:199], v[148:151]\n"
"v_mfma_f32_16x16x32_bf16 v[156:159], v[8:11], a[200:203], v[156:159]\n"
"buffer_load_dwordx4 v26, s[40:43], s65 offen offset:2048 lds\n"
"v_mfma_f32_16x16x32_bf16 v[156:159], v[12:15], a[204:207], v[156:159]\n"
"s_waitcnt lgkmcnt(3)\n"
"v_mfma_f32_16x16x32_bf16 v[160:163], v[0:3], a[160:163], v[160:163]\n"
"ds_read_b64_tr_b16 v[8:9], v35 offset:8200\n"
"v_mfma_f32_16x16x32_bf16 v[160:163], v[4:7], a[164:167], v[160:163]\n"
"ds_read_b64_tr_b16 v[10:11], v35 offset:8456\n"
"v_mfma_f32_16x16x32_bf16 v[168:171], v[0:3], a[168:171], v[168:171]\n"
"ds_read_b64_tr_b16 v[12:13], v35 offset:12296\n"
"v_mfma_f32_16x16x32_bf16 v[168:171], v[4:7], a[172:175], v[168:171]\n"
"ds_read_b64_tr_b16 v[14:15], v35 offset:12552\n"
"v_mfma_f32_16x16x32_bf16 v[176:179], v[0:3], a[176:179], v[176:179]\n"
"ds_read_b128 a[220:223], v34 offset:27136\n"
"v_mfma_f32_16x16x32_bf16 v[176:179], v[4:7], a[180:183], v[176:179]\n"
"ds_read_b128 a[224:227], v34 offset:28672\n"
"v_mfma_f32_16x16x32_bf16 v[184:187], v[0:3], a[184:187], v[184:187]\n"
"ds_read_b128 a[228:231], v34 offset:29184\n"
"v_mfma_f32_16x16x32_bf16 v[184:187], v[4:7], a[188:191], v[184:187]\n"
"v_mfma_f32_16x16x32_bf16 v[192:195], v[0:3], a[192:195], v[192:195]\n"
"buffer_load_dwordx4 v27, s[40:43], s65 offen offset:3072 lds\n"
"v_mfma_f32_16x16x32_bf16 v[192:195], v[4:7], a[196:199], v[192:195]\n"
"s_add_u32 s66, s80, 1024\n"
"s_add_u32 m0, s67, 3072\n"
"s_and_b32 s66, s66, 0x7ff\n"
"v_mfma_f32_16x16x32_bf16 v[200:203], v[0:3], a[200:203], v[200:203]\n"
"buffer_load_dwordx4 v31, s[36:39], s66 offen offset:0 lds\n"
"v_mfma_f32_16x16x32_bf16 v[200:203], v[4:7], a[204:207], v[200:203]\n"
"s_waitcnt lgkmcnt(3)\n"
"v_mfma_f32_16x16x32_bf16 v[164:167], v[8:11], a[160:163], v[164:167]\n"
"ds_read_b64_tr_b16 v[0:1], v35 offset:8704\n"
"v_mfma_f32_16x16x32_bf16 v[164:167], v[12:15], a[164:167], v[164:167]\n"
"ds_read_b64_tr_b16 v[2:3], v35 offset:8960\n"
"v_mfma_f32_16x16x32_bf16 v[172:175], v[8:11], a[168:171], v[172:175]\n"
"ds_read_b64_tr_b16 v[4:5], v35 offset:12800\n"
"v_mfma_f32_16x16x32_bf16 v[172:175], v[12:15], a[172:175], v[172:175]\n"
"ds_read_b64_tr_b16 v[6:7], v35 offset:13056\n"
"v_mfma_f32_16x16x32_bf16 v[180:183], v[8:11], a[176:179], v[180:183]\n"
"ds_read_b128 a[232:235], v34 offset:30720\n"
"v_mfma_f32_16x16x32_bf16 v[180:183], v[12:15], a[180:183], v[180:183]\n"
"ds_read_b128 a[236:239], v34 offset:31232\n"
"v_mfma_f32_16x16x32_bf16 v[188:191], v[8:11], a[184:187], v[188:191]\n"
"ds_read_b128 a[240:243], v34 offset:32768\n"
"v_mfma_f32_16x16x32_bf16 v[188:191], v[12:15], a[188:191], v[188:191]\n"
"buffer_load_dwordx4 v32, s[36:39], s66 offen offset:1024 lds\n"
"v_mfma_f32_16x16x32_bf16 v[196:199], v[8:11], a[192:195], v[196:199]\n"
"v_mfma_f32_16x16x32_bf16 v[196:199], v[12:15], a[196:199], v[196:199]\n"
"buffer_load_dwordx4 v33, s[36:39], s66 offen offset:2048 lds\n"
"v_mfma_f32_16x16x32_bf16 v[204:207], v[8:11], a[200:203], v[204:207]\n"
"v_mfma_f32_16x16x32_bf16 v[204:207], v[12:15], a[204:207], v[204:207]\n"
"s_waitcnt lgkmcnt(3)\n"
"v_mfma_f32_16x16x32_bf16 v[208:211], v[0:3], a[160:163], v[208:211]\n"
"ds_read_b64_tr_b16 v[8:9], v35 offset:8712\n"
"v_mfma_f32_16x16x32_bf16 v[208:211], v[4:7], a[164:167], v[208:211]\n"
"ds_read_b64_tr_b16 v[10:11], v35 offset:8968\n"
"v_mfma_f32_16x16x32_bf16 v[216:219], v[0:3], a[168:171], v[216:219]\n"
"ds_read_b64_tr_b16 v[12:13], v35 offset:12808\n"
"v_mfma_f32_16x16x32_bf16 v[216:219], v[4:7], a[172:175], v[216:219]\n"
"ds_read_b64_tr_b16 v[14:15], v35 offset:13064\n"
"v_mfma_f32_16x16x32_bf16 v[224:227], v[0:3], a[176:179], v[224:227]\n"
"ds_read_b128 a[244:247], v34 offset:33280\n"
"v_mfma_f32_16x16x32_bf16 v[224:227], v[4:7], a[180:183], v[224:227]\n"
"ds_read_b128 a[248:251], v34 offset:34816\n"
"v_mfma_f32_16x16x32_bf16 v[232:235], v[0:3], a[184:187], v[232:235]\n"
"ds_read_b128 a[252:255], v34 offset:35328\n"
"v_mfma_f32_16x16x32_bf16 v[232:235], v[4:7], a[188:191], v[232:235]\n"
"s_cmp_eq_u32 s94, 1\n"
"s_cselect_b64 vcc, -1, 0\n"
"s_add_u32 s56, s82, 8192\n"
"s_and_b32 s56, s56, 0x3ffff\n"
"s_cbranch_vccz K1_KS1_1_0\n"
"buffer_load_dwordx4 a[32:35], v44, s[48:51], s56 offen offset:0 nt\n"
"buffer_load_dwordx4 a[36:39], v45, s[48:51], s56 offen offset:1024 nt\n"
"buffer_load_dwordx4 a[40:43], v46, s[48:51], s56 offen offset:2048 nt\n"
"buffer_load_dwordx4 a[44:47], v47, s[48:51], s56 offen offset:3072 nt\n"
"K1_KS1_1_0:\n"
"v_mfma_f32_16x16x32_bf16 v[240:243], v[0:3], a[192:195], v[240:243]\n"
"v_mfma_f32_16x16x32_bf16 v[240:243], v[4:7], a[196:199], v[240:243]\n"
"s_add_u32 s56, s82, 12288\n"
"s_and_b32 s56, s56, 0x3ffff\n"
"s_cbranch_vccz K1_KS1_1_4\n"
"buffer_load_dwordx4 a[48:51], v44, s[48:51], s56 offen offset:0 nt\n"
"buffer_load_dwordx4 a[52:55], v45, s[48:51], s56 offen offset:1024 nt\n"
"buffer_load_dwordx4 a[56:59], v46, s[48:51], s56 offen offset:2048 nt\n"
"buffer_load_dwordx4 a[60:63], v47, s[48:51], s56 offen offset:3072 nt\n"
"K1_KS1_1_4:\n"
"v_mfma_f32_16x16x32_bf16 v[248:251], v[0:3], a[200:203], v[248:251]\n"
"v_mfma_f32_16x16x32_bf16 v[248:251], v[4:7], a[204:207], v[248:251]\n"
"s_waitcnt vmcnt(32) lgkmcnt(0)\n"
"s_barrier\n"
"v_mfma_f32_16x16x32_bf16 v[212:215], v[8:11], a[160:163], v[212:215]\n"
"ds_read_b64_tr_b16 v[0:1], v35 offset:16384\n"
"v_mfma_f32_16x16x32_bf16 v[212:215], v[12:15], a[164:167], v[212:215]\n"
"ds_read_b64_tr_b16 v[2:3], v35 offset:16640\n"
"v_mfma_f32_16x16x32_bf16 v[220:223], v[8:11], a[168:171], v[220:223]\n"
"ds_read_b64_tr_b16 v[4:5], v35 offset:20480\n"
"v_mfma_f32_16x16x32_bf16 v[220:223], v[12:15], a[172:175], v[220:223]\n"
"ds_read_b64_tr_b16 v[6:7], v35 offset:20736\n"
"v_mfma_f32_16x16x32_bf16 v[228:231], v[8:11], a[176:179], v[228:231]\n"
"v_mfma_f32_16x16x32_bf16 v[228:231], v[12:15], a[180:183], v[228:231]\n"
"s_add_u32 s65, s81, 4194304\n"
"v_mfma_f32_16x16x32_bf16 v[236:239], v[8:11], a[184:187], v[236:239]\n"
"s_add_u32 m0, s68, 8192\n"
"s_and_b32 s65, s65, 0x7fffff\n"
"v_mfma_f32_16x16x32_bf16 v[236:239], v[12:15], a[188:191], v[236:239]\n"
"buffer_load_dwordx4 v24, s[40:43], s65 offen offset:0 lds\n"
"v_mfma_f32_16x16x32_bf16 v[244:247], v[8:11], a[192:195], v[244:247]\n"
"v_mfma_f32_16x16x32_bf16 v[244:247], v[12:15], a[196:199], v[244:247]\n"
"buffer_load_dwordx4 v25, s[40:43], s65 offen offset:1024 lds\n"
"v_mfma_f32_16x16x32_bf16 v[252:255], v[8:11], a[200:203], v[252:255]\n"
"v_mfma_f32_16x16x32_bf16 v[252:255], v[12:15], a[204:207], v[252:255]\n"
"s_waitcnt lgkmcnt(0)\n"
"v_mfma_f32_16x16x32_bf16 v[64:67], v[0:3], a[208:211], v[64:67]\n"
"ds_read_b64_tr_b16 v[8:9], v35 offset:16392\n"
"v_mfma_f32_16x16x32_bf16 v[64:67], v[4:7], a[212:215], v[64:67]\n"
"ds_read_b64_tr_b16 v[10:11], v35 offset:16648\n"
"v_mfma_f32_16x16x32_bf16 v[72:75], v[0:3], a[216:219], v[72:75]\n"
"ds_read_b64_tr_b16 v[12:13], v35 offset:20488\n"
"v_mfma_f32_16x16x32_bf16 v[72:75], v[4:7], a[220:223], v[72:75]\n"
"ds_read_b64_tr_b16 v[14:15], v35 offset:20744\n"
"v_mfma_f32_16x16x32_bf16 v[80:83], v[0:3], a[224:227], v[80:83]\n"
"v_mfma_f32_16x16x32_bf16 v[80:83], v[4:7], a[228:231], v[80:83]\n"
"v_mfma_f32_16x16x32_bf16 v[88:91], v[0:3], a[232:235], v[88:91]\n"
"v_mfma_f32_16x16x32_bf16 v[88:91], v[4:7], a[236:239], v[88:91]\n"
"buffer_load_dwordx4 v26, s[40:43], s65 offen offset:2048 lds\n"
"v_mfma_f32_16x16x32_bf16 v[96:99], v[0:3], a[240:243], v[96:99]\n"
"v_mfma_f32_16x16x32_bf16 v[96:99], v[4:7], a[244:247], v[96:99]\n"
"buffer_load_dwordx4 v27, s[40:43], s65 offen offset:3072 lds\n"
"v_mfma_f32_16x16x32_bf16 v[104:107], v[0:3], a[248:251], v[104:107]\n"
"v_mfma_f32_16x16x32_bf16 v[104:107], v[4:7], a[252:255], v[104:107]\n"
"s_waitcnt lgkmcnt(0)\n"
"v_mfma_f32_16x16x32_bf16 v[68:71], v[8:11], a[208:211], v[68:71]\n"
"ds_read_b64_tr_b16 v[0:1], v35 offset:16896\n"
"v_mfma_f32_16x16x32_bf16 v[68:71], v[12:15], a[212:215], v[68:71]\n"
"ds_read_b64_tr_b16 v[2:3], v35 offset:17152\n"
"v_mfma_f32_16x16x32_bf16 v[76:79], v[8:11], a[216:219], v[76:79]\n"
"ds_read_b64_tr_b16 v[4:5], v35 offset:20992\n"
"v_mfma_f32_16x16x32_bf16 v[76:79], v[12:15], a[220:223], v[76:79]\n"
"ds_read_b64_tr_b16 v[6:7], v35 offset:21248\n"
"v_mfma_f32_16x16x32_bf16 v[84:87], v[8:11], a[224:227], v[84:87]\n"
"v_mfma_f32_16x16x32_bf16 v[84:87], v[12:15], a[228:231], v[84:87]\n"
"v_mfma_f32_16x16x32_bf16 v[92:95], v[8:11], a[232:235], v[92:95]\n"
"s_add_u32 s66, s80, 1152\n"
"v_mfma_f32_16x16x32_bf16 v[92:95], v[12:15], a[236:239], v[92:95]\n"
"s_add_u32 m0, s67, 24576\n"
"s_and_b32 s66, s66, 0x7ff\n"
"v_mfma_f32_16x16x32_bf16 v[100:103], v[8:11], a[240:243], v[100:103]\n"
"buffer_load_dwordx4 v28, s[36:39], s66 offen offset:0 lds\n"
"v_mfma_f32_16x16x32_bf16 v[100:103], v[12:15], a[244:247], v[100:103]\n"
"v_mfma_f32_16x16x32_bf16 v[108:111], v[8:11], a[248:251], v[108:111]\n"
"buffer_load_dwordx4 v29, s[36:39], s66 offen offset:1024 lds\n"
"v_mfma_f32_16x16x32_bf16 v[108:111], v[12:15], a[252:255], v[108:111]\n"
"s_waitcnt lgkmcnt(0)\n"
"v_mfma_f32_16x16x32_bf16 v[112:115], v[0:3], a[208:211], v[112:115]\n"
"ds_read_b64_tr_b16 v[8:9], v35 offset:16904\n"
"v_mfma_f32_16x16x32_bf16 v[112:115], v[4:7], a[212:215], v[112:115]\n"
"ds_read_b64_tr_b16 v[10:11], v35 offset:17160\n"
"v_mfma_f32_16x16x32_bf16 v[120:123], v[0:3], a[216:219], v[120:123]\n"
"ds_read_b64_tr_b16 v[12:13], v35 offset:21000\n"
"v_mfma_f32_16x16x32_bf16 v[120:123], v[4:7], a[220:223], v[120:123]\n"
"ds_read_b64_tr_b16 v[14:15], v35 offset:21256\n"
"v_mfma_f32_16x16x32_bf16 v[128:131], v[0:3], a[224:227], v[128:131]\n"
"v_mfma_f32_16x16x32_bf16 v[128:131], v[4:7], a[228:231], v[128:131]\n"
"v_mfma_f32_16x16x32_bf16 v[136:139], v[0:3], a[232:235], v[136:139]\n"
"v_mfma_f32_16x16x32_bf16 v[136:139], v[4:7], a[236:239], v[136:139]\n"
"v_mfma_f32_16x16x32_bf16 v[144:147], v[0:3], a[240:243], v[144:147]\n"
"buffer_load_dwordx4 v30, s[36:39], s66 offen offset:2048 lds\n"
"v_mfma_f32_16x16x32_bf16 v[144:147], v[4:7], a[244:247], v[144:147]\n"
"v_mfma_f32_16x16x32_bf16 v[152:155], v[0:3], a[248:251], v[152:155]\n"
"v_mfma_f32_16x16x32_bf16 v[152:155], v[4:7], a[252:255], v[152:155]\n"
"s_bitcmp1_b32 1, s94\n"
"s_cbranch_scc0 K1_W6\n"
"s_waitcnt vmcnt(21)\n"
"K1_W6:\n"
"s_waitcnt vmcnt(29) lgkmcnt(0)\n"
"s_barrier\n"
"v_mfma_f32_16x16x32_bf16 v[116:119], v[8:11], a[208:211], v[116:119]\n"
"ds_read_b64_tr_b16 v[0:1], v35 offset:24576\n"
"v_mfma_f32_16x16x32_bf16 v[116:119], v[12:15], a[212:215], v[116:119]\n"
"ds_read_b64_tr_b16 v[2:3], v35 offset:24832\n"
"v_mfma_f32_16x16x32_bf16 v[124:127], v[8:11], a[216:219], v[124:127]\n"
"ds_read_b64_tr_b16 v[4:5], v35 offset:28672\n"
"v_mfma_f32_16x16x32_bf16 v[124:127], v[12:15], a[220:223], v[124:127]\n"
"ds_read_b64_tr_b16 v[6:7], v35 offset:28928\n"
"v_mfma_f32_16x16x32_bf16 v[132:135], v[8:11], a[224:227], v[132:135]\n"
"ds_read_b128 a[160:163], v34 offset:49152\n"
"v_mfma_f32_16x16x32_bf16 v[132:135], v[12:15], a[228:231], v[132:135]\n"
"s_add_u32 s65, s81, 4194432\n"
"ds_read_b128 a[164:167], v34 offset:49664\n"
"v_mfma_f32_16x16x32_bf16 v[140:143], v[8:11], a[232:235], v[140:143]\n"
"s_add_u32 m0, s68, 16384\n"
"s_and_b32 s65, s65, 0x7fffff\n"
"ds_read_b128 a[168:171], v34 offset:51200\n"
"v_mfma_f32_16x16x32_bf16 v[140:143], v[12:15], a[236:239], v[140:143]\n"
"buffer_load_dwordx4 v24, s[40:43], s65 offen offset:0 lds\n"
"v_mfma_f32_16x16x32_bf16 v[148:151], v[8:11], a[240:243], v[148:151]\n"
"buffer_load_dwordx4 v25, s[40:43], s65 offen offset:1024 lds\n"
"v_mfma_f32_16x16x32_bf16 v[148:151], v[12:15], a[244:247], v[148:151]\n"
"v_mfma_f32_16x16x32_bf16 v[156:159], v[8:11], a[248:251], v[156:159]\n"
"buffer_load_dwordx4 v26, s[40:43], s65 offen offset:2048 lds\n"
"v_mfma_f32_16x16x32_bf16 v[156:159], v[12:15], a[252:255], v[156:159]\n"
"s_waitcnt lgkmcnt(3)\n"
"v_mfma_f32_16x16x32_bf16 v[160:163], v[0:3], a[208:211], v[160:163]\n"
"ds_read_b64_tr_b16 v[8:9], v35 offset:24584\n"
"v_mfma_f32_16x16x32_bf16 v[160:163], v[4:7], a[212:215], v[160:163]\n"
"ds_read_b64_tr_b16 v[10:11], v35 offset:24840\n"
"v_mfma_f32_16x16x32_bf16 v[168:171], v[0:3], a[216:219], v[168:171]\n"
"ds_read_b64_tr_b16 v[12:13], v35 offset:28680\n"
"v_mfma_f32_16x16x32_bf16 v[168:171], v[4:7], a[220:223], v[168:171]\n"
"ds_read_b64_tr_b16 v[14:15], v35 offset:28936\n"
"v_mfma_f32_16x16x32_bf16 v[176:179], v[0:3], a[224:227], v[176:179]\n"
"ds_read_b128 a[172:175], v34 offset:51712\n"
"v_mfma_f32_16x16x32_bf16 v[176:179], v[4:7], a[228:231], v[176:179]\n"
"ds_read_b128 a[176:179], v34 offset:53248\n"
"v_mfma_f32_16x16x32_bf16 v[184:187], v[0:3], a[232:235], v[184:187]\n"
"ds_read_b128 a[180:183], v34 offset:53760\n"
"v_mfma_f32_16x16x32_bf16 v[184:187], v[4:7], a[236:239], v[184:187]\n"
"v_mfma_f32_16x16x32_bf16 v[192:195], v[0:3], a[240:243], v[192:195]\n"
"buffer_load_dwordx4 v27, s[40:43], s65 offen offset:3072 lds\n"
"v_mfma_f32_16x16x32_bf16 v[192:195], v[4:7], a[244:247], v[192:195]\n"
"s_add_u32 s66, s80, 1152\n"
"s_add_u32 m0, s67, 27648\n"
"s_and_b32 s66, s66, 0x7ff\n"
"v_mfma_f32_16x16x32_bf16 v[200:203], v[0:3], a[248:251], v[200:203]\n"
"buffer_load_dwordx4 v31, s[36:39], s66 offen offset:0 lds\n"
"v_mfma_f32_16x16x32_bf16 v[200:203], v[4:7], a[252:255], v[200:203]\n"
"s_waitcnt lgkmcnt(3)\n"
"v_mfma_f32_16x16x32_bf16 v[164:167], v[8:11], a[208:211], v[164:167]\n"
"ds_read_b64_tr_b16 v[0:1], v35 offset:25088\n"
"v_mfma_f32_16x16x32_bf16 v[164:167], v[12:15], a[212:215], v[164:167]\n"
"ds_read_b64_tr_b16 v[2:3], v35 offset:25344\n"
"v_mfma_f32_16x16x32_bf16 v[172:175], v[8:11], a[216:219], v[172:175]\n"
"ds_read_b64_tr_b16 v[4:5], v35 offset:29184\n"
"v_mfma_f32_16x16x32_bf16 v[172:175], v[12:15], a[220:223], v[172:175]\n"
"ds_read_b64_tr_b16 v[6:7], v35 offset:29440\n"
"v_mfma_f32_16x16x32_bf16 v[180:183], v[8:11], a[224:227], v[180:183]\n"
"ds_read_b128 a[184:187], v34 offset:55296\n"
"v_mfma_f32_16x16x32_bf16 v[180:183], v[12:15], a[228:231], v[180:183]\n"
"ds_read_b128 a[188:191], v34 offset:55808\n"
"v_mfma_f32_16x16x32_bf16 v[188:191], v[8:11], a[232:235], v[188:191]\n"
"ds_read_b128 a[192:195], v34 offset:57344\n"
"v_mfma_f32_16x16x32_bf16 v[188:191], v[12:15], a[236:239], v[188:191]\n"
"buffer_load_dwordx4 v32, s[36:39], s66 offen offset:1024 lds\n"
"v_mfma_f32_16x16x32_bf16 v[196:199], v[8:11], a[240:243], v[196:199]\n"
"v_mfma_f32_16x16x32_bf16 v[196:199], v[12:15], a[244:247], v[196:199]\n"
"buffer_load_dwordx4 v33, s[36:39], s66 offen offset:2048 lds\n"
"v_mfma_f32_16x16x32_bf16 v[204:207], v[8:11], a[248:251], v[204:207]\n"
"v_mfma_f32_16x16x32_bf16 v[204:207], v[12:15], a[252:255], v[204:207]\n"
"s_waitcnt lgkmcnt(3)\n"
"v_mfma_f32_16x16x32_bf16 v[208:211], v[0:3], a[208:211], v[208:211]\n"
"ds_read_b64_tr_b16 v[8:9], v35 offset:25096\n"
"v_mfma_f32_16x16x32_bf16 v[208:211], v[4:7], a[212:215], v[208:211]\n"
"ds_read_b64_tr_b16 v[10:11], v35 offset:25352\n"
"v_mfma_f32_16x16x32_bf16 v[216:219], v[0:3], a[216:219], v[216:219]\n"
"ds_read_b64_tr_b16 v[12:13], v35 offset:29192\n"
"v_mfma_f32_16x16x32_bf16 v[216:219], v[4:7], a[220:223], v[216:219]\n"
"ds_read_b64_tr_b16 v[14:15], v35 offset:29448\n"
"v_mfma_f32_16x16x32_bf16 v[224:227], v[0:3], a[224:227], v[224:227]\n"
"ds_read_b128 a[196:199], v34 offset:57856\n"
"v_mfma_f32_16x16x32_bf16 v[224:227], v[4:7], a[228:231], v[224:227]\n"
"ds_read_b128 a[200:203], v34 offset:59392\n"
"v_mfma_f32_16x16x32_bf16 v[232:235], v[0:3], a[232:235], v[232:235]\n"
"ds_read_b128 a[204:207], v34 offset:59904\n"
"v_mfma_f32_16x16x32_bf16 v[232:235], v[4:7], a[236:239], v[232:235]\n"
"s_cmp_eq_u32 s94, 0\n"
"s_cselect_b64 vcc, -1, 0\n"
"s_add_u32 s56, s82, 16384\n"
"s_and_b32 s56, s56, 0x3ffff\n"
"s_cbranch_vccz K1_KS2_0_0\n"
"buffer_load_dwordx4 a[64:67], v44, s[48:51], s56 offen offset:0 nt\n"
"buffer_load_dwordx4 a[68:71], v45, s[48:51], s56 offen offset:1024 nt\n"
"buffer_load_dwordx4 a[72:75], v46, s[48:51], s56 offen offset:2048 nt\n"
"buffer_load_dwordx4 a[76:79], v47, s[48:51], s56 offen offset:3072 nt\n"
"K1_KS2_0_0:\n"
"v_mfma_f32_16x16x32_bf16 v[240:243], v[0:3], a[240:243], v[240:243]\n"
"v_mfma_f32_16x16x32_bf16 v[240:243], v[4:7], a[244:247], v[240:243]\n"
"s_add_u32 s56, s82, 20480\n"
"s_and_b32 s56, s56, 0x3ffff\n"
"s_cbranch_vccz K1_KS2_0_4\n"
"buffer_load_dwordx4 a[80:83], v44, s[48:51], s56 offen offset:0 nt\n"
"buffer_load_dwordx4 a[84:87], v45, s[48:51], s56 offen offset:1024 nt\n"
"buffer_load_dwordx4 a[88:91], v46, s[48:51], s56 offen offset:2048 nt\n"
"buffer_load_dwordx4 a[92:95], v47, s[48:51], s56 offen offset:3072 nt\n"
"K1_KS2_0_4:\n"
"v_mfma_f32_16x16x32_bf16 v[248:251], v[0:3], a[248:251], v[248:251]\n"
"v_mfma_f32_16x16x32_bf16 v[248:251], v[4:7], a[252:255], v[248:251]\n"
"s_waitcnt vmcnt(32) lgkmcnt(0)\n"
"s_barrier\n"
"v_mfma_f32_16x16x32_bf16 v[212:215], v[8:11], a[208:211], v[212:215]\n"
"ds_read_b64_tr_b16 v[0:1], v35 offset:32768\n"
"v_mfma_f32_16x16x32_bf16 v[212:215], v[12:15], a[212:215], v[212:215]\n"
"ds_read_b64_tr_b16 v[2:3], v35 offset:33024\n"
"v_mfma_f32_16x16x32_bf16 v[220:223], v[8:11], a[216:219], v[220:223]\n"
"ds_read_b64_tr_b16 v[4:5], v35 offset:36864\n"
"v_mfma_f32_16x16x32_bf16 v[220:223], v[12:15], a[220:223], v[220:223]\n"
"ds_read_b64_tr_b16 v[6:7], v35 offset:37120\n"
"v_mfma_f32_16x16x32_bf16 v[228:231], v[8:11], a[224:227], v[228:231]\n"
"v_mfma_f32_16x16x32_bf16 v[228:231], v[12:15], a[228:231], v[228:231]\n"
"s_add_u32 s65, s81, 4718592\n"
"v_mfma_f32_16x16x32_bf16 v[236:239], v[8:11], a[232:235], v[236:239]\n"
"s_add_u32 m0, s68, 24576\n"
"s_and_b32 s65, s65, 0x7fffff\n"
"v_mfma_f32_16x16x32_bf16 v[236:239], v[12:15], a[236:239], v[236:239]\n"
"buffer_load_dwordx4 v24, s[40:43], s65 offen offset:0 lds\n"
"v_mfma_f32_16x16x32_bf16 v[244:247], v[8:11], a[240:243], v[244:247]\n"
"v_mfma_f32_16x16x32_bf16 v[244:247], v[12:15], a[244:247], v[244:247]\n"
"buffer_load_dwordx4 v25, s[40:43], s65 offen offset:1024 lds\n"
"v_mfma_f32_16x16x32_bf16 v[252:255], v[8:11], a[248:251], v[252:255]\n"
"v_mfma_f32_16x16x32_bf16 v[252:255], v[12:15], a[252:255], v[252:255]\n"
"s_waitcnt lgkmcnt(0)\n"
"v_mfma_f32_16x16x32_bf16 v[64:67], v[0:3], a[160:163], v[64:67]\n"
"ds_read_b64_tr_b16 v[8:9], v35 offset:32776\n"
"v_mfma_f32_16x16x32_bf16 v[64:67], v[4:7], a[164:167], v[64:67]\n"
"ds_read_b64_tr_b16 v[10:11], v35 offset:33032\n"
"v_mfma_f32_16x16x32_bf16 v[72:75], v[0:3], a[168:171], v[72:75]\n"
"ds_read_b64_tr_b16 v[12:13], v35 offset:36872\n"
"v_mfma_f32_16x16x32_bf16 v[72:75], v[4:7], a[172:175], v[72:75]\n"
"ds_read_b64_tr_b16 v[14:15], v35 offset:37128\n"
"v_mfma_f32_16x16x32_bf16 v[80:83], v[0:3], a[176:179], v[80:83]\n"
"v_mfma_f32_16x16x32_bf16 v[80:83], v[4:7], a[180:183], v[80:83]\n"
"v_mfma_f32_16x16x32_bf16 v[88:91], v[0:3], a[184:187], v[88:91]\n"
"v_mfma_f32_16x16x32_bf16 v[88:91], v[4:7], a[188:191], v[88:91]\n"
"buffer_load_dwordx4 v26, s[40:43], s65 offen offset:2048 lds\n"
"v_mfma_f32_16x16x32_bf16 v[96:99], v[0:3], a[192:195], v[96:99]\n"
"v_mfma_f32_16x16x32_bf16 v[96:99], v[4:7], a[196:199], v[96:99]\n"
"buffer_load_dwordx4 v27, s[40:43], s65 offen offset:3072 lds\n"
"v_mfma_f32_16x16x32_bf16 v[104:107], v[0:3], a[200:203], v[104:107]\n"
"v_mfma_f32_16x16x32_bf16 v[104:107], v[4:7], a[204:207], v[104:107]\n"
"s_waitcnt lgkmcnt(0)\n"
"v_mfma_f32_16x16x32_bf16 v[68:71], v[8:11], a[160:163], v[68:71]\n"
"ds_read_b64_tr_b16 v[0:1], v35 offset:33280\n"
"v_mfma_f32_16x16x32_bf16 v[68:71], v[12:15], a[164:167], v[68:71]\n"
"ds_read_b64_tr_b16 v[2:3], v35 offset:33536\n"
"v_mfma_f32_16x16x32_bf16 v[76:79], v[8:11], a[168:171], v[76:79]\n"
"ds_read_b64_tr_b16 v[4:5], v35 offset:37376\n"
"v_mfma_f32_16x16x32_bf16 v[76:79], v[12:15], a[172:175], v[76:79]\n"
"ds_read_b64_tr_b16 v[6:7], v35 offset:37632\n"
"v_mfma_f32_16x16x32_bf16 v[84:87], v[8:11], a[176:179], v[84:87]\n"
"v_mfma_f32_16x16x32_bf16 v[84:87], v[12:15], a[180:183], v[84:87]\n"
"v_mfma_f32_16x16x32_bf16 v[92:95], v[8:11], a[184:187], v[92:95]\n"
"s_add_u32 s66, s80, 1280\n"
"v_mfma_f32_16x16x32_bf16 v[92:95], v[12:15], a[188:191], v[92:95]\n"
"s_add_u32 m0, s67, 49152\n"
"s_and_b32 s66, s66, 0x7ff\n"
"v_mfma_f32_16x16x32_bf16 v[100:103], v[8:11], a[192:195], v[100:103]\n"
"buffer_load_dwordx4 v28, s[36:39], s66 offen offset:0 lds\n"
"v_mfma_f32_16x16x32_bf16 v[100:103], v[12:15], a[196:199], v[100:103]\n"
"v_mfma_f32_16x16x32_bf16 v[108:111], v[8:11], a[200:203], v[108:111]\n"
"buffer_load_dwordx4 v29, s[36:39], s66 offen offset:1024 lds\n"
"v_mfma_f32_16x16x32_bf16 v[108:111], v[12:15], a[204:207], v[108:111]\n"
"s_waitcnt lgkmcnt(0)\n"
"v_mfma_f32_16x16x32_bf16 v[112:115], v[0:3], a[160:163], v[112:115]\n"
"ds_read_b64_tr_b16 v[8:9], v35 offset:33288\n"
"v_mfma_f32_16x16x32_bf16 v[112:115], v[4:7], a[164:167], v[112:115]\n"
"ds_read_b64_tr_b16 v[10:11], v35 offset:33544\n"
"v_mfma_f32_16x16x32_bf16 v[120:123], v[0:3], a[168:171], v[120:123]\n"
"ds_read_b64_tr_b16 v[12:13], v35 offset:37384\n"
"v_mfma_f32_16x16x32_bf16 v[120:123], v[4:7], a[172:175], v[120:123]\n"
"ds_read_b64_tr_b16 v[14:15], v35 offset:37640\n"
"v_mfma_f32_16x16x32_bf16 v[128:131], v[0:3], a[176:179], v[128:131]\n"
"v_mfma_f32_16x16x32_bf16 v[128:131], v[4:7], a[180:183], v[128:131]\n"
"v_mfma_f32_16x16x32_bf16 v[136:139], v[0:3], a[184:187], v[136:139]\n"
"v_mfma_f32_16x16x32_bf16 v[136:139], v[4:7], a[188:191], v[136:139]\n"
"v_mfma_f32_16x16x32_bf16 v[144:147], v[0:3], a[192:195], v[144:147]\n"
"buffer_load_dwordx4 v30, s[36:39], s66 offen offset:2048 lds\n"
"v_mfma_f32_16x16x32_bf16 v[144:147], v[4:7], a[196:199], v[144:147]\n"
"v_mfma_f32_16x16x32_bf16 v[152:155], v[0:3], a[200:203], v[152:155]\n"
"v_mfma_f32_16x16x32_bf16 v[152:155], v[4:7], a[204:207], v[152:155]\n"
"s_waitcnt vmcnt(29) lgkmcnt(0)\n"
"s_barrier\n"
"v_mfma_f32_16x16x32_bf16 v[116:119], v[8:11], a[160:163], v[116:119]\n"
"ds_read_b64_tr_b16 v[0:1], v35 offset:0\n"
"v_mfma_f32_16x16x32_bf16 v[116:119], v[12:15], a[164:167], v[116:119]\n"
"ds_read_b64_tr_b16 v[2:3], v35 offset:256\n"
"v_mfma_f32_16x16x32_bf16 v[124:127], v[8:11], a[168:171], v[124:127]\n"
"ds_read_b64_tr_b16 v[4:5], v35 offset:4096\n"
"v_mfma_f32_16x16x32_bf16 v[124:127], v[12:15], a[172:175], v[124:127]\n"
"ds_read_b64_tr_b16 v[6:7], v35 offset:4352\n"
"v_mfma_f32_16x16x32_bf16 v[132:135], v[8:11], a[176:179], v[132:135]\n"
"ds_read_b128 a[208:211], v34 offset:0\n"
"v_mfma_f32_16x16x32_bf16 v[132:135], v[12:15], a[180:183], v[132:135]\n"
"s_add_u32 s65, s81, 4718720\n"
"ds_read_b128 a[212:215], v34 offset:512\n"
"v_mfma_f32_16x16x32_bf16 v[140:143], v[8:11], a[184:187], v[140:143]\n"
"s_add_u32 m0, s68, 32768\n"
"s_and_b32 s65, s65, 0x7fffff\n"
"ds_read_b128 a[216:219], v34 offset:2048\n"
"v_mfma_f32_16x16x32_bf16 v[140:143], v[12:15], a[188:191], v[140:143]\n"
"buffer_load_dwordx4 v24, s[40:43], s65 offen offset:0 lds\n"
"v_mfma_f32_16x16x32_bf16 v[148:151], v[8:11], a[192:195], v[148:151]\n"
"v_mfma_f32_16x16x32_bf16 v[148:151], v[12:15], a[196:199], v[148:151]\n"
"buffer_load_dwordx4 v25, s[40:43], s65 offen offset:1024 lds\n"
"v_mfma_f32_16x16x32_bf16 v[156:159], v[8:11], a[200:203], v[156:159]\n"
"v_mfma_f32_16x16x32_bf16 v[156:159], v[12:15], a[204:207], v[156:159]\n"
"s_waitcnt lgkmcnt(3)\n"
"v_mfma_f32_16x16x32_bf16 v[160:163], v[0:3], a[160:163], v[160:163]\n"
"ds_read_b64_tr_b16 v[8:9], v35 offset:8\n"
"v_mfma_f32_16x16x32_bf16 v[160:163], v[4:7], a[164:167], v[160:163]\n"
"ds_read_b64_tr_b16 v[10:11], v35 offset:264\n"
"v_mfma_f32_16x16x32_bf16 v[168:171], v[0:3], a[168:171], v[168:171]\n"
"ds_read_b64_tr_b16 v[12:13], v35 offset:4104\n"
"v_mfma_f32_16x16x32_bf16 v[168:171], v[4:7], a[172:175], v[168:171]\n"
"ds_read_b64_tr_b16 v[14:15], v35 offset:4360\n"
"v_mfma_f32_16x16x32_bf16 v[176:179], v[0:3], a[176:179], v[176:179]\n"
"ds_read_b128 a[220:223], v34 offset:2560\n"
"v_mfma_f32_16x16x32_bf16 v[176:179], v[4:7], a[180:183], v[176:179]\n"
"ds_read_b128 a[224:227], v34 offset:4096\n"
"v_mfma_f32_16x16x32_bf16 v[184:187], v[0:3], a[184:187], v[184:187]\n"
"ds_read_b128 a[228:231], v34 offset:4608\n"
"v_mfma_f32_16x16x32_bf16 v[184:187], v[4:7], a[188:191], v[184:187]\n"
"buffer_load_dwordx4 v26, s[40:43], s65 offen offset:2048 lds\n"
"v_mfma_f32_16x16x32_bf16 v[192:195], v[0:3], a[192:195], v[192:195]\n"
"v_mfma_f32_16x16x32_bf16 v[192:195], v[4:7], a[196:199], v[192:195]\n"
"buffer_load_dwordx4 v27, s[40:43], s65 offen offset:3072 lds\n"
"v_mfma_f32_16x16x32_bf16 v[200:203], v[0:3], a[200:203], v[200:203]\n"
"v_mfma_f32_16x16x32_bf16 v[200:203], v[4:7], a[204:207], v[200:203]\n"
"s_waitcnt lgkmcnt(3)\n"
"v_mfma_f32_16x16x32_bf16 v[164:167], v[8:11], a[160:163], v[164:167]\n"
"ds_read_b64_tr_b16 v[0:1], v35 offset:512\n"
"v_mfma_f32_16x16x32_bf16 v[164:167], v[12:15], a[164:167], v[164:167]\n"
"ds_read_b64_tr_b16 v[2:3], v35 offset:768\n"
"v_mfma_f32_16x16x32_bf16 v[172:175], v[8:11], a[168:171], v[172:175]\n"
"ds_read_b64_tr_b16 v[4:5], v35 offset:4608\n"
"v_mfma_f32_16x16x32_bf16 v[172:175], v[12:15], a[172:175], v[172:175]\n"
"ds_read_b64_tr_b16 v[6:7], v35 offset:4864\n"
"v_mfma_f32_16x16x32_bf16 v[180:183], v[8:11], a[176:179], v[180:183]\n"
"ds_read_b128 a[232:235], v34 offset:6144\n"
"v_mfma_f32_16x16x32_bf16 v[180:183], v[12:15], a[180:183], v[180:183]\n"
"ds_read_b128 a[236:239], v34 offset:6656\n"
"v_mfma_f32_16x16x32_bf16 v[188:191], v[8:11], a[184:187], v[188:191]\n"
"s_add_u32 s66, s80, 1280\n"
"ds_read_b128 a[240:243], v34 offset:8192\n"
"v_mfma_f32_16x16x32_bf16 v[188:191], v[12:15], a[188:191], v[188:191]\n"
"s_add_u32 m0, s67, 52224\n"
"s_and_b32 s66, s66, 0x7ff\n"
"v_mfma_f32_16x16x32_bf16 v[196:199], v[8:11], a[192:195], v[196:199]\n"
"buffer_load_dwordx4 v31, s[36:39], s66 offen offset:0 lds\n"
"v_mfma_f32_16x16x32_bf16 v[196:199], v[12:15], a[196:199], v[196:199]\n"
"v_mfma_f32_16x16x32_bf16 v[204:207], v[8:11], a[200:203], v[204:207]\n"
"buffer_load_dwordx4 v32, s[36:39], s66 offen offset:1024 lds\n"
"v_mfma_f32_16x16x32_bf16 v[204:207], v[12:15], a[204:207], v[204:207]\n"
"s_waitcnt lgkmcnt(3)\n"
"v_mfma_f32_16x16x32_bf16 v[208:211], v[0:3], a[160:163], v[208:211]\n"
"ds_read_b64_tr_b16 v[8:9], v35 offset:520\n"
"v_mfma_f32_16x16x32_bf16 v[208:211], v[4:7], a[164:167], v[208:211]\n"
"ds_read_b64_tr_b16 v[10:11], v35 offset:776\n"
"v_mfma_f32_16x16x32_bf16 v[216:219], v[0:3], a[168:171], v[216:219]\n"
"ds_read_b64_tr_b16 v[12:13], v35 offset:4616\n"
"v_mfma_f32_16x16x32_bf16 v[216:219], v[4:7], a[172:175], v[216:219]\n"
"ds_read_b64_tr_b16 v[14:15], v35 offset:4872\n"
"v_mfma_f32_16x16x32_bf16 v[224:227], v[0:3], a[176:179], v[224:227]\n"
"ds_read_b128 a[244:247], v34 offset:8704\n"
"v_mfma_f32_16x16x32_bf16 v[224:227], v[4:7], a[180:183], v[224:227]\n"
"ds_read_b128 a[248:251], v34 offset:10240\n"
"v_mfma_f32_16x16x32_bf16 v[232:235], v[0:3], a[184:187], v[232:235]\n"
"ds_read_b128 a[252:255], v34 offset:10752\n"
"v_mfma_f32_16x16x32_bf16 v[232:235], v[4:7], a[188:191], v[232:235]\n"
"v_mfma_f32_16x16x32_bf16 v[240:243], v[0:3], a[192:195], v[240:243]\n"
"buffer_load_dwordx4 v33, s[36:39], s66 offen offset:2048 lds\n"
"v_mfma_f32_16x16x32_bf16 v[240:243], v[4:7], a[196:199], v[240:243]\n"
"v_mfma_f32_16x16x32_bf16 v[248:251], v[0:3], a[200:203], v[248:251]\n"
"v_mfma_f32_16x16x32_bf16 v[248:251], v[4:7], a[204:207], v[248:251]\n"
"s_bitcmp1_b32 2, s94\n"
"s_cbranch_scc0 K1_W7\n"
"s_waitcnt vmcnt(24)\n"
"K1_W7:\n"
"s_waitcnt vmcnt(32) lgkmcnt(0)\n"
"s_barrier\n"
"v_mfma_f32_16x16x32_bf16 v[212:215], v[8:11], a[160:163], v[212:215]\n"
"ds_read_b64_tr_b16 v[0:1], v35 offset:8192\n"
"v_mfma_f32_16x16x32_bf16 v[212:215], v[12:15], a[164:167], v[212:215]\n"
"ds_read_b64_tr_b16 v[2:3], v35 offset:8448\n"
"v_mfma_f32_16x16x32_bf16 v[220:223], v[8:11], a[168:171], v[220:223]\n"
"ds_read_b64_tr_b16 v[4:5], v35 offset:12288\n"
"v_mfma_f32_16x16x32_bf16 v[220:223], v[12:15], a[172:175], v[220:223]\n"
"ds_read_b64_tr_b16 v[6:7], v35 offset:12544\n"
"v_mfma_f32_16x16x32_bf16 v[228:231], v[8:11], a[176:179], v[228:231]\n"
"v_mfma_f32_16x16x32_bf16 v[228:231], v[12:15], a[180:183], v[228:231]\n"
"s_add_u32 s65, s81, 5242880\n"
"v_mfma_f32_16x16x32_bf16 v[236:239], v[8:11], a[184:187], v[236:239]\n"
"s_add_u32 m0, s68, 0\n"
"s_and_b32 s65, s65, 0x7fffff\n"
"v_mfma_f32_16x16x32_bf16 v[236:239], v[12:15], a[188:191], v[236:239]\n"
"buffer_load_dwordx4 v24, s[40:43], s65 offen offset:0 lds\n"
"v_mfma_f32_16x16x32_bf16 v[244:247], v[8:11], a[192:195], v[244:247]\n"
"buffer_load_dwordx4 v25, s[40:43], s65 offen offset:1024 lds\n"
"v_mfma_f32_16x16x32_bf16 v[244:247], v[12:15], a[196:199], v[244:247]\n"
"v_mfma_f32_16x16x32_bf16 v[252:255], v[8:11], a[200:203], v[252:255]\n"
"buffer_load_dwordx4 v26, s[40:43], s65 offen offset:2048 lds\n"
"v_mfma_f32_16x16x32_bf16 v[252:255], v[12:15], a[204:207], v[252:255]\n"
"s_waitcnt lgkmcnt(0)\n"
"v_mfma_f32_16x16x32_bf16 v[64:67], v[0:3], a[208:211], v[64:67]\n"
"ds_read_b64_tr_b16 v[8:9], v35 offset:8200\n"
"v_mfma_f32_16x16x32_bf16 v[64:67], v[4:7], a[212:215], v[64:67]\n"
"ds_read_b64_tr_b16 v[10:11], v35 offset:8456\n"
"v_mfma_f32_16x16x32_bf16 v[72:75], v[0:3], a[216:219], v[72:75]\n"
"ds_read_b64_tr_b16 v[12:13], v35 offset:12296\n"
"v_mfma_f32_16x16x32_bf16 v[72:75], v[4:7], a[220:223], v[72:75]\n"
"ds_read_b64_tr_b16 v[14:15], v35 offset:12552\n"
"v_mfma_f32_16x16x32_bf16 v[80:83], v[0:3], a[224:227], v[80:83]\n"
"v_mfma_f32_16x16x32_bf16 v[80:83], v[4:7], a[228:231], v[80:83]\n"
"v_mfma_f32_16x16x32_bf16 v[88:91], v[0:3], a[232:235], v[88:91]\n"
"v_mfma_f32_16x16x32_bf16 v[88:91], v[4:7], a[236:239], v[88:91]\n"
"v_mfma_f32_16x16x32_bf16 v[96:99], v[0:3], a[240:243], v[96:99]\n"
"buffer_load_dwordx4 v27, s[40:43], s65 offen offset:3072 lds\n"
"v_mfma_f32_16x16x32_bf16 v[96:99], v[4:7], a[244:247], v[96:99]\n"
"s_add_u32 s66, s80, 1408\n"
"s_add_u32 m0, s67, 0\n"
"s_and_b32 s66, s66, 0x7ff\n"
"v_mfma_f32_16x16x32_bf16 v[104:107], v[0:3], a[248:251], v[104:107]\n"
"buffer_load_dwordx4 v28, s[36:39], s66 offen offset:0 lds\n"
"v_mfma_f32_16x16x32_bf16 v[104:107], v[4:7], a[252:255], v[104:107]\n"
"s_waitcnt lgkmcnt(0)\n"
"v_mfma_f32_16x16x32_bf16 v[68:71], v[8:11], a[208:211], v[68:71]\n"
"ds_read_b64_tr_b16 v[0:1], v35 offset:8704\n"
"v_mfma_f32_16x16x32_bf16 v[68:71], v[12:15], a[212:215], v[68:71]\n"
"ds_read_b64_tr_b16 v[2:3], v35 offset:8960\n"
"v_mfma_f32_16x16x32_bf16 v[76:79], v[8:11], a[216:219], v[76:79]\n"
"ds_read_b64_tr_b16 v[4:5], v35 offset:12800\n"
"v_mfma_f32_16x16x32_bf16 v[76:79], v[12:15], a[220:223], v[76:79]\n"
"ds_read_b64_tr_b16 v[6:7], v35 offset:13056\n"
"v_mfma_f32_16x16x32_bf16 v[84:87], v[8:11], a[224:227], v[84:87]\n"
"v_mfma_f32_16x16x32_bf16 v[84:87], v[12:15], a[228:231], v[84:87]\n"
"v_mfma_f32_16x16x32_bf16 v[92:95], v[8:11], a[232:235], v[92:95]\n"
"v_mfma_f32_16x16x32_bf16 v[92:95], v[12:15], a[236:239], v[92:95]\n"
"buffer_load_dwordx4 v29, s[36:39], s66 offen offset:1024 lds\n"
"v_mfma_f32_16x16x32_bf16 v[100:103], v[8:11], a[240:243], v[100:103]\n"
"v_mfma_f32_16x16x32_bf16 v[100:103], v[12:15], a[244:247], v[100:103]\n"
"buffer_load_dwordx4 v30, s[36:39], s66 offen offset:2048 lds\n"
"v_mfma_f32_16x16x32_bf16 v[108:111], v[8:11], a[248:251], v[108:111]\n"
"v_mfma_f32_16x16x32_bf16 v[108:111], v[12:15], a[252:255], v[108:111]\n"
"s_waitcnt lgkmcnt(0)\n"
"v_mfma_f32_16x16x32_bf16 v[112:115], v[0:3], a[208:211], v[112:115]\n"
"ds_read_b64_tr_b16 v[8:9], v35 offset:8712\n"
"v_mfma_f32_16x16x32_bf16 v[112:115], v[4:7], a[212:215], v[112:115]\n"
"ds_read_b64_tr_b16 v[10:11], v35 offset:8968\n"
"v_mfma_f32_16x16x32_bf16 v[120:123], v[0:3], a[216:219], v[120:123]\n"
"ds_read_b64_tr_b16 v[12:13], v35 offset:12808\n"
"v_mfma_f32_16x16x32_bf16 v[120:123], v[4:7], a[220:223], v[120:123]\n"
"ds_read_b64_tr_b16 v[14:15], v35 offset:13064\n"
"v_mfma_f32_16x16x32_bf16 v[128:131], v[0:3], a[224:227], v[128:131]\n"
"v_mfma_f32_16x16x32_bf16 v[128:131], v[4:7], a[228:231], v[128:131]\n"
"v_mfma_f32_16x16x32_bf16 v[136:139], v[0:3], a[232:235], v[136:139]\n"
"v_mfma_f32_16x16x32_bf16 v[136:139], v[4:7], a[236:239], v[136:139]\n"
"s_cmp_eq_u32 s94, 1\n"
"s_cselect_b64 vcc, -1, 0\n"
"s_add_u32 s56, s82, 16384\n"
"s_and_b32 s56, s56, 0x3ffff\n"
"s_cbranch_vccz K1_KS2_1_0\n"
"buffer_load_dwordx4 a[64:67], v44, s[48:51], s56 offen offset:0 nt\n"
"buffer_load_dwordx4 a[68:71], v45, s[48:51], s56 offen offset:1024 nt\n"
"buffer_load_dwordx4 a[72:75], v46, s[48:51], s56 offen offset:2048 nt\n"
"buffer_load_dwordx4 a[76:79], v47, s[48:51], s56 offen offset:3072 nt\n"
"K1_KS2_1_0:\n"
"v_mfma_f32_16x16x32_bf16 v[144:147], v[0:3], a[240:243], v[144:147]\n"
"v_mfma_f32_16x16x32_bf16 v[144:147], v[4:7], a[244:247], v[144:147]\n"
"s_add_u32 s56, s82, 20480\n"
"s_and_b32 s56, s56, 0x3ffff\n"
"s_cbranch_vccz K1_KS2_1_4\n"
"buffer_load_dwordx4 a[80:83], v44, s[48:51], s56 offen offset:0 nt\n"
"buffer_load_dwordx4 a[84:87], v45, s[48:51], s56 offen offset:1024 nt\n"
"buffer_load_dwordx4 a[88:91], v46, s[48:51], s56 offen offset:2048 nt\n"
"buffer_load_dwordx4 a[92:95], v47, s[48:51], s56 offen offset:3072 nt\n"
"K1_KS2_1_4:\n"
"v_mfma_f32_16x16x32_bf16 v[152:155], v[0:3], a[248:251], v[152:155]\n"
"v_mfma_f32_16x16x32_bf16 v[152:155], v[4:7], a[252:255], v[152:155]\n"
"s_waitcnt vmcnt(29) lgkmcnt(0)\n"
"s_barrier\n"
"v_mfma_f32_16x16x32_bf16 v[116:119], v[8:11], a[208:211], v[116:119]\n"
"ds_read_b64_tr_b16 v[0:1], v35 offset:16384\n"
"v_mfma_f32_16x16x32_bf16 v[116:119], v[12:15], a[212:215], v[116:119]\n"
"ds_read_b64_tr_b16 v[2:3], v35 offset:16640\n"
"v_mfma_f32_16x16x32_bf16 v[124:127], v[8:11], a[216:219], v[124:127]\n"
"ds_read_b64_tr_b16 v[4:5], v35 offset:20480\n"
"v_mfma_f32_16x16x32_bf16 v[124:127], v[12:15], a[220:223], v[124:127]\n"
"ds_read_b64_tr_b16 v[6:7], v35 offset:20736\n"
"v_mfma_f32_16x16x32_bf16 v[132:135], v[8:11], a[224:227], v[132:135]\n"
"ds_read_b128 a[160:163], v34 offset:24576\n"
"v_mfma_f32_16x16x32_bf16 v[132:135], v[12:15], a[228:231], v[132:135]\n"
"s_add_u32 s65, s81, 5243008\n"
"ds_read_b128 a[164:167], v34 offset:25088\n"
"v_mfma_f32_16x16x32_bf16 v[140:143], v[8:11], a[232:235], v[140:143]\n"
"s_add_u32 m0, s68, 8192\n"
"s_and_b32 s65, s65, 0x7fffff\n"
"ds_read_b128 a[168:171], v34 offset:26624\n"
"v_mfma_f32_16x16x32_bf16 v[140:143], v[12:15], a[236:239], v[140:143]\n"
"buffer_load_dwordx4 v24, s[40:43], s65 offen offset:0 lds\n"
"v_mfma_f32_16x16x32_bf16 v[148:151], v[8:11], a[240:243], v[148:151]\n"
"v_mfma_f32_16x16x32_bf16 v[148:151], v[12:15], a[244:247], v[148:151]\n"
"buffer_load_dwordx4 v25, s[40:43], s65 offen offset:1024 lds\n"
"v_mfma_f32_16x16x32_bf16 v[156:159], v[8:11], a[248:251], v[156:159]\n"
"v_mfma_f32_16x16x32_bf16 v[156:159], v[12:15], a[252:255], v[156:159]\n"
"s_waitcnt lgkmcnt(3)\n"
"v_mfma_f32_16x16x32_bf16 v[160:163], v[0:3], a[208:211], v[160:163]\n"
"ds_read_b64_tr_b16 v[8:9], v35 offset:16392\n"
"v_mfma_f32_16x16x32_bf16 v[160:163], v[4:7], a[212:215], v[160:163]\n"
"ds_read_b64_tr_b16 v[10:11], v35 offset:16648\n"
"v_mfma_f32_16x16x32_bf16 v[168:171], v[0:3], a[216:219], v[168:171]\n"
"ds_read_b64_tr_b16 v[12:13], v35 offset:20488\n"
"v_mfma_f32_16x16x32_bf16 v[168:171], v[4:7], a[220:223], v[168:171]\n"
"ds_read_b64_tr_b16 v[14:15], v35 offset:20744\n"
"v_mfma_f32_16x16x32_bf16 v[176:179], v[0:3], a[224:227], v[176:179]\n"
"ds_read_b128 a[172:175], v34 offset:27136\n"
"v_mfma_f32_16x16x32_bf16 v[176:179], v[4:7], a[228:231], v[176:179]\n"
"ds_read_b128 a[176:179], v34 offset:28672\n"
"v_mfma_f32_16x16x32_bf16 v[184:187], v[0:3], a[232:235], v[184:187]\n"
"ds_read_b128 a[180:183], v34 offset:29184\n"
"v_mfma_f32_16x16x32_bf16 v[184:187], v[4:7], a[236:239], v[184:187]\n"
"buffer_load_dwordx4 v26, s[40:43], s65 offen offset:2048 lds\n"
"v_mfma_f32_16x16x32_bf16 v[192:195], v[0:3], a[240:243], v[192:195]\n"
"v_mfma_f32_16x16x32_bf16 v[192:195], v[4:7], a[244:247], v[192:195]\n"
"buffer_load_dwordx4 v27, s[40:43], s65 offen offset:3072 lds\n"
"v_mfma_f32_16x16x32_bf16 v[200:203], v[0:3], a[248:251], v[200:203]\n"
"v_mfma_f32_16x16x32_bf16 v[200:203], v[4:7], a[252:255], v[200:203]\n"
"s_waitcnt lgkmcnt(3)\n"
"v_mfma_f32_16x16x32_bf16 v[164:167], v[8:11], a[208:211], v[164:167]\n"
"ds_read_b64_tr_b16 v[0:1], v35 offset:16896\n"
"v_mfma_f32_16x16x32_bf16 v[164:167], v[12:15], a[212:215], v[164:167]\n"
"ds_read_b64_tr_b16 v[2:3], v35 offset:17152\n"
"v_mfma_f32_16x16x32_bf16 v[172:175], v[8:11], a[216:219], v[172:175]\n"
"ds_read_b64_tr_b16 v[4:5], v35 offset:20992\n"
"v_mfma_f32_16x16x32_bf16 v[172:175], v[12:15], a[220:223], v[172:175]\n"
"ds_read_b64_tr_b16 v[6:7], v35 offset:21248\n"
"v_mfma_f32_16x16x32_bf16 v[180:183], v[8:11], a[224:227], v[180:183]\n"
"ds_read_b128 a[184:187], v34 offset:30720\n"
"v_mfma_f32_16x16x32_bf16 v[180:183], v[12:15], a[228:231], v[180:183]\n"
"ds_read_b128 a[188:191], v34 offset:31232\n"
"v_mfma_f32_16x16x32_bf16 v[188:191], v[8:11], a[232:235], v[188:191]\n"
"s_add_u32 s66, s80, 1408\n"
"ds_read_b128 a[192:195], v34 offset:32768\n"
"v_mfma_f32_16x16x32_bf16 v[188:191], v[12:15], a[236:239], v[188:191]\n"
"s_add_u32 m0, s67, 3072\n"
"s_and_b32 s66, s66, 0x7ff\n"
"v_mfma_f32_16x16x32_bf16 v[196:199], v[8:11], a[240:243], v[196:199]\n"
"buffer_load_dwordx4 v31, s[36:39], s66 offen offset:0 lds\n"
"v_mfma_f32_16x16x32_bf16 v[196:199], v[12:15], a[244:247], v[196:199]\n"
"v_mfma_f32_16x16x32_bf16 v[204:207], v[8:11], a[248:251], v[204:207]\n"
"buffer_load_dwordx4 v32, s[36:39], s66 offen offset:1024 lds\n"
"v_mfma_f32_16x16x32_bf16 v[204:207], v[12:15], a[252:255], v[204:207]\n"
"s_waitcnt lgkmcnt(3)\n"
"v_mfma_f32_16x16x32_bf16 v[208:211], v[0:3], a[208:211], v[208:211]\n"
"ds_read_b64_tr_b16 v[8:9], v35 offset:16904\n"
"v_mfma_f32_16x16x32_bf16 v[208:211], v[4:7], a[212:215], v[208:211]\n"
"ds_read_b64_tr_b16 v[10:11], v35 offset:17160\n"
"v_mfma_f32_16x16x32_bf16 v[216:219], v[0:3], a[216:219], v[216:219]\n"
"ds_read_b64_tr_b16 v[12:13], v35 offset:21000\n"
"v_mfma_f32_16x16x32_bf16 v[216:219], v[4:7], a[220:223], v[216:219]\n"
"ds_read_b64_tr_b16 v[14:15], v35 offset:21256\n"
"v_mfma_f32_16x16x32_bf16 v[224:227], v[0:3], a[224:227], v[224:227]\n"
"ds_read_b128 a[196:199], v34 offset:33280\n"
"v_mfma_f32_16x16x32_bf16 v[224:227], v[4:7], a[228:231], v[224:227]\n"
"ds_read_b128 a[200:203], v34 offset:34816\n"
"v_mfma_f32_16x16x32_bf16 v[232:235], v[0:3], a[232:235], v[232:235]\n"
"ds_read_b128 a[204:207], v34 offset:35328\n"
"v_mfma_f32_16x16x32_bf16 v[232:235], v[4:7], a[236:239], v[232:235]\n"
"v_mfma_f32_16x16x32_bf16 v[240:243], v[0:3], a[240:243], v[240:243]\n"
"buffer_load_dwordx4 v33, s[36:39], s66 offen offset:2048 lds\n"
"v_mfma_f32_16x16x32_bf16 v[240:243], v[4:7], a[244:247], v[240:243]\n"
"v_mfma_f32_16x16x32_bf16 v[248:251], v[0:3], a[248:251], v[248:251]\n"
"v_mfma_f32_16x16x32_bf16 v[248:251], v[4:7], a[252:255], v[248:251]\n"
"s_bitcmp1_b32 1, s94\n"
"s_cbranch_scc0 K1_W8\n"
"s_waitcnt vmcnt(24)\n"
"K1_W8:\n"
"s_waitcnt vmcnt(32) lgkmcnt(0)\n"
"s_barrier\n"
"v_mfma_f32_16x16x32_bf16 v[212:215], v[8:11], a[208:211], v[212:215]\n"
"ds_read_b64_tr_b16 v[0:1], v35 offset:24576\n"
"v_mfma_f32_16x16x32_bf16 v[212:215], v[12:15], a[212:215], v[212:215]\n"
"ds_read_b64_tr_b16 v[2:3], v35 offset:24832\n"
"v_mfma_f32_16x16x32_bf16 v[220:223], v[8:11], a[216:219], v[220:223]\n"
"ds_read_b64_tr_b16 v[4:5], v35 offset:28672\n"
"v_mfma_f32_16x16x32_bf16 v[220:223], v[12:15], a[220:223], v[220:223]\n"
"ds_read_b64_tr_b16 v[6:7], v35 offset:28928\n"
"v_mfma_f32_16x16x32_bf16 v[228:231], v[8:11], a[224:227], v[228:231]\n"
"v_mfma_f32_16x16x32_bf16 v[228:231], v[12:15], a[228:231], v[228:231]\n"
"s_add_u32 s65, s81, 5767168\n"
"v_mfma_f32_16x16x32_bf16 v[236:239], v[8:11], a[232:235], v[236:239]\n"
"s_add_u32 m0, s68, 16384\n"
"s_and_b32 s65, s65, 0x7fffff\n"
"v_mfma_f32_16x16x32_bf16 v[236:239], v[12:15], a[236:239], v[236:239]\n"
"buffer_load_dwordx4 v24, s[40:43], s65 offen offset:0 lds\n"
"v_mfma_f32_16x16x32_bf16 v[244:247], v[8:11], a[240:243], v[244:247]\n"
"buffer_load_dwordx4 v25, s[40:43], s65 offen offset:1024 lds\n"
"v_mfma_f32_16x16x32_bf16 v[244:247], v[12:15], a[244:247], v[244:247]\n"
"v_mfma_f32_16x16x32_bf16 v[252:255], v[8:11], a[248:251], v[252:255]\n"
"buffer_load_dwordx4 v26, s[40:43], s65 offen offset:2048 lds\n"
"v_mfma_f32_16x16x32_bf16 v[252:255], v[12:15], a[252:255], v[252:255]\n"
"s_waitcnt lgkmcnt(0)\n"
"v_mfma_f32_16x16x32_bf16 v[64:67], v[0:3], a[160:163], v[64:67]\n"
"ds_read_b64_tr_b16 v[8:9], v35 offset:24584\n"
"v_mfma_f32_16x16x32_bf16 v[64:67], v[4:7], a[164:167], v[64:67]\n"
"ds_read_b64_tr_b16 v[10:11], v35 offset:24840\n"
"v_mfma_f32_16x16x32_bf16 v[72:75], v[0:3], a[168:171], v[72:75]\n"
"ds_read_b64_tr_b16 v[12:13], v35 offset:28680\n"
"v_mfma_f32_16x16x32_bf16 v[72:75], v[4:7], a[172:175], v[72:75]\n"
"ds_read_b64_tr_b16 v[14:15], v35 offset:28936\n"
"v_mfma_f32_16x16x32_bf16 v[80:83], v[0:3], a[176:179], v[80:83]\n"
"v_mfma_f32_16x16x32_bf16 v[80:83], v[4:7], a[180:183], v[80:83]\n"
"v_mfma_f32_16x16x32_bf16 v[88:91], v[0:3], a[184:187], v[88:91]\n"
"v_mfma_f32_16x16x32_bf16 v[88:91], v[4:7], a[188:191], v[88:91]\n"
"v_mfma_f32_16x16x32_bf16 v[96:99], v[0:3], a[192:195], v[96:99]\n"
"buffer_load_dwordx4 v27, s[40:43], s65 offen offset:3072 lds\n"
"v_mfma_f32_16x16x32_bf16 v[96:99], v[4:7], a[196:199], v[96:99]\n"
"s_add_u32 s66, s80, 1536\n"
"s_add_u32 m0, s67, 24576\n"
"s_and_b32 s66, s66, 0x7ff\n"
"v_mfma_f32_16x16x32_bf16 v[104:107], v[0:3], a[200:203], v[104:107]\n"
"buffer_load_dwordx4 v28, s[36:39], s66 offen offset:0 lds\n"
"v_mfma_f32_16x16x32_bf16 v[104:107], v[4:7], a[204:207], v[104:107]\n"
"s_waitcnt lgkmcnt(0)\n"
"v_mfma_f32_16x16x32_bf16 v[68:71], v[8:11], a[160:163], v[68:71]\n"
"ds_read_b64_tr_b16 v[0:1], v35 offset:25088\n"
"v_mfma_f32_16x16x32_bf16 v[68:71], v[12:15], a[164:167], v[68:71]\n"
"ds_read_b64_tr_b16 v[2:3], v35 offset:25344\n"
"v_mfma_f32_16x16x32_bf16 v[76:79], v[8:11], a[168:171], v[76:79]\n"
"ds_read_b64_tr_b16 v[4:5], v35 offset:29184\n"
"v_mfma_f32_16x16x32_bf16 v[76:79], v[12:15], a[172:175], v[76:79]\n"
"ds_read_b64_tr_b16 v[6:7], v35 offset:29440\n"
"v_mfma_f32_16x16x32_bf16 v[84:87], v[8:11], a[176:179], v[84:87]\n"
"v_mfma_f32_16x16x32_bf16 v[84:87], v[12:15], a[180:183], v[84:87]\n"
"v_mfma_f32_16x16x32_bf16 v[92:95], v[8:11], a[184:187], v[92:95]\n"
"v_mfma_f32_16x16x32_bf16 v[92:95], v[12:15], a[188:191], v[92:95]\n"
"buffer_load_dwordx4 v29, s[36:39], s66 offen offset:1024 lds\n"
"v_mfma_f32_16x16x32_bf16 v[100:103], v[8:11], a[192:195], v[100:103]\n"
"v_mfma_f32_16x16x32_bf16 v[100:103], v[12:15], a[196:199], v[100:103]\n"
"buffer_load_dwordx4 v30, s[36:39], s66 offen offset:2048 lds\n"
"v_mfma_f32_16x16x32_bf16 v[108:111], v[8:11], a[200:203], v[108:111]\n"
"v_mfma_f32_16x16x32_bf16 v[108:111], v[12:15], a[204:207], v[108:111]\n"
"s_waitcnt lgkmcnt(0)\n"
"v_mfma_f32_16x16x32_bf16 v[112:115], v[0:3], a[160:163], v[112:115]\n"
"ds_read_b64_tr_b16 v[8:9], v35 offset:25096\n"
"v_mfma_f32_16x16x32_bf16 v[112:115], v[4:7], a[164:167], v[112:115]\n"
"ds_read_b64_tr_b16 v[10:11], v35 offset:25352\n"
"v_mfma_f32_16x16x32_bf16 v[120:123], v[0:3], a[168:171], v[120:123]\n"
"ds_read_b64_tr_b16 v[12:13], v35 offset:29192\n"
"v_mfma_f32_16x16x32_bf16 v[120:123], v[4:7], a[172:175], v[120:123]\n"
"ds_read_b64_tr_b16 v[14:15], v35 offset:29448\n"
"v_mfma_f32_16x16x32_bf16 v[128:131], v[0:3], a[176:179], v[128:131]\n"
"v_mfma_f32_16x16x32_bf16 v[128:131], v[4:7], a[180:183], v[128:131]\n"
"v_mfma_f32_16x16x32_bf16 v[136:139], v[0:3], a[184:187], v[136:139]\n"
"v_mfma_f32_16x16x32_bf16 v[136:139], v[4:7], a[188:191], v[136:139]\n"
"s_cmp_eq_u32 s94, 0\n"
"s_cselect_b64 vcc, -1, 0\n"
"s_add_u32 s56, s82, 24576\n"
"s_and_b32 s56, s56, 0x3ffff\n"
"s_cbranch_vccz K1_KS3_0_0\n"
"buffer_load_dwordx4 a[96:99], v44, s[48:51], s56 offen offset:0\n"
"buffer_load_dwordx4 a[100:103], v45, s[48:51], s56 offen offset:1024\n"
"buffer_load_dwordx4 a[104:107], v46, s[48:51], s56 offen offset:2048\n"
"buffer_load_dwordx4 a[108:111], v47, s[48:51], s56 offen offset:3072\n"
"K1_KS3_0_0:\n"
"v_mfma_f32_16x16x32_bf16 v[144:147], v[0:3], a[192:195], v[144:147]\n"
"v_mfma_f32_16x16x32_bf16 v[144:147], v[4:7], a[196:199], v[144:147]\n"
"s_add_u32 s56, s82, 28672\n"
"s_and_b32 s56, s56, 0x3ffff\n"
"s_cbranch_vccz K1_KS3_0_4\n"
"buffer_load_dwordx4 a[112:115], v44, s[48:51], s56 offen offset:0\n"
"buffer_load_dwordx4 a[116:119], v45, s[48:51], s56 offen offset:1024\n"
"buffer_load_dwordx4 a[120:123], v46, s[48:51], s56 offen offset:2048\n"
"buffer_load_dwordx4 a[124:127], v47, s[48:51], s56 offen offset:3072\n"
"K1_KS3_0_4:\n"
"v_mfma_f32_16x16x32_bf16 v[152:155], v[0:3], a[200:203], v[152:155]\n"
"v_mfma_f32_16x16x32_bf16 v[152:155], v[4:7], a[204:207], v[152:155]\n"
"s_waitcnt vmcnt(29) lgkmcnt(0)\n"
"s_barrier\n"
"v_mfma_f32_16x16x32_bf16 v[116:119], v[8:11], a[160:163], v[116:119]\n"
"ds_read_b64_tr_b16 v[0:1], v35 offset:32768\n"
"v_mfma_f32_16x16x32_bf16 v[116:119], v[12:15], a[164:167], v[116:119]\n"
"ds_read_b64_tr_b16 v[2:3], v35 offset:33024\n"
"v_mfma_f32_16x16x32_bf16 v[124:127], v[8:11], a[168:171], v[124:127]\n"
"ds_read_b64_tr_b16 v[4:5], v35 offset:36864\n"
"v_mfma_f32_16x16x32_bf16 v[124:127], v[12:15], a[172:175], v[124:127]\n"
"ds_read_b64_tr_b16 v[6:7], v35 offset:37120\n"
"v_mfma_f32_16x16x32_bf16 v[132:135], v[8:11], a[176:179], v[132:135]\n"
"ds_read_b128 a[208:211], v34 offset:49152\n"
"v_mfma_f32_16x16x32_bf16 v[132:135], v[12:15], a[180:183], v[132:135]\n"
"s_add_u32 s65, s81, 5767296\n"
"ds_read_b128 a[212:215], v34 offset:49664\n"
"v_mfma_f32_16x16x32_bf16 v[140:143], v[8:11], a[184:187], v[140:143]\n"
"s_add_u32 m0, s68, 24576\n"
"s_and_b32 s65, s65, 0x7fffff\n"
"ds_read_b128 a[216:219], v34 offset:51200\n"
"v_mfma_f32_16x16x32_bf16 v[140:143], v[12:15], a[188:191], v[140:143]\n"
"buffer_load_dwordx4 v24, s[40:43], s65 offen offset:0 lds\n"
"v_mfma_f32_16x16x32_bf16 v[148:151], v[8:11], a[192:195], v[148:151]\n"
"v_mfma_f32_16x16x32_bf16 v[148:151], v[12:15], a[196:199], v[148:151]\n"
"buffer_load_dwordx4 v25, s[40:43], s65 offen offset:1024 lds\n"
"v_mfma_f32_16x16x32_bf16 v[156:159], v[8:11], a[200:203], v[156:159]\n"
"v_mfma_f32_16x16x32_bf16 v[156:159], v[12:15], a[204:207], v[156:159]\n"
"s_waitcnt lgkmcnt(3)\n"
"v_mfma_f32_16x16x32_bf16 v[160:163], v[0:3], a[160:163], v[160:163]\n"
"ds_read_b64_tr_b16 v[8:9], v35 offset:32776\n"
"v_mfma_f32_16x16x32_bf16 v[160:163], v[4:7], a[164:167], v[160:163]\n"
"ds_read_b64_tr_b16 v[10:11], v35 offset:33032\n"
"v_mfma_f32_16x16x32_bf16 v[168:171], v[0:3], a[168:171], v[168:171]\n"
"ds_read_b64_tr_b16 v[12:13], v35 offset:36872\n"
"v_mfma_f32_16x16x32_bf16 v[168:171], v[4:7], a[172:175], v[168:171]\n"
"ds_read_b64_tr_b16 v[14:15], v35 offset:37128\n"
"v_mfma_f32_16x16x32_bf16 v[176:179], v[0:3], a[176:179], v[176:179]\n"
"ds_read_b128 a[220:223], v34 offset:51712\n"
"v_mfma_f32_16x16x32_bf16 v[176:179], v[4:7], a[180:183], v[176:179]\n"
"ds_read_b128 a[224:227], v34 offset:53248\n"
"v_mfma_f32_16x16x32_bf16 v[184:187], v[0:3], a[184:187], v[184:187]\n"
"ds_read_b128 a[228:231], v34 offset:53760\n"
"v_mfma_f32_16x16x32_bf16 v[184:187], v[4:7], a[188:191], v[184:187]\n"
"buffer_load_dwordx4 v26, s[40:43], s65 offen offset:2048 lds\n"
"v_mfma_f32_16x16x32_bf16 v[192:195], v[0:3], a[192:195], v[192:195]\n"
"v_mfma_f32_16x16x32_bf16 v[192:195], v[4:7], a[196:199], v[192:195]\n"
"buffer_load_dwordx4 v27, s[40:43], s65 offen offset:3072 lds\n"
"v_mfma_f32_16x16x32_bf16 v[200:203], v[0:3], a[200:203], v[200:203]\n"
"v_mfma_f32_16x16x32_bf16 v[200:203], v[4:7], a[204:207], v[200:203]\n"
"s_waitcnt lgkmcnt(3)\n"
"v_mfma_f32_16x16x32_bf16 v[164:167], v[8:11], a[160:163], v[164:167]\n"
"ds_read_b64_tr_b16 v[0:1], v35 offset:33280\n"
"v_mfma_f32_16x16x32_bf16 v[164:167], v[12:15], a[164:167], v[164:167]\n"
"ds_read_b64_tr_b16 v[2:3], v35 offset:33536\n"
"v_mfma_f32_16x16x32_bf16 v[172:175], v[8:11], a[168:171], v[172:175]\n"
"ds_read_b64_tr_b16 v[4:5], v35 offset:37376\n"
"v_mfma_f32_16x16x32_bf16 v[172:175], v[12:15], a[172:175], v[172:175]\n"
"ds_read_b64_tr_b16 v[6:7], v35 offset:37632\n"
"v_mfma_f32_16x16x32_bf16 v[180:183], v[8:11], a[176:179], v[180:183]\n"
"ds_read_b128 a[232:235], v34 offset:55296\n"
"v_mfma_f32_16x16x32_bf16 v[180:183], v[12:15], a[180:183], v[180:183]\n"
"ds_read_b128 a[236:239], v34 offset:55808\n"
"v_mfma_f32_16x16x32_bf16 v[188:191], v[8:11], a[184:187], v[188:191]\n"
"s_add_u32 s66, s80, 1536\n"
"ds_read_b128 a[240:243], v34 offset:57344\n"
"v_mfma_f32_16x16x32_bf16 v[188:191], v[12:15], a[188:191], v[188:191]\n"
"s_add_u32 m0, s67, 27648\n"
"s_and_b32 s66, s66, 0x7ff\n"
"v_mfma_f32_16x16x32_bf16 v[196:199], v[8:11], a[192:195], v[196:199]\n"
"buffer_load_dwordx4 v31, s[36:39], s66 offen offset:0 lds\n"
"v_mfma_f32_16x16x32_bf16 v[196:199], v[12:15], a[196:199], v[196:199]\n"
"v_mfma_f32_16x16x32_bf16 v[204:207], v[8:11], a[200:203], v[204:207]\n"
"buffer_load_dwordx4 v32, s[36:39], s66 offen offset:1024 lds\n"
"v_mfma_f32_16x16x32_bf16 v[204:207], v[12:15], a[204:207], v[204:207]\n"
"s_waitcnt lgkmcnt(3)\n"
"v_mfma_f32_16x16x32_bf16 v[208:211], v[0:3], a[160:163], v[208:211]\n"
"ds_read_b64_tr_b16 v[8:9], v35 offset:33288\n"
"v_mfma_f32_16x16x32_bf16 v[208:211], v[4:7], a[164:167], v[208:211]\n"
"ds_read_b64_tr_b16 v[10:11], v35 offset:33544\n"
"v_mfma_f32_16x16x32_bf16 v[216:219], v[0:3], a[168:171], v[216:219]\n"
"ds_read_b64_tr_b16 v[12:13], v35 offset:37384\n"
"v_mfma_f32_16x16x32_bf16 v[216:219], v[4:7], a[172:175], v[216:219]\n"
"ds_read_b64_tr_b16 v[14:15], v35 offset:37640\n"
"v_mfma_f32_16x16x32_bf16 v[224:227], v[0:3], a[176:179], v[224:227]\n"
"ds_read_b128 a[244:247], v34 offset:57856\n"
"v_mfma_f32_16x16x32_bf16 v[224:227], v[4:7], a[180:183], v[224:227]\n"
"ds_read_b128 a[248:251], v34 offset:59392\n"
"v_mfma_f32_16x16x32_bf16 v[232:235], v[0:3], a[184:187], v[232:235]\n"
"ds_read_b128 a[252:255], v34 offset:59904\n"
"v_mfma_f32_16x16x32_bf16 v[232:235], v[4:7], a[188:191], v[232:235]\n"
"v_mfma_f32_16x16x32_bf16 v[240:243], v[0:3], a[192:195], v[240:243]\n"
"buffer_load_dwordx4 v33, s[36:39], s66 offen offset:2048 lds\n"
"v_mfma_f32_16x16x32_bf16 v[240:243], v[4:7], a[196:199], v[240:243]\n"
"v_mfma_f32_16x16x32_bf16 v[248:251], v[0:3], a[200:203], v[248:251]\n"
"v_mfma_f32_16x16x32_bf16 v[248:251], v[4:7], a[204:207], v[248:251]\n"
"s_waitcnt vmcnt(32) lgkmcnt(0)\n"
"s_barrier\n"
"v_mfma_f32_16x16x32_bf16 v[212:215], v[8:11], a[160:163], v[212:215]\n"
"ds_read_b64_tr_b16 v[0:1], v35 offset:0\n"
"v_mfma_f32_16x16x32_bf16 v[212:215], v[12:15], a[164:167], v[212:215]\n"
"ds_read_b64_tr_b16 v[2:3], v35 offset:256\n"
"v_mfma_f32_16x16x32_bf16 v[220:223], v[8:11], a[168:171], v[220:223]\n"
"ds_read_b64_tr_b16 v[4:5], v35 offset:4096\n"
"v_mfma_f32_16x16x32_bf16 v[220:223], v[12:15], a[172:175], v[220:223]\n"
"ds_read_b64_tr_b16 v[6:7], v35 offset:4352\n"
"v_mfma_f32_16x16x32_bf16 v[228:231], v[8:11], a[176:179], v[228:231]\n"
"v_mfma_f32_16x16x32_bf16 v[228:231], v[12:15], a[180:183], v[228:231]\n"
"s_add_u32 s65, s81, 6291456\n"
"v_mfma_f32_16x16x32_bf16 v[236:239], v[8:11], a[184:187], v[236:239]\n"
"s_add_u32 m0, s68, 32768\n"
"s_and_b32 s65, s65, 0x7fffff\n"
"v_mfma_f32_16x16x32_bf16 v[236:239], v[12:15], a[188:191], v[236:239]\n"
"buffer_load_dwordx4 v24, s[40:43], s65 offen offset:0 lds\n"
"v_mfma_f32_16x16x32_bf16 v[244:247], v[8:11], a[192:195], v[244:247]\n"
"v_mfma_f32_16x16x32_bf16 v[244:247], v[12:15], a[196:199], v[244:247]\n"
"buffer_load_dwordx4 v25, s[40:43], s65 offen offset:1024 lds\n"
"v_mfma_f32_16x16x32_bf16 v[252:255], v[8:11], a[200:203], v[252:255]\n"
"v_mfma_f32_16x16x32_bf16 v[252:255], v[12:15], a[204:207], v[252:255]\n"
"s_waitcnt lgkmcnt(0)\n"
"v_mfma_f32_16x16x32_bf16 v[64:67], v[0:3], a[208:211], v[64:67]\n"
"ds_read_b64_tr_b16 v[8:9], v35 offset:8\n"
"v_mfma_f32_16x16x32_bf16 v[64:67], v[4:7], a[212:215], v[64:67]\n"
"ds_read_b64_tr_b16 v[10:11], v35 offset:264\n"
"v_mfma_f32_16x16x32_bf16 v[72:75], v[0:3], a[216:219], v[72:75]\n"
"ds_read_b64_tr_b16 v[12:13], v35 offset:4104\n"
"v_mfma_f32_16x16x32_bf16 v[72:75], v[4:7], a[220:223], v[72:75]\n"
"ds_read_b64_tr_b16 v[14:15], v35 offset:4360\n"
"v_mfma_f32_16x16x32_bf16 v[80:83], v[0:3], a[224:227], v[80:83]\n"
"v_mfma_f32_16x16x32_bf16 v[80:83], v[4:7], a[228:231], v[80:83]\n"
"v_mfma_f32_16x16x32_bf16 v[88:91], v[0:3], a[232:235], v[88:91]\n"
"v_mfma_f32_16x16x32_bf16 v[88:91], v[4:7], a[236:239], v[88:91]\n"
"buffer_load_dwordx4 v26, s[40:43], s65 offen offset:2048 lds\n"
"v_mfma_f32_16x16x32_bf16 v[96:99], v[0:3], a[240:243], v[96:99]\n"
"v_mfma_f32_16x16x32_bf16 v[96:99], v[4:7], a[244:247], v[96:99]\n"
"buffer_load_dwordx4 v27, s[40:43], s65 offen offset:3072 lds\n"
"v_mfma_f32_16x16x32_bf16 v[104:107], v[0:3], a[248:251], v[104:107]\n"
"v_mfma_f32_16x16x32_bf16 v[104:107], v[4:7], a[252:255], v[104:107]\n"
"s_waitcnt lgkmcnt(0)\n"
"v_mfma_f32_16x16x32_bf16 v[68:71], v[8:11], a[208:211], v[68:71]\n"
"ds_read_b64_tr_b16 v[0:1], v35 offset:512\n"
"v_mfma_f32_16x16x32_bf16 v[68:71], v[12:15], a[212:215], v[68:71]\n"
"ds_read_b64_tr_b16 v[2:3], v35 offset:768\n"
"v_mfma_f32_16x16x32_bf16 v[76:79], v[8:11], a[216:219], v[76:79]\n"
"ds_read_b64_tr_b16 v[4:5], v35 offset:4608\n"
"v_mfma_f32_16x16x32_bf16 v[76:79], v[12:15], a[220:223], v[76:79]\n"
"ds_read_b64_tr_b16 v[6:7], v35 offset:4864\n"
"v_mfma_f32_16x16x32_bf16 v[84:87], v[8:11], a[224:227], v[84:87]\n"
"v_mfma_f32_16x16x32_bf16 v[84:87], v[12:15], a[228:231], v[84:87]\n"
"v_mfma_f32_16x16x32_bf16 v[92:95], v[8:11], a[232:235], v[92:95]\n"
"s_add_u32 s66, s80, 1664\n"
"v_mfma_f32_16x16x32_bf16 v[92:95], v[12:15], a[236:239], v[92:95]\n"
"s_add_u32 m0, s67, 49152\n"
"s_and_b32 s66, s66, 0x7ff\n"
"v_mfma_f32_16x16x32_bf16 v[100:103], v[8:11], a[240:243], v[100:103]\n"
"buffer_load_dwordx4 v28, s[36:39], s66 offen offset:0 lds\n"
"v_mfma_f32_16x16x32_bf16 v[100:103], v[12:15], a[244:247], v[100:103]\n"
"v_mfma_f32_16x16x32_bf16 v[108:111], v[8:11], a[248:251], v[108:111]\n"
"buffer_load_dwordx4 v29, s[36:39], s66 offen offset:1024 lds\n"
"v_mfma_f32_16x16x32_bf16 v[108:111], v[12:15], a[252:255], v[108:111]\n"
"s_waitcnt lgkmcnt(0)\n"
"v_mfma_f32_16x16x32_bf16 v[112:115], v[0:3], a[208:211], v[112:115]\n"
"ds_read_b64_tr_b16 v[8:9], v35 offset:520\n"
"v_mfma_f32_16x16x32_bf16 v[112:115], v[4:7], a[212:215], v[112:115]\n"
"ds_read_b64_tr_b16 v[10:11], v35 offset:776\n"
"v_mfma_f32_16x16x32_bf16 v[120:123], v[0:3], a[216:219], v[120:123]\n"
"ds_read_b64_tr_b16 v[12:13], v35 offset:4616\n"
"v_mfma_f32_16x16x32_bf16 v[120:123], v[4:7], a[220:223], v[120:123]\n"
"ds_read_b64_tr_b16 v[14:15], v35 offset:4872\n"
"v_mfma_f32_16x16x32_bf16 v[128:131], v[0:3], a[224:227], v[128:131]\n"
"v_mfma_f32_16x16x32_bf16 v[128:131], v[4:7], a[228:231], v[128:131]\n"
"v_mfma_f32_16x16x32_bf16 v[136:139], v[0:3], a[232:235], v[136:139]\n"
"v_mfma_f32_16x16x32_bf16 v[136:139], v[4:7], a[236:239], v[136:139]\n"
"v_mfma_f32_16x16x32_bf16 v[144:147], v[0:3], a[240:243], v[144:147]\n"
"buffer_load_dwordx4 v30, s[36:39], s66 offen offset:2048 lds\n"
"v_mfma_f32_16x16x32_bf16 v[144:147], v[4:7], a[244:247], v[144:147]\n"
"v_mfma_f32_16x16x32_bf16 v[152:155], v[0:3], a[248:251], v[152:155]\n"
"v_mfma_f32_16x16x32_bf16 v[152:155], v[4:7], a[252:255], v[152:155]\n"
"s_bitcmp1_b32 2, s94\n"
"s_cbranch_scc0 K1_W9\n"
"s_waitcnt vmcnt(21)\n"
"K1_W9:\n"
"s_waitcnt vmcnt(29) lgkmcnt(0)\n"
"s_barrier\n"
"v_mfma_f32_16x16x32_bf16 v[116:119], v[8:11], a[208:211], v[116:119]\n"
"ds_read_b64_tr_b16 v[0:1], v35 offset:8192\n"
"v_mfma_f32_16x16x32_bf16 v[116:119], v[12:15], a[212:215], v[116:119]\n"
"ds_read_b64_tr_b16 v[2:3], v35 offset:8448\n"
"v_mfma_f32_16x16x32_bf16 v[124:127], v[8:11], a[216:219], v[124:127]\n"
"ds_read_b64_tr_b16 v[4:5], v35 offset:12288\n"
"v_mfma_f32_16x16x32_bf16 v[124:127], v[12:15], a[220:223], v[124:127]\n"
"ds_read_b64_tr_b16 v[6:7], v35 offset:12544\n"
"v_mfma_f32_16x16x32_bf16 v[132:135], v[8:11], a[224:227], v[132:135]\n"
"ds_read_b128 a[160:163], v34 offset:0\n"
"v_mfma_f32_16x16x32_bf16 v[132:135], v[12:15], a[228:231], v[132:135]\n"
"s_add_u32 s65, s81, 6291584\n"
"ds_read_b128 a[164:167], v34 offset:512\n"
"v_mfma_f32_16x16x32_bf16 v[140:143], v[8:11], a[232:235], v[140:143]\n"
"s_add_u32 m0, s68, 0\n"
"s_and_b32 s65, s65, 0x7fffff\n"
"ds_read_b128 a[168:171], v34 offset:2048\n"
"v_mfma_f32_16x16x32_bf16 v[140:143], v[12:15], a[236:239], v[140:143]\n"
"buffer_load_dwordx4 v24, s[40:43], s65 offen offset:0 lds\n"
"v_mfma_f32_16x16x32_bf16 v[148:151], v[8:11], a[240:243], v[148:151]\n"
"buffer_load_dwordx4 v25, s[40:43], s65 offen offset:1024 lds\n"
"v_mfma_f32_16x16x32_bf16 v[148:151], v[12:15], a[244:247], v[148:151]\n"
"v_mfma_f32_16x16x32_bf16 v[156:159], v[8:11], a[248:251], v[156:159]\n"
"buffer_load_dwordx4 v26, s[40:43], s65 offen offset:2048 lds\n"
"v_mfma_f32_16x16x32_bf16 v[156:159], v[12:15], a[252:255], v[156:159]\n"
"s_waitcnt lgkmcnt(3)\n"
"v_mfma_f32_16x16x32_bf16 v[160:163], v[0:3], a[208:211], v[160:163]\n"
"ds_read_b64_tr_b16 v[8:9], v35 offset:8200\n"
"v_mfma_f32_16x16x32_bf16 v[160:163], v[4:7], a[212:215], v[160:163]\n"
"ds_read_b64_tr_b16 v[10:11], v35 offset:8456\n"
"v_mfma_f32_16x16x32_bf16 v[168:171], v[0:3], a[216:219], v[168:171]\n"
"ds_read_b64_tr_b16 v[12:13], v35 offset:12296\n"
"v_mfma_f32_16x16x32_bf16 v[168:171], v[4:7], a[220:223], v[168:171]\n"
"ds_read_b64_tr_b16 v[14:15], v35 offset:12552\n"
"v_mfma_f32_16x16x32_bf16 v[176:179], v[0:3], a[224:227], v[176:179]\n"
"ds_read_b128 a[172:175], v34 offset:2560\n"
"v_mfma_f32_16x16x32_bf16 v[176:179], v[4:7], a[228:231], v[176:179]\n"
"ds_read_b128 a[176:179], v34 offset:4096\n"
"v_mfma_f32_16x16x32_bf16 v[184:187], v[0:3], a[232:235], v[184:187]\n"
"ds_read_b128 a[180:183], v34 offset:4608\n"
"v_mfma_f32_16x16x32_bf16 v[184:187], v[4:7], a[236:239], v[184:187]\n"
"v_mfma_f32_16x16x32_bf16 v[192:195], v[0:3], a[240:243], v[192:195]\n"
"buffer_load_dwordx4 v27, s[40:43], s65 offen offset:3072 lds\n"
"v_mfma_f32_16x16x32_bf16 v[192:195], v[4:7], a[244:247], v[192:195]\n"
"s_add_u32 s66, s80, 1664\n"
"s_add_u32 m0, s67, 52224\n"
"s_and_b32 s66, s66, 0x7ff\n"
"v_mfma_f32_16x16x32_bf16 v[200:203], v[0:3], a[248:251], v[200:203]\n"
"buffer_load_dwordx4 v31, s[36:39], s66 offen offset:0 lds\n"
"v_mfma_f32_16x16x32_bf16 v[200:203], v[4:7], a[252:255], v[200:203]\n"
"s_waitcnt lgkmcnt(3)\n"
"v_mfma_f32_16x16x32_bf16 v[164:167], v[8:11], a[208:211], v[164:167]\n"
"ds_read_b64_tr_b16 v[0:1], v35 offset:8704\n"
"v_mfma_f32_16x16x32_bf16 v[164:167], v[12:15], a[212:215], v[164:167]\n"
"ds_read_b64_tr_b16 v[2:3], v35 offset:8960\n"
"v_mfma_f32_16x16x32_bf16 v[172:175], v[8:11], a[216:219], v[172:175]\n"
"ds_read_b64_tr_b16 v[4:5], v35 offset:12800\n"
"v_mfma_f32_16x16x32_bf16 v[172:175], v[12:15], a[220:223], v[172:175]\n"
"ds_read_b64_tr_b16 v[6:7], v35 offset:13056\n"
"v_mfma_f32_16x16x32_bf16 v[180:183], v[8:11], a[224:227], v[180:183]\n"
"ds_read_b128 a[184:187], v34 offset:6144\n"
"v_mfma_f32_16x16x32_bf16 v[180:183], v[12:15], a[228:231], v[180:183]\n"
"ds_read_b128 a[188:191], v34 offset:6656\n"
"v_mfma_f32_16x16x32_bf16 v[188:191], v[8:11], a[232:235], v[188:191]\n"
"ds_read_b128 a[192:195], v34 offset:8192\n"
"v_mfma_f32_16x16x32_bf16 v[188:191], v[12:15], a[236:239], v[188:191]\n"
"buffer_load_dwordx4 v32, s[36:39], s66 offen offset:1024 lds\n"
"v_mfma_f32_16x16x32_bf16 v[196:199], v[8:11], a[240:243], v[196:199]\n"
"v_mfma_f32_16x16x32_bf16 v[196:199], v[12:15], a[244:247], v[196:199]\n"
"buffer_load_dwordx4 v33, s[36:39], s66 offen offset:2048 lds\n"
"v_mfma_f32_16x16x32_bf16 v[204:207], v[8:11], a[248:251], v[204:207]\n"
"v_mfma_f32_16x16x32_bf16 v[204:207], v[12:15], a[252:255], v[204:207]\n"
"s_waitcnt lgkmcnt(3)\n"
"v_mfma_f32_16x16x32_bf16 v[208:211], v[0:3], a[208:211], v[208:211]\n"
"ds_read_b64_tr_b16 v[8:9], v35 offset:8712\n"
"v_mfma_f32_16x16x32_bf16 v[208:211], v[4:7], a[212:215], v[208:211]\n"
"ds_read_b64_tr_b16 v[10:11], v35 offset:8968\n"
"v_mfma_f32_16x16x32_bf16 v[216:219], v[0:3], a[216:219], v[216:219]\n"
"ds_read_b64_tr_b16 v[12:13], v35 offset:12808\n"
"v_mfma_f32_16x16x32_bf16 v[216:219], v[4:7], a[220:223], v[216:219]\n"
"ds_read_b64_tr_b16 v[14:15], v35 offset:13064\n"
"v_mfma_f32_16x16x32_bf16 v[224:227], v[0:3], a[224:227], v[224:227]\n"
"ds_read_b128 a[196:199], v34 offset:8704\n"
"v_mfma_f32_16x16x32_bf16 v[224:227], v[4:7], a[228:231], v[224:227]\n"
"ds_read_b128 a[200:203], v34 offset:10240\n"
"v_mfma_f32_16x16x32_bf16 v[232:235], v[0:3], a[232:235], v[232:235]\n"
"ds_read_b128 a[204:207], v34 offset:10752\n"
"v_mfma_f32_16x16x32_bf16 v[232:235], v[4:7], a[236:239], v[232:235]\n"
"s_cmp_eq_u32 s94, 1\n"
"s_cselect_b64 vcc, -1, 0\n"
"s_add_u32 s56, s82, 24576\n"
"s_and_b32 s56, s56, 0x3ffff\n"
"s_cbranch_vccz K1_KS3_1_0\n"
"buffer_load_dwordx4 a[96:99], v44, s[48:51], s56 offen offset:0\n"
"buffer_load_dwordx4 a[100:103], v45, s[48:51], s56 offen offset:1024\n"
"buffer_load_dwordx4 a[104:107], v46, s[48:51], s56 offen offset:2048\n"
"buffer_load_dwordx4 a[108:111], v47, s[48:51], s56 offen offset:3072\n"
"K1_KS3_1_0:\n"
"v_mfma_f32_16x16x32_bf16 v[240:243], v[0:3], a[240:243], v[240:243]\n"
"v_mfma_f32_16x16x32_bf16 v[240:243], v[4:7], a[244:247], v[240:243]\n"
"s_add_u32 s56, s82, 28672\n"
"s_and_b32 s56, s56, 0x3ffff\n"
"s_cbranch_vccz K1_KS3_1_4\n"
"buffer_load_dwordx4 a[112:115], v44, s[48:51], s56 offen offset:0\n"
"buffer_load_dwordx4 a[116:119], v45, s[48:51], s56 offen offset:1024\n"
"buffer_load_dwordx4 a[120:123], v46, s[48:51], s56 offen offset:2048\n"
"buffer_load_dwordx4 a[124:127], v47, s[48:51], s56 offen offset:3072\n"
"K1_KS3_1_4:\n"
"v_mfma_f32_16x16x32_bf16 v[248:251], v[0:3], a[248:251], v[248:251]\n"
"v_mfma_f32_16x16x32_bf16 v[248:251], v[4:7], a[252:255], v[248:251]\n"
"s_waitcnt vmcnt(32) lgkmcnt(0)\n"
"s_barrier\n"
"v_mfma_f32_16x16x32_bf16 v[212:215], v[8:11], a[208:211], v[212:215]\n"
"ds_read_b64_tr_b16 v[0:1], v35 offset:16384\n"
"v_mfma_f32_16x16x32_bf16 v[212:215], v[12:15], a[212:215], v[212:215]\n"
"ds_read_b64_tr_b16 v[2:3], v35 offset:16640\n"
"v_mfma_f32_16x16x32_bf16 v[220:223], v[8:11], a[216:219], v[220:223]\n"
"ds_read_b64_tr_b16 v[4:5], v35 offset:20480\n"
"v_mfma_f32_16x16x32_bf16 v[220:223], v[12:15], a[220:223], v[220:223]\n"
"ds_read_b64_tr_b16 v[6:7], v35 offset:20736\n"
"v_mfma_f32_16x16x32_bf16 v[228:231], v[8:11], a[224:227], v[228:231]\n"
"v_mfma_f32_16x16x32_bf16 v[228:231], v[12:15], a[228:231], v[228:231]\n"
"s_add_u32 s65, s81, 6815744\n"
"v_mfma_f32_16x16x32_bf16 v[236:239], v[8:11], a[232:235], v[236:239]\n"
"s_add_u32 m0, s68, 8192\n"
"s_and_b32 s65, s65, 0x7fffff\n"
"v_mfma_f32_16x16x32_bf16 v[236:239], v[12:15], a[236:239], v[236:239]\n"
"buffer_load_dwordx4 v24, s[40:43], s65 offen offset:0 lds\n"
"v_mfma_f32_16x16x32_bf16 v[244:247], v[8:11], a[240:243], v[244:247]\n"
"v_mfma_f32_16x16x32_bf16 v[244:247], v[12:15], a[244:247], v[244:247]\n"
"buffer_load_dwordx4 v25, s[40:43], s65 offen offset:1024 lds\n"
"v_mfma_f32_16x16x32_bf16 v[252:255], v[8:11], a[248:251], v[252:255]\n"
"v_mfma_f32_16x16x32_bf16 v[252:255], v[12:15], a[252:255], v[252:255]\n"
"s_waitcnt lgkmcnt(0)\n"
"v_mfma_f32_16x16x32_bf16 v[64:67], v[0:3], a[160:163], v[64:67]\n"
"ds_read_b64_tr_b16 v[8:9], v35 offset:16392\n"
"v_mfma_f32_16x16x32_bf16 v[64:67], v[4:7], a[164:167], v[64:67]\n"
"ds_read_b64_tr_b16 v[10:11], v35 offset:16648\n"
"v_mfma_f32_16x16x32_bf16 v[72:75], v[0:3], a[168:171], v[72:75]\n"
"ds_read_b64_tr_b16 v[12:13], v35 offset:20488\n"
"v_mfma_f32_16x16x32_bf16 v[72:75], v[4:7], a[172:175], v[72:75]\n"
"ds_read_b64_tr_b16 v[14:15], v35 offset:20744\n"
"v_mfma_f32_16x16x32_bf16 v[80:83], v[0:3], a[176:179], v[80:83]\n"
"v_mfma_f32_16x16x32_bf16 v[80:83], v[4:7], a[180:183], v[80:83]\n"
"v_mfma_f32_16x16x32_bf16 v[88:91], v[0:3], a[184:187], v[88:91]\n"
"v_mfma_f32_16x16x32_bf16 v[88:91], v[4:7], a[188:191], v[88:91]\n"
"buffer_load_dwordx4 v26, s[40:43], s65 offen offset:2048 lds\n"
"v_mfma_f32_16x16x32_bf16 v[96:99], v[0:3], a[192:195], v[96:99]\n"
"v_mfma_f32_16x16x32_bf16 v[96:99], v[4:7], a[196:199], v[96:99]\n"
"buffer_load_dwordx4 v27, s[40:43], s65 offen offset:3072 lds\n"
"v_mfma_f32_16x16x32_bf16 v[104:107], v[0:3], a[200:203], v[104:107]\n"
"v_mfma_f32_16x16x32_bf16 v[104:107], v[4:7], a[204:207], v[104:107]\n"
"s_waitcnt lgkmcnt(0)\n"
"v_mfma_f32_16x16x32_bf16 v[68:71], v[8:11], a[160:163], v[68:71]\n"
"ds_read_b64_tr_b16 v[0:1], v35 offset:16896\n"
"v_mfma_f32_16x16x32_bf16 v[68:71], v[12:15], a[164:167], v[68:71]\n"
"ds_read_b64_tr_b16 v[2:3], v35 offset:17152\n"
"v_mfma_f32_16x16x32_bf16 v[76:79], v[8:11], a[168:171], v[76:79]\n"
"ds_read_b64_tr_b16 v[4:5], v35 offset:20992\n"
"v_mfma_f32_16x16x32_bf16 v[76:79], v[12:15], a[172:175], v[76:79]\n"
"ds_read_b64_tr_b16 v[6:7], v35 offset:21248\n"
"v_mfma_f32_16x16x32_bf16 v[84:87], v[8:11], a[176:179], v[84:87]\n"
"v_mfma_f32_16x16x32_bf16 v[84:87], v[12:15], a[180:183], v[84:87]\n"
"v_mfma_f32_16x16x32_bf16 v[92:95], v[8:11], a[184:187], v[92:95]\n"
"s_add_u32 s66, s80, 1792\n"
"v_mfma_f32_16x16x32_bf16 v[92:95], v[12:15], a[188:191], v[92:95]\n"
"s_add_u32 m0, s67, 0\n"
"s_and_b32 s66, s66, 0x7ff\n"
"v_mfma_f32_16x16x32_bf16 v[100:103], v[8:11], a[192:195], v[100:103]\n"
"buffer_load_dwordx4 v28, s[36:39], s66 offen offset:0 lds\n"
"v_mfma_f32_16x16x32_bf16 v[100:103], v[12:15], a[196:199], v[100:103]\n"
"v_mfma_f32_16x16x32_bf16 v[108:111], v[8:11], a[200:203], v[108:111]\n"
"buffer_load_dwordx4 v29, s[36:39], s66 offen offset:1024 lds\n"
"v_mfma_f32_16x16x32_bf16 v[108:111], v[12:15], a[204:207], v[108:111]\n"
"s_waitcnt lgkmcnt(0)\n"
"v_mfma_f32_16x16x32_bf16 v[112:115], v[0:3], a[160:163], v[112:115]\n"
"ds_read_b64_tr_b16 v[8:9], v35 offset:16904\n"
"v_mfma_f32_16x16x32_bf16 v[112:115], v[4:7], a[164:167], v[112:115]\n"
"ds_read_b64_tr_b16 v[10:11], v35 offset:17160\n"
"v_mfma_f32_16x16x32_bf16 v[120:123], v[0:3], a[168:171], v[120:123]\n"
"ds_read_b64_tr_b16 v[12:13], v35 offset:21000\n"
"v_mfma_f32_16x16x32_bf16 v[120:123], v[4:7], a[172:175], v[120:123]\n"
"ds_read_b64_tr_b16 v[14:15], v35 offset:21256\n"
"v_mfma_f32_16x16x32_bf16 v[128:131], v[0:3], a[176:179], v[128:131]\n"
"v_mfma_f32_16x16x32_bf16 v[128:131], v[4:7], a[180:183], v[128:131]\n"
"v_mfma_f32_16x16x32_bf16 v[136:139], v[0:3], a[184:187], v[136:139]\n"
"v_mfma_f32_16x16x32_bf16 v[136:139], v[4:7], a[188:191], v[136:139]\n"
"v_mfma_f32_16x16x32_bf16 v[144:147], v[0:3], a[192:195], v[144:147]\n"
"buffer_load_dwordx4 v30, s[36:39], s66 offen offset:2048 lds\n"
"v_mfma_f32_16x16x32_bf16 v[144:147], v[4:7], a[196:199], v[144:147]\n"
"v_mfma_f32_16x16x32_bf16 v[152:155], v[0:3], a[200:203], v[152:155]\n"
"v_mfma_f32_16x16x32_bf16 v[152:155], v[4:7], a[204:207], v[152:155]\n"
"s_bitcmp1_b32 1, s94\n"
"s_cbranch_scc0 K1_W10\n"
"s_waitcnt vmcnt(21)\n"
"K1_W10:\n"
"s_waitcnt vmcnt(29) lgkmcnt(0)\n"
"s_barrier\n"
"v_mfma_f32_16x16x32_bf16 v[116:119], v[8:11], a[160:163], v[116:119]\n"
"ds_read_b64_tr_b16 v[0:1], v35 offset:24576\n"
"v_mfma_f32_16x16x32_bf16 v[116:119], v[12:15], a[164:167], v[116:119]\n"
"ds_read_b64_tr_b16 v[2:3], v35 offset:24832\n"
"v_mfma_f32_16x16x32_bf16 v[124:127], v[8:11], a[168:171], v[124:127]\n"
"ds_read_b64_tr_b16 v[4:5], v35 offset:28672\n"
"v_mfma_f32_16x16x32_bf16 v[124:127], v[12:15], a[172:175], v[124:127]\n"
"ds_read_b64_tr_b16 v[6:7], v35 offset:28928\n"
"v_mfma_f32_16x16x32_bf16 v[132:135], v[8:11], a[176:179], v[132:135]\n"
"ds_read_b128 a[208:211], v34 offset:24576\n"
"v_mfma_f32_16x16x32_bf16 v[132:135], v[12:15], a[180:183], v[132:135]\n"
"s_add_u32 s65, s81, 6815872\n"
"ds_read_b128 a[212:215], v34 offset:25088\n"
"v_mfma_f32_16x16x32_bf16 v[140:143], v[8:11], a[184:187], v[140:143]\n"
"s_add_u32 m0, s68, 16384\n"
"s_and_b32 s65, s65, 0x7fffff\n"
"ds_read_b128 a[216:219], v34 offset:26624\n"
"v_mfma_f32_16x16x32_bf16 v[140:143], v[12:15], a[188:191], v[140:143]\n"
"buffer_load_dwordx4 v24, s[40:43], s65 offen offset:0 lds\n"
"v_mfma_f32_16x16x32_bf16 v[148:151], v[8:11], a[192:195], v[148:151]\n"
"buffer_load_dwordx4 v25, s[40:43], s65 offen offset:1024 lds\n"
"v_mfma_f32_16x16x32_bf16 v[148:151], v[12:15], a[196:199], v[148:151]\n"
"v_mfma_f32_16x16x32_bf16 v[156:159], v[8:11], a[200:203], v[156:159]\n"
"buffer_load_dwordx4 v26, s[40:43], s65 offen offset:2048 lds\n"
"v_mfma_f32_16x16x32_bf16 v[156:159], v[12:15], a[204:207], v[156:159]\n"
"s_waitcnt lgkmcnt(3)\n"
"v_mfma_f32_16x16x32_bf16 v[160:163], v[0:3], a[160:163], v[160:163]\n"
"ds_read_b64_tr_b16 v[8:9], v35 offset:24584\n"
"v_mfma_f32_16x16x32_bf16 v[160:163], v[4:7], a[164:167], v[160:163]\n"
"ds_read_b64_tr_b16 v[10:11], v35 offset:24840\n"
"v_mfma_f32_16x16x32_bf16 v[168:171], v[0:3], a[168:171], v[168:171]\n"
"ds_read_b64_tr_b16 v[12:13], v35 offset:28680\n"
"v_mfma_f32_16x16x32_bf16 v[168:171], v[4:7], a[172:175], v[168:171]\n"
"ds_read_b64_tr_b16 v[14:15], v35 offset:28936\n"
"v_mfma_f32_16x16x32_bf16 v[176:179], v[0:3], a[176:179], v[176:179]\n"
"ds_read_b128 a[220:223], v34 offset:27136\n"
"v_mfma_f32_16x16x32_bf16 v[176:179], v[4:7], a[180:183], v[176:179]\n"
"ds_read_b128 a[224:227], v34 offset:28672\n"
"v_mfma_f32_16x16x32_bf16 v[184:187], v[0:3], a[184:187], v[184:187]\n"
"ds_read_b128 a[228:231], v34 offset:29184\n"
"v_mfma_f32_16x16x32_bf16 v[184:187], v[4:7], a[188:191], v[184:187]\n"
"v_mfma_f32_16x16x32_bf16 v[192:195], v[0:3], a[192:195], v[192:195]\n"
"buffer_load_dwordx4 v27, s[40:43], s65 offen offset:3072 lds\n"
"v_mfma_f32_16x16x32_bf16 v[192:195], v[4:7], a[196:199], v[192:195]\n"
"s_add_u32 s66, s80, 1792\n"
"s_add_u32 m0, s67, 3072\n"
"s_and_b32 s66, s66, 0x7ff\n"
"v_mfma_f32_16x16x32_bf16 v[200:203], v[0:3], a[200:203], v[200:203]\n"
"buffer_load_dwordx4 v31, s[36:39], s66 offen offset:0 lds\n"
"v_mfma_f32_16x16x32_bf16 v[200:203], v[4:7], a[204:207], v[200:203]\n"
"s_waitcnt lgkmcnt(3)\n"
"v_mfma_f32_16x16x32_bf16 v[164:167], v[8:11], a[160:163], v[164:167]\n"
"ds_read_b64_tr_b16 v[0:1], v35 offset:25088\n"
"v_mfma_f32_16x16x32_bf16 v[164:167], v[12:15], a[164:167], v[164:167]\n"
"ds_read_b64_tr_b16 v[2:3], v35 offset:25344\n"
"v_mfma_f32_16x16x32_bf16 v[172:175], v[8:11], a[168:171], v[172:175]\n"
"ds_read_b64_tr_b16 v[4:5], v35 offset:29184\n"
"v_mfma_f32_16x16x32_bf16 v[172:175], v[12:15], a[172:175], v[172:175]\n"
"ds_read_b64_tr_b16 v[6:7], v35 offset:29440\n"
"v_mfma_f32_16x16x32_bf16 v[180:183], v[8:11], a[176:179], v[180:183]\n"
"ds_read_b128 a[232:235], v34 offset:30720\n"
"v_mfma_f32_16x16x32_bf16 v[180:183], v[12:15], a[180:183], v[180:183]\n"
"ds_read_b128 a[236:239], v34 offset:31232\n"
"v_mfma_f32_16x16x32_bf16 v[188:191], v[8:11], a[184:187], v[188:191]\n"
"ds_read_b128 a[240:243], v34 offset:32768\n"
"v_mfma_f32_16x16x32_bf16 v[188:191], v[12:15], a[188:191], v[188:191]\n"
"buffer_load_dwordx4 v32, s[36:39], s66 offen offset:1024 lds\n"
"v_mfma_f32_16x16x32_bf16 v[196:199], v[8:11], a[192:195], v[196:199]\n"
"v_mfma_f32_16x16x32_bf16 v[196:199], v[12:15], a[196:199], v[196:199]\n"
"buffer_load_dwordx4 v33, s[36:39], s66 offen offset:2048 lds\n"
"v_mfma_f32_16x16x32_bf16 v[204:207], v[8:11], a[200:203], v[204:207]\n"
"v_mfma_f32_16x16x32_bf16 v[204:207], v[12:15], a[204:207], v[204:207]\n"
"s_waitcnt lgkmcnt(3)\n"
"v_mfma_f32_16x16x32_bf16 v[208:211], v[0:3], a[160:163], v[208:211]\n"
"ds_read_b64_tr_b16 v[8:9], v35 offset:25096\n"
"v_mfma_f32_16x16x32_bf16 v[208:211], v[4:7], a[164:167], v[208:211]\n"
"ds_read_b64_tr_b16 v[10:11], v35 offset:25352\n"
"v_mfma_f32_16x16x32_bf16 v[216:219], v[0:3], a[168:171], v[216:219]\n"
"ds_read_b64_tr_b16 v[12:13], v35 offset:29192\n"
"v_mfma_f32_16x16x32_bf16 v[216:219], v[4:7], a[172:175], v[216:219]\n"
"ds_read_b64_tr_b16 v[14:15], v35 offset:29448\n"
"v_mfma_f32_16x16x32_bf16 v[224:227], v[0:3], a[176:179], v[224:227]\n"
"ds_read_b128 a[244:247], v34 offset:33280\n"
"v_mfma_f32_16x16x32_bf16 v[224:227], v[4:7], a[180:183], v[224:227]\n"
"ds_read_b128 a[248:251], v34 offset:34816\n"
"v_mfma_f32_16x16x32_bf16 v[232:235], v[0:3], a[184:187], v[232:235]\n"
"ds_read_b128 a[252:255], v34 offset:35328\n"
"v_mfma_f32_16x16x32_bf16 v[232:235], v[4:7], a[188:191], v[232:235]\n"
"s_cmp_eq_u32 s94, 0\n"
"s_cselect_b64 vcc, -1, 0\n"
"s_add_u32 s56, s82, 32768\n"
"s_and_b32 s56, s56, 0x3ffff\n"
"s_cbranch_vccz K1_KS4_0_0\n"
"buffer_load_dwordx4 a[128:131], v44, s[48:51], s56 offen offset:0\n"
"buffer_load_dwordx4 a[132:135], v45, s[48:51], s56 offen offset:1024\n"
"buffer_load_dwordx4 a[136:139], v46, s[48:51], s56 offen offset:2048\n"
"buffer_load_dwordx4 a[140:143], v47, s[48:51], s56 offen offset:3072\n"
"K1_KS4_0_0:\n"
"v_mfma_f32_16x16x32_bf16 v[240:243], v[0:3], a[192:195], v[240:243]\n"
"v_mfma_f32_16x16x32_bf16 v[240:243], v[4:7], a[196:199], v[240:243]\n"
"s_add_u32 s56, s82, 36864\n"
"s_and_b32 s56, s56, 0x3ffff\n"
"s_cbranch_vccz K1_KS4_0_4\n"
"buffer_load_dwordx4 a[144:147], v44, s[48:51], s56 offen offset:0\n"
"buffer_load_dwordx4 a[148:151], v45, s[48:51], s56 offen offset:1024\n"
"buffer_load_dwordx4 a[152:155], v46, s[48:51], s56 offen offset:2048\n"
"buffer_load_dwordx4 a[156:159], v47, s[48:51], s56 offen offset:3072\n"
"K1_KS4_0_4:\n"
"v_mfma_f32_16x16x32_bf16 v[248:251], v[0:3], a[200:203], v[248:251]\n"
"v_mfma_f32_16x16x32_bf16 v[248:251], v[4:7], a[204:207], v[248:251]\n"
"s_waitcnt vmcnt(32) lgkmcnt(0)\n"
"s_barrier\n"
"v_mfma_f32_16x16x32_bf16 v[212:215], v[8:11], a[160:163], v[212:215]\n"
"ds_read_b64_tr_b16 v[0:1], v35 offset:32768\n"
"v_mfma_f32_16x16x32_bf16 v[212:215], v[12:15], a[164:167], v[212:215]\n"
"ds_read_b64_tr_b16 v[2:3], v35 offset:33024\n"
"v_mfma_f32_16x16x32_bf16 v[220:223], v[8:11], a[168:171], v[220:223]\n"
"ds_read_b64_tr_b16 v[4:5], v35 offset:36864\n"
"v_mfma_f32_16x16x32_bf16 v[220:223], v[12:15], a[172:175], v[220:223]\n"
"ds_read_b64_tr_b16 v[6:7], v35 offset:37120\n"
"v_mfma_f32_16x16x32_bf16 v[228:231], v[8:11], a[176:179], v[228:231]\n"
"v_mfma_f32_16x16x32_bf16 v[228:231], v[12:15], a[180:183], v[228:231]\n"
"s_add_u32 s65, s81, 7340032\n"
"v_mfma_f32_16x16x32_bf16 v[236:239], v[8:11], a[184:187], v[236:239]\n"
"s_add_u32 m0, s68, 24576\n"
"s_and_b32 s65, s65, 0x7fffff\n"
"v_mfma_f32_16x16x32_bf16 v[236:239], v[12:15], a[188:191], v[236:239]\n"
"buffer_load_dwordx4 v24, s[40:43], s65 offen offset:0 lds\n"
"v_mfma_f32_16x16x32_bf16 v[244:247], v[8:11], a[192:195], v[244:247]\n"
"v_mfma_f32_16x16x32_bf16 v[244:247], v[12:15], a[196:199], v[244:247]\n"
"buffer_load_dwordx4 v25, s[40:43], s65 offen offset:1024 lds\n"
"v_mfma_f32_16x16x32_bf16 v[252:255], v[8:11], a[200:203], v[252:255]\n"
"v_mfma_f32_16x16x32_bf16 v[252:255], v[12:15], a[204:207], v[252:255]\n"
"s_waitcnt lgkmcnt(0)\n"
"v_mfma_f32_16x16x32_bf16 v[64:67], v[0:3], a[208:211], v[64:67]\n"
"ds_read_b64_tr_b16 v[8:9], v35 offset:32776\n"
"v_mfma_f32_16x16x32_bf16 v[64:67], v[4:7], a[212:215], v[64:67]\n"
"ds_read_b64_tr_b16 v[10:11], v35 offset:33032\n"
"v_mfma_f32_16x16x32_bf16 v[72:75], v[0:3], a[216:219], v[72:75]\n"
"ds_read_b64_tr_b16 v[12:13], v35 offset:36872\n"
"v_mfma_f32_16x16x32_bf16 v[72:75], v[4:7], a[220:223], v[72:75]\n"
"ds_read_b64_tr_b16 v[14:15], v35 offset:37128\n"
"v_mfma_f32_16x16x32_bf16 v[80:83], v[0:3], a[224:227], v[80:83]\n"
"v_mfma_f32_16x16x32_bf16 v[80:83], v[4:7], a[228:231], v[80:83]\n"
"v_mfma_f32_16x16x32_bf16 v[88:91], v[0:3], a[232:235], v[88:91]\n"
"v_mfma_f32_16x16x32_bf16 v[88:91], v[4:7], a[236:239], v[88:91]\n"
"buffer_load_dwordx4 v26, s[40:43], s65 offen offset:2048 lds\n"
"v_mfma_f32_16x16x32_bf16 v[96:99], v[0:3], a[240:243], v[96:99]\n"
"v_mfma_f32_16x16x32_bf16 v[96:99], v[4:7], a[244:247], v[96:99]\n"
"buffer_load_dwordx4 v27, s[40:43], s65 offen offset:3072 lds\n"
"v_mfma_f32_16x16x32_bf16 v[104:107], v[0:3], a[248:251], v[104:107]\n"
"v_mfma_f32_16x16x32_bf16 v[104:107], v[4:7], a[252:255], v[104:107]\n"
"s_waitcnt lgkmcnt(0)\n"
"v_mfma_f32_16x16x32_bf16 v[68:71], v[8:11], a[208:211], v[68:71]\n"
"ds_read_b64_tr_b16 v[0:1], v35 offset:33280\n"
"v_mfma_f32_16x16x32_bf16 v[68:71], v[12:15], a[212:215], v[68:71]\n"
"ds_read_b64_tr_b16 v[2:3], v35 offset:33536\n"
"v_mfma_f32_16x16x32_bf16 v[76:79], v[8:11], a[216:219], v[76:79]\n"
"ds_read_b64_tr_b16 v[4:5], v35 offset:37376\n"
"v_mfma_f32_16x16x32_bf16 v[76:79], v[12:15], a[220:223], v[76:79]\n"
"ds_read_b64_tr_b16 v[6:7], v35 offset:37632\n"
"v_mfma_f32_16x16x32_bf16 v[84:87], v[8:11], a[224:227], v[84:87]\n"
"v_mfma_f32_16x16x32_bf16 v[84:87], v[12:15], a[228:231], v[84:87]\n"
"v_mfma_f32_16x16x32_bf16 v[92:95], v[8:11], a[232:235], v[92:95]\n"
"s_add_u32 s66, s80, 1920\n"
"v_mfma_f32_16x16x32_bf16 v[92:95], v[12:15], a[236:239], v[92:95]\n"
"s_add_u32 m0, s67, 24576\n"
"s_and_b32 s66, s66, 0x7ff\n"
"v_mfma_f32_16x16x32_bf16 v[100:103], v[8:11], a[240:243], v[100:103]\n"
"buffer_load_dwordx4 v28, s[36:39], s66 offen offset:0 lds\n"
"v_mfma_f32_16x16x32_bf16 v[100:103], v[12:15], a[244:247], v[100:103]\n"
"v_mfma_f32_16x16x32_bf16 v[108:111], v[8:11], a[248:251], v[108:111]\n"
"buffer_load_dwordx4 v29, s[36:39], s66 offen offset:1024 lds\n"
"v_mfma_f32_16x16x32_bf16 v[108:111], v[12:15], a[252:255], v[108:111]\n"
"s_waitcnt lgkmcnt(0)\n"
"v_mfma_f32_16x16x32_bf16 v[112:115], v[0:3], a[208:211], v[112:115]\n"
"ds_read_b64_tr_b16 v[8:9], v35 offset:33288\n"
"v_mfma_f32_16x16x32_bf16 v[112:115], v[4:7], a[212:215], v[112:115]\n"
"ds_read_b64_tr_b16 v[10:11], v35 offset:33544\n"
"v_mfma_f32_16x16x32_bf16 v[120:123], v[0:3], a[216:219], v[120:123]\n"
"ds_read_b64_tr_b16 v[12:13], v35 offset:37384\n"
"v_mfma_f32_16x16x32_bf16 v[120:123], v[4:7], a[220:223], v[120:123]\n"
"ds_read_b64_tr_b16 v[14:15], v35 offset:37640\n"
"v_mfma_f32_16x16x32_bf16 v[128:131], v[0:3], a[224:227], v[128:131]\n"
"v_mfma_f32_16x16x32_bf16 v[128:131], v[4:7], a[228:231], v[128:131]\n"
"v_mfma_f32_16x16x32_bf16 v[136:139], v[0:3], a[232:235], v[136:139]\n"
"v_mfma_f32_16x16x32_bf16 v[136:139], v[4:7], a[236:239], v[136:139]\n"
"v_mfma_f32_16x16x32_bf16 v[144:147], v[0:3], a[240:243], v[144:147]\n"
"buffer_load_dwordx4 v30, s[36:39], s66 offen offset:2048 lds\n"
"v_mfma_f32_16x16x32_bf16 v[144:147], v[4:7], a[244:247], v[144:147]\n"
"v_mfma_f32_16x16x32_bf16 v[152:155], v[0:3], a[248:251], v[152:155]\n"
"v_mfma_f32_16x16x32_bf16 v[152:155], v[4:7], a[252:255], v[152:155]\n"
"s_waitcnt vmcnt(29) lgkmcnt(0)\n"
"s_barrier\n"
"v_mfma_f32_16x16x32_bf16 v[116:119], v[8:11], a[208:211], v[116:119]\n"
"ds_read_b64_tr_b16 v[0:1], v35 offset:0\n"
"v_mfma_f32_16x16x32_bf16 v[116:119], v[12:15], a[212:215], v[116:119]\n"
"ds_read_b64_tr_b16 v[2:3], v35 offset:256\n"
"v_mfma_f32_16x16x32_bf16 v[124:127], v[8:11], a[216:219], v[124:127]\n"
"ds_read_b64_tr_b16 v[4:5], v35 offset:4096\n"
"v_mfma_f32_16x16x32_bf16 v[124:127], v[12:15], a[220:223], v[124:127]\n"
"ds_read_b64_tr_b16 v[6:7], v35 offset:4352\n"
"v_mfma_f32_16x16x32_bf16 v[132:135], v[8:11], a[224:227], v[132:135]\n"
"ds_read_b128 a[160:163], v34 offset:49152\n"
"v_mfma_f32_16x16x32_bf16 v[132:135], v[12:15], a[228:231], v[132:135]\n"
"s_add_u32 s65, s81, 7340160\n"
"ds_read_b128 a[164:167], v34 offset:49664\n"
"v_mfma_f32_16x16x32_bf16 v[140:143], v[8:11], a[232:235], v[140:143]\n"
"s_add_u32 m0, s68, 32768\n"
"s_and_b32 s65, s65, 0x7fffff\n"
"ds_read_b128 a[168:171], v34 offset:51200\n"
"v_mfma_f32_16x16x32_bf16 v[140:143], v[12:15], a[236:239], v[140:143]\n"
"buffer_load_dwordx4 v24, s[40:43], s65 offen offset:0 lds\n"
"v_mfma_f32_16x16x32_bf16 v[148:151], v[8:11], a[240:243], v[148:151]\n"
"v_mfma_f32_16x16x32_bf16 v[148:151], v[12:15], a[244:247], v[148:151]\n"
"buffer_load_dwordx4 v25, s[40:43], s65 offen offset:1024 lds\n"
"v_mfma_f32_16x16x32_bf16 v[156:159], v[8:11], a[248:251], v[156:159]\n"
"v_mfma_f32_16x16x32_bf16 v[156:159], v[12:15], a[252:255], v[156:159]\n"
"s_waitcnt lgkmcnt(3)\n"
"v_mfma_f32_16x16x32_bf16 v[160:163], v[0:3], a[208:211], v[160:163]\n"
"ds_read_b64_tr_b16 v[8:9], v35 offset:8\n"
"v_mfma_f32_16x16x32_bf16 v[160:163], v[4:7], a[212:215], v[160:163]\n"
"ds_read_b64_tr_b16 v[10:11], v35 offset:264\n"
"v_mfma_f32_16x16x32_bf16 v[168:171], v[0:3], a[216:219], v[168:171]\n"
"ds_read_b64_tr_b16 v[12:13], v35 offset:4104\n"
"v_mfma_f32_16x16x32_bf16 v[168:171], v[4:7], a[220:223], v[168:171]\n"
"ds_read_b64_tr_b16 v[14:15], v35 offset:4360\n"
"v_mfma_f32_16x16x32_bf16 v[176:179], v[0:3], a[224:227], v[176:179]\n"
"ds_read_b128 a[172:175], v34 offset:51712\n"
"v_mfma_f32_16x16x32_bf16 v[176:179], v[4:7], a[228:231], v[176:179]\n"
"ds_read_b128 a[176:179], v34 offset:53248\n"
"v_mfma_f32_16x16x32_bf16 v[184:187], v[0:3], a[232:235], v[184:187]\n"
"ds_read_b128 a[180:183], v34 offset:53760\n"
"v_mfma_f32_16x16x32_bf16 v[184:187], v[4:7], a[236:239], v[184:187]\n"
"buffer_load_dwordx4 v26, s[40:43], s65 offen offset:2048 lds\n"
"v_mfma_f32_16x16x32_bf16 v[192:195], v[0:3], a[240:243], v[192:195]\n"
"v_mfma_f32_16x16x32_bf16 v[192:195], v[4:7], a[244:247], v[192:195]\n"
"buffer_load_dwordx4 v27, s[40:43], s65 offen offset:3072 lds\n"
"v_mfma_f32_16x16x32_bf16 v[200:203], v[0:3], a[248:251], v[200:203]\n"
"v_mfma_f32_16x16x32_bf16 v[200:203], v[4:7], a[252:255], v[200:203]\n"
"s_waitcnt lgkmcnt(3)\n"
"v_mfma_f32_16x16x32_bf16 v[164:167], v[8:11], a[208:211], v[164:167]\n"
"ds_read_b64_tr_b16 v[0:1], v35 offset:512\n"
"v_mfma_f32_16x16x32_bf16 v[164:167], v[12:15], a[212:215], v[164:167]\n"
"ds_read_b64_tr_b16 v[2:3], v35 offset:768\n"
"v_mfma_f32_16x16x32_bf16 v[172:175], v[8:11], a[216:219], v[172:175]\n"
"ds_read_b64_tr_b16 v[4:5], v35 offset:4608\n"
"v_mfma_f32_16x16x32_bf16 v[172:175], v[12:15], a[220:223], v[172:175]\n"
"ds_read_b64_tr_b16 v[6:7], v35 offset:4864\n"
"v_mfma_f32_16x16x32_bf16 v[180:183], v[8:11], a[224:227], v[180:183]\n"
"ds_read_b128 a[184:187], v34 offset:55296\n"
"v_mfma_f32_16x16x32_bf16 v[180:183], v[12:15], a[228:231], v[180:183]\n"
"ds_read_b128 a[188:191], v34 offset:55808\n"
"v_mfma_f32_16x16x32_bf16 v[188:191], v[8:11], a[232:235], v[188:191]\n"
"s_add_u32 s66, s80, 1920\n"
"ds_read_b128 a[192:195], v34 offset:57344\n"
"v_mfma_f32_16x16x32_bf16 v[188:191], v[12:15], a[236:239], v[188:191]\n"
"s_add_u32 m0, s67, 27648\n"
"s_and_b32 s66, s66, 0x7ff\n"
"v_mfma_f32_16x16x32_bf16 v[196:199], v[8:11], a[240:243], v[196:199]\n"
"buffer_load_dwordx4 v31, s[36:39], s66 offen offset:0 lds\n"
"v_mfma_f32_16x16x32_bf16 v[196:199], v[12:15], a[244:247], v[196:199]\n"
"v_mfma_f32_16x16x32_bf16 v[204:207], v[8:11], a[248:251], v[204:207]\n"
"buffer_load_dwordx4 v32, s[36:39], s66 offen offset:1024 lds\n"
"v_mfma_f32_16x16x32_bf16 v[204:207], v[12:15], a[252:255], v[204:207]\n"
"s_waitcnt lgkmcnt(3)\n"
"v_mfma_f32_16x16x32_bf16 v[208:211], v[0:3], a[208:211], v[208:211]\n"
"ds_read_b64_tr_b16 v[8:9], v35 offset:520\n"
"v_mfma_f32_16x16x32_bf16 v[208:211], v[4:7], a[212:215], v[208:211]\n"
"ds_read_b64_tr_b16 v[10:11], v35 offset:776\n"
"v_mfma_f32_16x16x32_bf16 v[216:219], v[0:3], a[216:219], v[216:219]\n"
"ds_read_b64_tr_b16 v[12:13], v35 offset:4616\n"
"v_mfma_f32_16x16x32_bf16 v[216:219], v[4:7], a[220:223], v[216:219]\n"
"ds_read_b64_tr_b16 v[14:15], v35 offset:4872\n"
"v_mfma_f32_16x16x32_bf16 v[224:227], v[0:3], a[224:227], v[224:227]\n"
"ds_read_b128 a[196:199], v34 offset:57856\n"
"v_mfma_f32_16x16x32_bf16 v[224:227], v[4:7], a[228:231], v[224:227]\n"
"ds_read_b128 a[200:203], v34 offset:59392\n"
"v_mfma_f32_16x16x32_bf16 v[232:235], v[0:3], a[232:235], v[232:235]\n"
"ds_read_b128 a[204:207], v34 offset:59904\n"
"v_mfma_f32_16x16x32_bf16 v[232:235], v[4:7], a[236:239], v[232:235]\n"
"v_mfma_f32_16x16x32_bf16 v[240:243], v[0:3], a[240:243], v[240:243]\n"
"buffer_load_dwordx4 v33, s[36:39], s66 offen offset:2048 lds\n"
"v_mfma_f32_16x16x32_bf16 v[240:243], v[4:7], a[244:247], v[240:243]\n"
"v_mfma_f32_16x16x32_bf16 v[248:251], v[0:3], a[248:251], v[248:251]\n"
"v_mfma_f32_16x16x32_bf16 v[248:251], v[4:7], a[252:255], v[248:251]\n"
"s_bitcmp1_b32 2, s94\n"
"s_cbranch_scc0 K1_W11\n"
"s_waitcnt vmcnt(24)\n"
"K1_W11:\n"
"s_waitcnt vmcnt(32) lgkmcnt(0)\n"
"s_barrier\n"
"K1_Q26:\n"
"v_mfma_f32_16x16x32_bf16 v[212:215], v[8:11], a[208:211], v[212:215]\n"
"ds_read_b64_tr_b16 v[0:1], v35 offset:8192\n"
"v_mfma_f32_16x16x32_bf16 v[212:215], v[12:15], a[212:215], v[212:215]\n"
"ds_read_b64_tr_b16 v[2:3], v35 offset:8448\n"
"v_mfma_f32_16x16x32_bf16 v[220:223], v[8:11], a[216:219], v[220:223]\n"
"ds_read_b64_tr_b16 v[4:5], v35 offset:12288\n"
"v_mfma_f32_16x16x32_bf16 v[220:223], v[12:15], a[220:223], v[220:223]\n"
"ds_read_b64_tr_b16 v[6:7], v35 offset:12544\n"
"v_mfma_f32_16x16x32_bf16 v[228:231], v[8:11], a[224:227], v[228:231]\n"
"v_mfma_f32_16x16x32_bf16 v[228:231], v[12:15], a[228:231], v[228:231]\n"
"s_add_u32 s65, s81, 7864320\n"
"s_lshl_b32 s93, s52, 7\n"
"v_lshl_add_u32 v52, v57, 9, s93\n"
"v_min_u32 v52, K1_LOOPE-K1_ATT-4, v52\n"
"v_add_u32 v52, K1_ATT-K1_PC, v52\n"
"global_load_dword v37, v52, s[72:73]\n"
"v_mfma_f32_16x16x32_bf16 v[236:239], v[8:11], a[232:235], v[236:239]\n"
"s_add_u32 m0, s68, 0\n"
"s_and_b32 s65, s65, 0x7fffff\n"
"v_mfma_f32_16x16x32_bf16 v[236:239], v[12:15], a[236:239], v[236:239]\n"
"buffer_load_dwordx4 v24, s[40:43], s65 offen offset:0 lds\n"
"v_mfma_f32_16x16x32_bf16 v[244:247], v[8:11], a[240:243], v[244:247]\n"
"v_mfma_f32_16x16x32_bf16 v[244:247], v[12:15], a[244:247], v[244:247]\n"
"buffer_load_dwordx4 v25, s[40:43], s65 offen offset:1024 lds\n"
"v_mfma_f32_16x16x32_bf16 v[252:255], v[8:11], a[248:251], v[252:255]\n"
"v_mfma_f32_16x16x32_bf16 v[252:255], v[12:15], a[252:255], v[252:255]\n"
"s_waitcnt lgkmcnt(0)\n"
"v_mfma_f32_16x16x32_bf16 v[64:67], v[0:3], a[160:163], v[64:67]\n"
"ds_read_b64_tr_b16 v[8:9], v35 offset:8200\n"
"v_mfma_f32_16x16x32_bf16 v[64:67], v[4:7], a[164:167], v[64:67]\n"
"ds_read_b64_tr_b16 v[10:11], v35 offset:8456\n"
"v_mfma_f32_16x16x32_bf16 v[72:75], v[0:3], a[168:171], v[72:75]\n"
"ds_read_b64_tr_b16 v[12:13], v35 offset:12296\n"
"v_mfma_f32_16x16x32_bf16 v[72:75], v[4:7], a[172:175], v[72:75]\n"
"ds_read_b64_tr_b16 v[14:15], v35 offset:12552\n"
"v_mfma_f32_16x16x32_bf16 v[80:83], v[0:3], a[176:179], v[80:83]\n"
"v_mfma_f32_16x16x32_bf16 v[80:83], v[4:7], a[180:183], v[80:83]\n"
"v_mfma_f32_16x16x32_bf16 v[88:91], v[0:3], a[184:187], v[88:91]\n"
"v_mfma_f32_16x16x32_bf16 v[88:91], v[4:7], a[188:191], v[88:91]\n"
"v_mfma_f32_16x16x32_bf16 v[96:99], v[0:3], a[192:195], v[96:99]\n"
"buffer_load_dwordx4 v26, s[40:43], s65 offen offset:2048 lds\n"
"v_mfma_f32_16x16x32_bf16 v[96:99], v[4:7], a[196:199], v[96:99]\n"
"v_mfma_f32_16x16x32_bf16 v[104:107], v[0:3], a[200:203], v[104:107]\n"
"v_mfma_f32_16x16x32_bf16 v[104:107], v[4:7], a[204:207], v[104:107]\n"
"s_waitcnt lgkmcnt(0)\n"
"v_mfma_f32_16x16x32_bf16 v[68:71], v[8:11], a[160:163], v[68:71]\n"
"ds_read_b64_tr_b16 v[0:1], v35 offset:8704\n"
"v_mfma_f32_16x16x32_bf16 v[68:71], v[12:15], a[164:167], v[68:71]\n"
"ds_read_b64_tr_b16 v[2:3], v35 offset:8960\n"
"v_mfma_f32_16x16x32_bf16 v[76:79], v[8:11], a[168:171], v[76:79]\n"
"ds_read_b64_tr_b16 v[4:5], v35 offset:12800\n"
"v_mfma_f32_16x16x32_bf16 v[76:79], v[12:15], a[172:175], v[76:79]\n"
"ds_read_b64_tr_b16 v[6:7], v35 offset:13056\n"
"v_mfma_f32_16x16x32_bf16 v[84:87], v[8:11], a[176:179], v[84:87]\n"
"v_mfma_f32_16x16x32_bf16 v[84:87], v[12:15], a[180:183], v[84:87]\n"
"v_mfma_f32_16x16x32_bf16 v[92:95], v[8:11], a[184:187], v[92:95]\n"
"v_mfma_f32_16x16x32_bf16 v[92:95], v[12:15], a[188:191], v[92:95]\n"
"buffer_load_dwordx4 v27, s[40:43], s65 offen offset:3072 lds\n"
"v_mfma_f32_16x16x32_bf16 v[100:103], v[8:11], a[192:195], v[100:103]\n"
"v_mfma_f32_16x16x32_bf16 v[100:103], v[12:15], a[196:199], v[100:103]\n"
"s_cmp_eq_u32 s94, 1\n"
"s_cselect_b64 vcc, -1, 0\n"
"s_add_u32 s56, s82, 32768\n"
"s_and_b32 s56, s56, 0x3ffff\n"
"s_cbranch_vccz K1_KS4_1_0\n"
"buffer_load_dwordx4 a[128:131], v44, s[48:51], s56 offen offset:0\n"
"buffer_load_dwordx4 a[132:135], v45, s[48:51], s56 offen offset:1024\n"
"buffer_load_dwordx4 a[136:139], v46, s[48:51], s56 offen offset:2048\n"
"buffer_load_dwordx4 a[140:143], v47, s[48:51], s56 offen offset:3072\n"
"K1_KS4_1_0:\n"
"v_mfma_f32_16x16x32_bf16 v[108:111], v[8:11], a[200:203], v[108:111]\n"
"v_mfma_f32_16x16x32_bf16 v[108:111], v[12:15], a[204:207], v[108:111]\n"
"s_waitcnt lgkmcnt(0)\n"
"v_mfma_f32_16x16x32_bf16 v[112:115], v[0:3], a[160:163], v[112:115]\n"
"ds_read_b64_tr_b16 v[8:9], v35 offset:8712\n"
"v_mfma_f32_16x16x32_bf16 v[112:115], v[4:7], a[164:167], v[112:115]\n"
"ds_read_b64_tr_b16 v[10:11], v35 offset:8968\n"
"v_mfma_f32_16x16x32_bf16 v[120:123], v[0:3], a[168:171], v[120:123]\n"
"ds_read_b64_tr_b16 v[12:13], v35 offset:12808\n"
"v_mfma_f32_16x16x32_bf16 v[120:123], v[4:7], a[172:175], v[120:123]\n"
"ds_read_b64_tr_b16 v[14:15], v35 offset:13064\n"
"v_mfma_f32_16x16x32_bf16 v[128:131], v[0:3], a[176:179], v[128:131]\n"
"v_mfma_f32_16x16x32_bf16 v[128:131], v[4:7], a[180:183], v[128:131]\n"
"v_mfma_f32_16x16x32_bf16 v[136:139], v[0:3], a[184:187], v[136:139]\n"
"v_mfma_f32_16x16x32_bf16 v[136:139], v[4:7], a[188:191], v[136:139]\n"
"v_mfma_f32_16x16x32_bf16 v[144:147], v[0:3], a[192:195], v[144:147]\n"
"s_add_u32 s56, s82, 36864\n"
"s_and_b32 s56, s56, 0x3ffff\n"
"s_cbranch_vccz K1_KS4_1_4\n"
"buffer_load_dwordx4 a[144:147], v44, s[48:51], s56 offen offset:0\n"
"buffer_load_dwordx4 a[148:151], v45, s[48:51], s56 offen offset:1024\n"
"buffer_load_dwordx4 a[152:155], v46, s[48:51], s56 offen offset:2048\n"
"buffer_load_dwordx4 a[156:159], v47, s[48:51], s56 offen offset:3072\n"
"K1_KS4_1_4:\n"
"v_mfma_f32_16x16x32_bf16 v[144:147], v[4:7], a[196:199], v[144:147]\n"
"v_mfma_f32_16x16x32_bf16 v[152:155], v[0:3], a[200:203], v[152:155]\n"
"v_mfma_f32_16x16x32_bf16 v[152:155], v[4:7], a[204:207], v[152:155]\n"
"s_waitcnt vmcnt(27) lgkmcnt(0)\n"
"s_barrier\n"
"v_mfma_f32_16x16x32_bf16 v[116:119], v[8:11], a[160:163], v[116:119]\n"
"ds_read_b64_tr_b16 v[0:1], v35 offset:16384\n"
"v_mfma_f32_16x16x32_bf16 v[116:119], v[12:15], a[164:167], v[116:119]\n"
"ds_read_b64_tr_b16 v[2:3], v35 offset:16640\n"
"v_mfma_f32_16x16x32_bf16 v[124:127], v[8:11], a[168:171], v[124:127]\n"
"ds_read_b64_tr_b16 v[4:5], v35 offset:20480\n"
"v_mfma_f32_16x16x32_bf16 v[124:127], v[12:15], a[172:175], v[124:127]\n"
"ds_read_b64_tr_b16 v[6:7], v35 offset:20736\n"
"v_mfma_f32_16x16x32_bf16 v[132:135], v[8:11], a[176:179], v[132:135]\n"
"ds_read_b128 a[208:211], v34 offset:0\n"
"v_mfma_f32_16x16x32_bf16 v[132:135], v[12:15], a[180:183], v[132:135]\n"
"s_add_u32 s65, s81, 7864448\n"
"ds_read_b128 a[212:215], v34 offset:512\n"
"v_mfma_f32_16x16x32_bf16 v[140:143], v[8:11], a[184:187], v[140:143]\n"
"s_add_u32 m0, s68, 8192\n"
"s_and_b32 s65, s65, 0x7fffff\n"
"ds_read_b128 a[216:219], v34 offset:2048\n"
"v_mfma_f32_16x16x32_bf16 v[140:143], v[12:15], a[188:191], v[140:143]\n"
"buffer_load_dwordx4 v24, s[40:43], s65 offen offset:0 lds\n"
"v_mfma_f32_16x16x32_bf16 v[148:151], v[8:11], a[192:195], v[148:151]\n"
"v_mfma_f32_16x16x32_bf16 v[148:151], v[12:15], a[196:199], v[148:151]\n"
"v_mfma_f32_16x16x32_bf16 v[156:159], v[8:11], a[200:203], v[156:159]\n"
"v_mfma_f32_16x16x32_bf16 v[156:159], v[12:15], a[204:207], v[156:159]\n"
"s_waitcnt lgkmcnt(3)\n"
"v_mfma_f32_16x16x32_bf16 v[160:163], v[0:3], a[160:163], v[160:163]\n"
"ds_read_b64_tr_b16 v[8:9], v35 offset:16392\n"
"v_mfma_f32_16x16x32_bf16 v[160:163], v[4:7], a[164:167], v[160:163]\n"
"ds_read_b64_tr_b16 v[10:11], v35 offset:16648\n"
"v_mfma_f32_16x16x32_bf16 v[168:171], v[0:3], a[168:171], v[168:171]\n"
"ds_read_b64_tr_b16 v[12:13], v35 offset:20488\n"
"v_mfma_f32_16x16x32_bf16 v[168:171], v[4:7], a[172:175], v[168:171]\n"
"ds_read_b64_tr_b16 v[14:15], v35 offset:20744\n"
"v_mfma_f32_16x16x32_bf16 v[176:179], v[0:3], a[176:179], v[176:179]\n"
"ds_read_b128 a[220:223], v34 offset:2560\n"
"v_mfma_f32_16x16x32_bf16 v[176:179], v[4:7], a[180:183], v[176:179]\n"
"ds_read_b128 a[224:227], v34 offset:4096\n"
"v_mfma_f32_16x16x32_bf16 v[184:187], v[0:3], a[184:187], v[184:187]\n"
"ds_read_b128 a[228:231], v34 offset:4608\n"
"v_mfma_f32_16x16x32_bf16 v[184:187], v[4:7], a[188:191], v[184:187]\n"
"buffer_load_dwordx4 v25, s[40:43], s65 offen offset:1024 lds\n"
"v_mfma_f32_16x16x32_bf16 v[192:195], v[0:3], a[192:195], v[192:195]\n"
"v_mfma_f32_16x16x32_bf16 v[192:195], v[4:7], a[196:199], v[192:195]\n"
"v_mfma_f32_16x16x32_bf16 v[200:203], v[0:3], a[200:203], v[200:203]\n"
"v_mfma_f32_16x16x32_bf16 v[200:203], v[4:7], a[204:207], v[200:203]\n"
"s_waitcnt lgkmcnt(3)\n"
"v_mfma_f32_16x16x32_bf16 v[164:167], v[8:11], a[160:163], v[164:167]\n"
"ds_read_b64_tr_b16 v[0:1], v35 offset:16896\n"
"v_mfma_f32_16x16x32_bf16 v[164:167], v[12:15], a[164:167], v[164:167]\n"
"ds_read_b64_tr_b16 v[2:3], v35 offset:17152\n"
"v_mfma_f32_16x16x32_bf16 v[172:175], v[8:11], a[168:171], v[172:175]\n"
"ds_read_b64_tr_b16 v[4:5], v35 offset:20992\n"
"v_mfma_f32_16x16x32_bf16 v[172:175], v[12:15], a[172:175], v[172:175]\n"
"ds_read_b64_tr_b16 v[6:7], v35 offset:21248\n"
"v_mfma_f32_16x16x32_bf16 v[180:183], v[8:11], a[176:179], v[180:183]\n"
"ds_read_b128 a[232:235], v34 offset:6144\n"
"v_mfma_f32_16x16x32_bf16 v[180:183], v[12:15], a[180:183], v[180:183]\n"
"ds_read_b128 a[236:239], v34 offset:6656\n"
"v_mfma_f32_16x16x32_bf16 v[188:191], v[8:11], a[184:187], v[188:191]\n"
"ds_read_b128 a[240:243], v34 offset:8192\n"
"v_mfma_f32_16x16x32_bf16 v[188:191], v[12:15], a[188:191], v[188:191]\n"
"buffer_load_dwordx4 v26, s[40:43], s65 offen offset:2048 lds\n"
"v_mfma_f32_16x16x32_bf16 v[196:199], v[8:11], a[192:195], v[196:199]\n"
"v_mfma_f32_16x16x32_bf16 v[196:199], v[12:15], a[196:199], v[196:199]\n"
"v_mfma_f32_16x16x32_bf16 v[204:207], v[8:11], a[200:203], v[204:207]\n"
"v_mfma_f32_16x16x32_bf16 v[204:207], v[12:15], a[204:207], v[204:207]\n"
"s_waitcnt lgkmcnt(3)\n"
"v_mfma_f32_16x16x32_bf16 v[208:211], v[0:3], a[160:163], v[208:211]\n"
"ds_read_b64_tr_b16 v[8:9], v35 offset:16904\n"
"v_mfma_f32_16x16x32_bf16 v[208:211], v[4:7], a[164:167], v[208:211]\n"
"ds_read_b64_tr_b16 v[10:11], v35 offset:17160\n"
"v_mfma_f32_16x16x32_bf16 v[216:219], v[0:3], a[168:171], v[216:219]\n"
"ds_read_b64_tr_b16 v[12:13], v35 offset:21000\n"
"v_mfma_f32_16x16x32_bf16 v[216:219], v[4:7], a[172:175], v[216:219]\n"
"ds_read_b64_tr_b16 v[14:15], v35 offset:21256\n"
"v_mfma_f32_16x16x32_bf16 v[224:227], v[0:3], a[176:179], v[224:227]\n"
"ds_read_b128 a[244:247], v34 offset:8704\n"
"v_mfma_f32_16x16x32_bf16 v[224:227], v[4:7], a[180:183], v[224:227]\n"
"ds_read_b128 a[248:251], v34 offset:10240\n"
"v_mfma_f32_16x16x32_bf16 v[232:235], v[0:3], a[184:187], v[232:235]\n"
"ds_read_b128 a[252:255], v34 offset:10752\n"
"v_mfma_f32_16x16x32_bf16 v[232:235], v[4:7], a[188:191], v[232:235]\n"
"buffer_load_dwordx4 v27, s[40:43], s65 offen offset:3072 lds\n"
"v_mfma_f32_16x16x32_bf16 v[240:243], v[0:3], a[192:195], v[240:243]\n"
"v_mfma_f32_16x16x32_bf16 v[240:243], v[4:7], a[196:199], v[240:243]\n"
"v_mfma_f32_16x16x32_bf16 v[248:251], v[0:3], a[200:203], v[248:251]\n"
"v_mfma_f32_16x16x32_bf16 v[248:251], v[4:7], a[204:207], v[248:251]\n"
"s_bitcmp1_b32 1, s94\n"
"s_cbranch_scc0 K1_W12\n"
"s_waitcnt vmcnt(19)\n"
"K1_W12:\n"
"s_waitcnt vmcnt(27) lgkmcnt(0)\n"
"s_barrier\n"
"v_mfma_f32_16x16x32_bf16 v[212:215], v[8:11], a[160:163], v[212:215]\n"
"ds_read_b64_tr_b16 v[0:1], v35 offset:24576\n"
"v_mfma_f32_16x16x32_bf16 v[212:215], v[12:15], a[164:167], v[212:215]\n"
"ds_read_b64_tr_b16 v[2:3], v35 offset:24832\n"
"v_mfma_f32_16x16x32_bf16 v[220:223], v[8:11], a[168:171], v[220:223]\n"
"ds_read_b64_tr_b16 v[4:5], v35 offset:28672\n"
"v_mfma_f32_16x16x32_bf16 v[220:223], v[12:15], a[172:175], v[220:223]\n"
"ds_read_b64_tr_b16 v[6:7], v35 offset:28928\n"
"v_mfma_f32_16x16x32_bf16 v[228:231], v[8:11], a[176:179], v[228:231]\n"
"s_waitcnt vmcnt(63)\n"
"ds_write_b128 v23, a[0:3] offset:16384\n"
"v_mfma_f32_16x16x32_bf16 v[228:231], v[12:15], a[180:183], v[228:231]\n"
"v_mfma_f32_16x16x32_bf16 v[236:239], v[8:11], a[184:187], v[236:239]\n"
"v_mfma_f32_16x16x32_bf16 v[236:239], v[12:15], a[188:191], v[236:239]\n"
"v_mfma_f32_16x16x32_bf16 v[244:247], v[8:11], a[192:195], v[244:247]\n"
"ds_write_b128 v23, a[4:7] offset:17408\n"
"v_mfma_f32_16x16x32_bf16 v[244:247], v[12:15], a[196:199], v[244:247]\n"
"v_mfma_f32_16x16x32_bf16 v[252:255], v[8:11], a[200:203], v[252:255]\n"
"v_mfma_f32_16x16x32_bf16 v[252:255], v[12:15], a[204:207], v[252:255]\n"
"s_waitcnt lgkmcnt(2)\n"
"v_mfma_f32_16x16x32_bf16 v[64:67], v[0:3], a[208:211], v[64:67]\n"
"ds_read_b64_tr_b16 v[8:9], v35 offset:24584\n"
"v_mfma_f32_16x16x32_bf16 v[64:67], v[4:7], a[212:215], v[64:67]\n"
"ds_read_b64_tr_b16 v[10:11], v35 offset:24840\n"
"v_mfma_f32_16x16x32_bf16 v[72:75], v[0:3], a[216:219], v[72:75]\n"
"ds_read_b64_tr_b16 v[12:13], v35 offset:28680\n"
"v_mfma_f32_16x16x32_bf16 v[72:75], v[4:7], a[220:223], v[72:75]\n"
"ds_read_b64_tr_b16 v[14:15], v35 offset:28936\n"
"v_mfma_f32_16x16x32_bf16 v[80:83], v[0:3], a[224:227], v[80:83]\n"
"ds_write_b128 v23, a[8:11] offset:18432\n"
"v_mfma_f32_16x16x32_bf16 v[80:83], v[4:7], a[228:231], v[80:83]\n"
"s_add_u32 s93, s95, 40960\n"
"v_lshl_add_u32 v24, v57, 7, s93\n"
"v_mfma_f32_16x16x32_bf16 v[88:91], v[0:3], a[232:235], v[88:91]\n"
"v_mfma_f32_16x16x32_bf16 v[88:91], v[4:7], a[236:239], v[88:91]\n"
"v_and_b32 v24, 0x3ffff, v24\n"
"v_mfma_f32_16x16x32_bf16 v[96:99], v[0:3], a[240:243], v[96:99]\n"
"ds_write_b128 v23, a[12:15] offset:19456\n"
"v_mfma_f32_16x16x32_bf16 v[96:99], v[4:7], a[244:247], v[96:99]\n"
"buffer_load_dword v55, v24, s[48:51], 0 offen offset:1024\n"
"v_mfma_f32_16x16x32_bf16 v[104:107], v[0:3], a[248:251], v[104:107]\n"
"v_mfma_f32_16x16x32_bf16 v[104:107], v[4:7], a[252:255], v[104:107]\n"
"s_waitcnt lgkmcnt(2)\n"
"v_mfma_f32_16x16x32_bf16 v[68:71], v[8:11], a[208:211], v[68:71]\n"
"ds_read_b64_tr_b16 v[0:1], v35 offset:25088\n"
"v_mfma_f32_16x16x32_bf16 v[68:71], v[12:15], a[212:215], v[68:71]\n"
"ds_read_b64_tr_b16 v[2:3], v35 offset:25344\n"
"v_mfma_f32_16x16x32_bf16 v[76:79], v[8:11], a[216:219], v[76:79]\n"
"ds_read_b64_tr_b16 v[4:5], v35 offset:29184\n"
"v_mfma_f32_16x16x32_bf16 v[76:79], v[12:15], a[220:223], v[76:79]\n"
"ds_read_b64_tr_b16 v[6:7], v35 offset:29440\n"
"v_mfma_f32_16x16x32_bf16 v[84:87], v[8:11], a[224:227], v[84:87]\n"
"ds_write_b128 v23, a[16:19] offset:20480\n"
"v_mfma_f32_16x16x32_bf16 v[84:87], v[12:15], a[228:231], v[84:87]\n"
"v_mfma_f32_16x16x32_bf16 v[92:95], v[8:11], a[232:235], v[92:95]\n"
"v_mfma_f32_16x16x32_bf16 v[92:95], v[12:15], a[236:239], v[92:95]\n"
"v_mfma_f32_16x16x32_bf16 v[100:103], v[8:11], a[240:243], v[100:103]\n"
"ds_write_b128 v23, a[20:23] offset:21504\n"
"v_mfma_f32_16x16x32_bf16 v[100:103], v[12:15], a[244:247], v[100:103]\n"
"v_mfma_f32_16x16x32_bf16 v[108:111], v[8:11], a[248:251], v[108:111]\n"
"v_mfma_f32_16x16x32_bf16 v[108:111], v[12:15], a[252:255], v[108:111]\n"
"s_waitcnt lgkmcnt(2)\n"
"v_mfma_f32_16x16x32_bf16 v[112:115], v[0:3], a[208:211], v[112:115]\n"
"ds_read_b64_tr_b16 v[8:9], v35 offset:25096\n"
"v_mfma_f32_16x16x32_bf16 v[112:115], v[4:7], a[212:215], v[112:115]\n"
"ds_read_b64_tr_b16 v[10:11], v35 offset:25352\n"
"v_mfma_f32_16x16x32_bf16 v[120:123], v[0:3], a[216:219], v[120:123]\n"
"ds_read_b64_tr_b16 v[12:13], v35 offset:29192\n"
"v_mfma_f32_16x16x32_bf16 v[120:123], v[4:7], a[220:223], v[120:123]\n"
"ds_read_b64_tr_b16 v[14:15], v35 offset:29448\n"
"v_mfma_f32_16x16x32_bf16 v[128:131], v[0:3], a[224:227], v[128:131]\n"
"ds_write_b128 v23, a[24:27] offset:22528\n"
"v_mfma_f32_16x16x32_bf16 v[128:131], v[4:7], a[228:231], v[128:131]\n"
"v_mfma_f32_16x16x32_bf16 v[136:139], v[0:3], a[232:235], v[136:139]\n"
"v_mfma_f32_16x16x32_bf16 v[136:139], v[4:7], a[236:239], v[136:139]\n"
"v_mfma_f32_16x16x32_bf16 v[144:147], v[0:3], a[240:243], v[144:147]\n"
"ds_write_b128 v23, a[28:31] offset:23552\n"
"v_mfma_f32_16x16x32_bf16 v[144:147], v[4:7], a[244:247], v[144:147]\n"
"v_mfma_f32_16x16x32_bf16 v[152:155], v[0:3], a[248:251], v[152:155]\n"
"v_mfma_f32_16x16x32_bf16 v[152:155], v[4:7], a[252:255], v[152:155]\n"
"s_bitcmp1_b32 1, s94\n"
"s_cbranch_scc0 K1_W13\n"
"s_waitcnt vmcnt(10)\n"
"K1_W13:\n"
"s_waitcnt vmcnt(18) lgkmcnt(0)\n"
"s_barrier\n"
"v_mfma_f32_16x16x32_bf16 v[116:119], v[8:11], a[208:211], v[116:119]\n"
"ds_read_b64_tr_b16 v[0:1], v35 offset:32768\n"
"v_mfma_f32_16x16x32_bf16 v[116:119], v[12:15], a[212:215], v[116:119]\n"
"ds_read_b64_tr_b16 v[2:3], v35 offset:33024\n"
"v_mfma_f32_16x16x32_bf16 v[124:127], v[8:11], a[216:219], v[124:127]\n"
"ds_read_b64_tr_b16 v[4:5], v35 offset:36864\n"
"v_mfma_f32_16x16x32_bf16 v[124:127], v[12:15], a[220:223], v[124:127]\n"
"ds_read_b64_tr_b16 v[6:7], v35 offset:37120\n"
"v_mfma_f32_16x16x32_bf16 v[132:135], v[8:11], a[224:227], v[132:135]\n"
"ds_read_b128 a[160:163], v34 offset:24576\n"
"v_mfma_f32_16x16x32_bf16 v[132:135], v[12:15], a[228:231], v[132:135]\n"
"ds_read_b128 a[164:167], v34 offset:25088\n"
"v_mfma_f32_16x16x32_bf16 v[140:143], v[8:11], a[232:235], v[140:143]\n"
"ds_read_b128 a[168:171], v34 offset:26624\n"
"v_mfma_f32_16x16x32_bf16 v[140:143], v[12:15], a[236:239], v[140:143]\n"
"v_mfma_f32_16x16x32_bf16 v[148:151], v[8:11], a[240:243], v[148:151]\n"
"v_mfma_f32_16x16x32_bf16 v[148:151], v[12:15], a[244:247], v[148:151]\n"
"v_mfma_f32_16x16x32_bf16 v[156:159], v[8:11], a[248:251], v[156:159]\n"
"v_mfma_f32_16x16x32_bf16 v[156:159], v[12:15], a[252:255], v[156:159]\n"
"s_waitcnt lgkmcnt(3)\n"
"v_mfma_f32_16x16x32_bf16 v[160:163], v[0:3], a[208:211], v[160:163]\n"
"ds_read_b64_tr_b16 v[8:9], v35 offset:32776\n"
"v_mfma_f32_16x16x32_bf16 v[160:163], v[4:7], a[212:215], v[160:163]\n"
"ds_read_b64_tr_b16 v[10:11], v35 offset:33032\n"
"v_mfma_f32_16x16x32_bf16 v[168:171], v[0:3], a[216:219], v[168:171]\n"
"ds_read_b64_tr_b16 v[12:13], v35 offset:36872\n"
"v_mfma_f32_16x16x32_bf16 v[168:171], v[4:7], a[220:223], v[168:171]\n"
"ds_read_b64_tr_b16 v[14:15], v35 offset:37128\n"
"v_mfma_f32_16x16x32_bf16 v[176:179], v[0:3], a[224:227], v[176:179]\n"
"ds_read_b128 a[172:175], v34 offset:27136\n"
"v_mfma_f32_16x16x32_bf16 v[176:179], v[4:7], a[228:231], v[176:179]\n"
"ds_read_b128 a[176:179], v34 offset:28672\n"
"s_add_u32 s93, s95, 106496\n"
"v_lshl_add_u32 v25, v57, 7, s93\n"
"v_mfma_f32_16x16x32_bf16 v[184:187], v[0:3], a[232:235], v[184:187]\n"
"ds_read_b128 a[180:183], v34 offset:29184\n"
"v_mfma_f32_16x16x32_bf16 v[184:187], v[4:7], a[236:239], v[184:187]\n"
"v_and_b32 v25, 0x3ffff, v25\n"
"v_mfma_f32_16x16x32_bf16 v[192:195], v[0:3], a[240:243], v[192:195]\n"
"v_mfma_f32_16x16x32_bf16 v[192:195], v[4:7], a[244:247], v[192:195]\n"
"buffer_load_dword v55, v25, s[48:51], 0 offen offset:1024\n"
"v_mfma_f32_16x16x32_bf16 v[200:203], v[0:3], a[248:251], v[200:203]\n"
"v_mfma_f32_16x16x32_bf16 v[200:203], v[4:7], a[252:255], v[200:203]\n"
"s_waitcnt lgkmcnt(3)\n"
"v_mfma_f32_16x16x32_bf16 v[164:167], v[8:11], a[208:211], v[164:167]\n"
"ds_read_b64_tr_b16 v[0:1], v35 offset:33280\n"
"v_mfma_f32_16x16x32_bf16 v[164:167], v[12:15], a[212:215], v[164:167]\n"
"ds_read_b64_tr_b16 v[2:3], v35 offset:33536\n"
"v_mfma_f32_16x16x32_bf16 v[172:175], v[8:11], a[216:219], v[172:175]\n"
"ds_read_b64_tr_b16 v[4:5], v35 offset:37376\n"
"v_mfma_f32_16x16x32_bf16 v[172:175], v[12:15], a[220:223], v[172:175]\n"
"ds_read_b64_tr_b16 v[6:7], v35 offset:37632\n"
"v_mfma_f32_16x16x32_bf16 v[180:183], v[8:11], a[224:227], v[180:183]\n"
"ds_read_b128 a[184:187], v34 offset:30720\n"
"v_mfma_f32_16x16x32_bf16 v[180:183], v[12:15], a[228:231], v[180:183]\n"
"ds_read_b128 a[188:191], v34 offset:31232\n"
"v_mfma_f32_16x16x32_bf16 v[188:191], v[8:11], a[232:235], v[188:191]\n"
"ds_read_b128 a[192:195], v34 offset:32768\n"
"v_mfma_f32_16x16x32_bf16 v[188:191], v[12:15], a[236:239], v[188:191]\n"
"v_mfma_f32_16x16x32_bf16 v[196:199], v[8:11], a[240:243], v[196:199]\n"
"v_mfma_f32_16x16x32_bf16 v[196:199], v[12:15], a[244:247], v[196:199]\n"
"v_mfma_f32_16x16x32_bf16 v[204:207], v[8:11], a[248:251], v[204:207]\n"
"v_mfma_f32_16x16x32_bf16 v[204:207], v[12:15], a[252:255], v[204:207]\n"
"s_waitcnt lgkmcnt(3)\n"
"v_mfma_f32_16x16x32_bf16 v[208:211], v[0:3], a[208:211], v[208:211]\n"
"ds_read_b64_tr_b16 v[8:9], v35 offset:33288\n"
"v_mfma_f32_16x16x32_bf16 v[208:211], v[4:7], a[212:215], v[208:211]\n"
"ds_read_b64_tr_b16 v[10:11], v35 offset:33544\n"
"v_mfma_f32_16x16x32_bf16 v[216:219], v[0:3], a[216:219], v[216:219]\n"
"ds_read_b64_tr_b16 v[12:13], v35 offset:37384\n"
"v_mfma_f32_16x16x32_bf16 v[216:219], v[4:7], a[220:223], v[216:219]\n"
"ds_read_b64_tr_b16 v[14:15], v35 offset:37640\n"
"v_mfma_f32_16x16x32_bf16 v[224:227], v[0:3], a[224:227], v[224:227]\n"
"ds_read_b128 a[196:199], v34 offset:33280\n"
"v_mfma_f32_16x16x32_bf16 v[224:227], v[4:7], a[228:231], v[224:227]\n"
"ds_read_b128 a[200:203], v34 offset:34816\n"
"v_mfma_f32_16x16x32_bf16 v[232:235], v[0:3], a[232:235], v[232:235]\n"
"ds_read_b128 a[204:207], v34 offset:35328\n"
"v_mfma_f32_16x16x32_bf16 v[232:235], v[4:7], a[236:239], v[232:235]\n"
"v_mfma_f32_16x16x32_bf16 v[240:243], v[0:3], a[240:243], v[240:243]\n"
"v_mfma_f32_16x16x32_bf16 v[240:243], v[4:7], a[244:247], v[240:243]\n"
"v_mfma_f32_16x16x32_bf16 v[248:251], v[0:3], a[248:251], v[248:251]\n"
"v_mfma_f32_16x16x32_bf16 v[248:251], v[4:7], a[252:255], v[248:251]\n"
"s_bitcmp1_b32 1, s94\n"
"s_cbranch_scc0 K1_W14\n"
"s_waitcnt vmcnt(6)\n"
"K1_W14:\n"
"s_waitcnt vmcnt(14) lgkmcnt(0)\n"
"s_barrier\n"
"v_mfma_f32_16x16x32_bf16 v[212:215], v[8:11], a[208:211], v[212:215]\n"
"ds_read_b64_tr_b16 v[0:1], v35 offset:0\n"
"v_mfma_f32_16x16x32_bf16 v[212:215], v[12:15], a[212:215], v[212:215]\n"
"ds_read_b64_tr_b16 v[2:3], v35 offset:256\n"
"v_mfma_f32_16x16x32_bf16 v[220:223], v[8:11], a[216:219], v[220:223]\n"
"ds_read_b64_tr_b16 v[4:5], v35 offset:4096\n"
"v_mfma_f32_16x16x32_bf16 v[220:223], v[12:15], a[220:223], v[220:223]\n"
"ds_read_b64_tr_b16 v[6:7], v35 offset:4352\n"
"v_mfma_f32_16x16x32_bf16 v[228:231], v[8:11], a[224:227], v[228:231]\n"
"s_waitcnt vmcnt(63)\n"
"ds_write_b128 v23, a[32:35] offset:24576\n"
"v_mfma_f32_16x16x32_bf16 v[228:231], v[12:15], a[228:231], v[228:231]\n"
"v_mfma_f32_16x16x32_bf16 v[236:239], v[8:11], a[232:235], v[236:239]\n"
"ds_write_b128 v23, a[36:39] offset:25600\n"
"v_mfma_f32_16x16x32_bf16 v[236:239], v[12:15], a[236:239], v[236:239]\n"
"v_mfma_f32_16x16x32_bf16 v[244:247], v[8:11], a[240:243], v[244:247]\n"
"ds_write_b128 v23, a[40:43] offset:26624\n"
"v_mfma_f32_16x16x32_bf16 v[244:247], v[12:15], a[244:247], v[244:247]\n"
"v_mfma_f32_16x16x32_bf16 v[252:255], v[8:11], a[248:251], v[252:255]\n"
"ds_write_b128 v23, a[44:47] offset:27648\n"
"v_mfma_f32_16x16x32_bf16 v[252:255], v[12:15], a[252:255], v[252:255]\n"
"s_waitcnt lgkmcnt(4)\n"
"v_mfma_f32_16x16x32_bf16 v[64:67], v[0:3], a[160:163], v[64:67]\n"
"ds_read_b64_tr_b16 v[8:9], v35 offset:8\n"
"v_mfma_f32_16x16x32_bf16 v[64:67], v[4:7], a[164:167], v[64:67]\n"
"ds_read_b64_tr_b16 v[10:11], v35 offset:264\n"
"v_mfma_f32_16x16x32_bf16 v[72:75], v[0:3], a[168:171], v[72:75]\n"
"ds_read_b64_tr_b16 v[12:13], v35 offset:4104\n"
"v_mfma_f32_16x16x32_bf16 v[72:75], v[4:7], a[172:175], v[72:75]\n"
"ds_read_b64_tr_b16 v[14:15], v35 offset:4360\n"
"v_mfma_f32_16x16x32_bf16 v[80:83], v[0:3], a[176:179], v[80:83]\n"
"ds_write_b128 v23, a[48:51] offset:28672\n"
"v_mfma_f32_16x16x32_bf16 v[80:83], v[4:7], a[180:183], v[80:83]\n"
"s_add_u32 s93, s95, 172032\n"
"v_lshl_add_u32 v26, v57, 7, s93\n"
"v_mfma_f32_16x16x32_bf16 v[88:91], v[0:3], a[184:187], v[88:91]\n"
"ds_write_b128 v23, a[52:55] offset:29696\n"
"v_mfma_f32_16x16x32_bf16 v[88:91], v[4:7], a[188:191], v[88:91]\n"
"v_and_b32 v26, 0x3ffff, v26\n"
"v_mfma_f32_16x16x32_bf16 v[96:99], v[0:3], a[192:195], v[96:99]\n"
"ds_write_b128 v23, a[56:59] offset:30720\n"
"v_mfma_f32_16x16x32_bf16 v[96:99], v[4:7], a[196:199], v[96:99]\n"
"buffer_load_dword v55, v26, s[48:51], 0 offen offset:1024\n"
"v_mfma_f32_16x16x32_bf16 v[104:107], v[0:3], a[200:203], v[104:107]\n"
"ds_write_b128 v23, a[60:63] offset:31744\n"
"v_mfma_f32_16x16x32_bf16 v[104:107], v[4:7], a[204:207], v[104:107]\n"
"s_waitcnt lgkmcnt(4)\n"
"v_mfma_f32_16x16x32_bf16 v[68:71], v[8:11], a[160:163], v[68:71]\n"
"ds_read_b64_tr_b16 v[0:1], v35 offset:512\n"
"v_mfma_f32_16x16x32_bf16 v[68:71], v[12:15], a[164:167], v[68:71]\n"
"ds_read_b64_tr_b16 v[2:3], v35 offset:768\n"
"v_mfma_f32_16x16x32_bf16 v[76:79], v[8:11], a[168:171], v[76:79]\n"
"ds_read_b64_tr_b16 v[4:5], v35 offset:4608\n"
"v_mfma_f32_16x16x32_bf16 v[76:79], v[12:15], a[172:175], v[76:79]\n"
"ds_read_b64_tr_b16 v[6:7], v35 offset:4864\n"
"v_mfma_f32_16x16x32_bf16 v[84:87], v[8:11], a[176:179], v[84:87]\n"
"ds_write_b128 v23, a[64:67] offset:32768\n"
"v_mfma_f32_16x16x32_bf16 v[84:87], v[12:15], a[180:183], v[84:87]\n"
"v_mfma_f32_16x16x32_bf16 v[92:95], v[8:11], a[184:187], v[92:95]\n"
"ds_write_b128 v23, a[68:71] offset:33792\n"
"v_mfma_f32_16x16x32_bf16 v[92:95], v[12:15], a[188:191], v[92:95]\n"
"v_mfma_f32_16x16x32_bf16 v[100:103], v[8:11], a[192:195], v[100:103]\n"
"ds_write_b128 v23, a[72:75] offset:34816\n"
"v_mfma_f32_16x16x32_bf16 v[100:103], v[12:15], a[196:199], v[100:103]\n"
"v_mfma_f32_16x16x32_bf16 v[108:111], v[8:11], a[200:203], v[108:111]\n"
"ds_write_b128 v23, a[76:79] offset:35840\n"
"v_mfma_f32_16x16x32_bf16 v[108:111], v[12:15], a[204:207], v[108:111]\n"
"s_waitcnt lgkmcnt(4)\n"
"v_mfma_f32_16x16x32_bf16 v[112:115], v[0:3], a[160:163], v[112:115]\n"
"ds_read_b64_tr_b16 v[8:9], v35 offset:520\n"
"v_mfma_f32_16x16x32_bf16 v[112:115], v[4:7], a[164:167], v[112:115]\n"
"ds_read_b64_tr_b16 v[10:11], v35 offset:776\n"
"v_mfma_f32_16x16x32_bf16 v[120:123], v[0:3], a[168:171], v[120:123]\n"
"ds_read_b64_tr_b16 v[12:13], v35 offset:4616\n"
"v_mul_f32 v64, s54, v64\n"
"v_mfma_f32_16x16x32_bf16 v[120:123], v[4:7], a[172:175], v[120:123]\n"
"ds_read_b64_tr_b16 v[14:15], v35 offset:4872\n"
"v_mul_f32 v65, s54, v65\n"
"v_mfma_f32_16x16x32_bf16 v[128:131], v[0:3], a[176:179], v[128:131]\n"
"ds_write_b128 v23, a[80:83] offset:36864\n"
"v_mul_f32 v66, s54, v66\n"
"v_mfma_f32_16x16x32_bf16 v[128:131], v[4:7], a[180:183], v[128:131]\n"
"v_mul_f32 v67, s54, v67\n"
"v_mul_f32 v68, s54, v68\n"
"v_mfma_f32_16x16x32_bf16 v[136:139], v[0:3], a[184:187], v[136:139]\n"
"ds_write_b128 v23, a[84:87] offset:37888\n"
"v_mul_f32 v69, s54, v69\n"
"v_mfma_f32_16x16x32_bf16 v[136:139], v[4:7], a[188:191], v[136:139]\n"
"v_mul_f32 v70, s54, v70\n"
"v_mul_f32 v71, s54, v71\n"
"v_mfma_f32_16x16x32_bf16 v[144:147], v[0:3], a[192:195], v[144:147]\n"
"ds_write_b128 v23, a[88:91] offset:38912\n"
"v_cvt_pk_bf16_f32 v64, v64, v65\n"
"v_mfma_f32_16x16x32_bf16 v[144:147], v[4:7], a[196:199], v[144:147]\n"
"v_cvt_pk_bf16_f32 v65, v66, v67\n"
"v_cvt_pk_bf16_f32 v66, v68, v69\n"
"v_mfma_f32_16x16x32_bf16 v[152:155], v[0:3], a[200:203], v[152:155]\n"
"ds_write_b128 v23, a[92:95] offset:39936\n"
"v_cvt_pk_bf16_f32 v67, v70, v71\n"
"v_mfma_f32_16x16x32_bf16 v[152:155], v[4:7], a[204:207], v[152:155]\n"
"v_mul_f32 v72, s54, v72\n"
"v_mul_f32 v73, s54, v73\n"
"s_waitcnt vmcnt(3) lgkmcnt(0)\n"
"s_barrier\n"
"v_mfma_f32_16x16x32_bf16 v[116:119], v[8:11], a[160:163], v[116:119]\n"
"ds_read_b64_tr_b16 v[0:1], v35 offset:8192\n"
"v_mul_f32 v74, s54, v74\n"
"v_mfma_f32_16x16x32_bf16 v[116:119], v[12:15], a[164:167], v[116:119]\n"
"ds_read_b64_tr_b16 v[2:3], v35 offset:8448\n"
"v_mul_f32 v75, s54, v75\n"
"v_mfma_f32_16x16x32_bf16 v[124:127], v[8:11], a[168:171], v[124:127]\n"
"ds_read_b64_tr_b16 v[4:5], v35 offset:12288\n"
"v_mul_f32 v76, s54, v76\n"
"v_mfma_f32_16x16x32_bf16 v[124:127], v[12:15], a[172:175], v[124:127]\n"
"ds_read_b64_tr_b16 v[6:7], v35 offset:12544\n"
"v_mul_f32 v77, s54, v77\n"
"v_mfma_f32_16x16x32_bf16 v[132:135], v[8:11], a[176:179], v[132:135]\n"
"s_bitcmp1_b32 2, s94\n"
"s_cbranch_scc0 K1_W15\n"
"s_waitcnt vmcnt(48)\n"
"K1_W15:\n"
"s_waitcnt vmcnt(63)\n"
"ds_write_b128 v23, a[96:99] offset:0\n"
"v_mul_f32 v78, s54, v78\n"
"v_mfma_f32_16x16x32_bf16 v[132:135], v[12:15], a[180:183], v[132:135]\n"
"v_mul_f32 v79, s54, v79\n"
"v_cvt_pk_bf16_f32 v68, v72, v73\n"
"v_mfma_f32_16x16x32_bf16 v[140:143], v[8:11], a[184:187], v[140:143]\n"
"v_cvt_pk_bf16_f32 v69, v74, v75\n"
"v_cvt_pk_bf16_f32 v70, v76, v77\n"
"v_mfma_f32_16x16x32_bf16 v[140:143], v[12:15], a[188:191], v[140:143]\n"
"v_cvt_pk_bf16_f32 v71, v78, v79\n"
"v_mul_f32 v80, s54, v80\n"
"v_mfma_f32_16x16x32_bf16 v[148:151], v[8:11], a[192:195], v[148:151]\n"
"ds_write_b128 v23, a[100:103] offset:1024\n"
"v_mul_f32 v81, s54, v81\n"
"v_mfma_f32_16x16x32_bf16 v[148:151], v[12:15], a[196:199], v[148:151]\n"
"v_mul_f32 v82, s54, v82\n"
"v_mul_f32 v83, s54, v83\n"
"v_mfma_f32_16x16x32_bf16 v[156:159], v[8:11], a[200:203], v[156:159]\n"
"v_mul_f32 v84, s54, v84\n"
"v_mul_f32 v85, s54, v85\n"
"v_mfma_f32_16x16x32_bf16 v[156:159], v[12:15], a[204:207], v[156:159]\n"
"v_mul_f32 v86, s54, v86\n"
"v_mul_f32 v87, s54, v87\n"
"s_waitcnt lgkmcnt(2)\n"
"v_mfma_f32_16x16x32_bf16 v[160:163], v[0:3], a[160:163], v[160:163]\n"
"ds_read_b64_tr_b16 v[8:9], v35 offset:8200\n"
"v_mfma_f32_16x16x32_bf16 v[160:163], v[4:7], a[164:167], v[160:163]\n"
"ds_read_b64_tr_b16 v[10:11], v35 offset:8456\n"
"v_mfma_f32_16x16x32_bf16 v[168:171], v[0:3], a[168:171], v[168:171]\n"
"ds_read_b64_tr_b16 v[12:13], v35 offset:12296\n"
"v_cvt_pk_bf16_f32 v72, v80, v81\n"
"v_mfma_f32_16x16x32_bf16 v[168:171], v[4:7], a[172:175], v[168:171]\n"
"ds_read_b64_tr_b16 v[14:15], v35 offset:12552\n"
"v_cvt_pk_bf16_f32 v73, v82, v83\n"
"v_mfma_f32_16x16x32_bf16 v[176:179], v[0:3], a[176:179], v[176:179]\n"
"ds_write_b128 v23, a[104:107] offset:2048\n"
"v_cvt_pk_bf16_f32 v74, v84, v85\n"
"v_mfma_f32_16x16x32_bf16 v[176:179], v[4:7], a[180:183], v[176:179]\n"
"v_cvt_pk_bf16_f32 v75, v86, v87\n"
"v_mul_f32 v88, s54, v88\n"
"v_mfma_f32_16x16x32_bf16 v[184:187], v[0:3], a[184:187], v[184:187]\n"
"v_mul_f32 v89, s54, v89\n"
"v_mul_f32 v90, s54, v90\n"
"v_mfma_f32_16x16x32_bf16 v[184:187], v[4:7], a[188:191], v[184:187]\n"
"v_mul_f32 v91, s54, v91\n"
"v_mul_f32 v92, s54, v92\n"
"v_mfma_f32_16x16x32_bf16 v[192:195], v[0:3], a[192:195], v[192:195]\n"
"ds_write_b128 v23, a[108:111] offset:3072\n"
"v_mul_f32 v93, s54, v93\n"
"v_mfma_f32_16x16x32_bf16 v[192:195], v[4:7], a[196:199], v[192:195]\n"
"v_mul_f32 v94, s54, v94\n"
"v_mul_f32 v95, s54, v95\n"
"v_mfma_f32_16x16x32_bf16 v[200:203], v[0:3], a[200:203], v[200:203]\n"
"v_cvt_pk_bf16_f32 v76, v88, v89\n"
"v_cvt_pk_bf16_f32 v77, v90, v91\n"
"v_mfma_f32_16x16x32_bf16 v[200:203], v[4:7], a[204:207], v[200:203]\n"
"v_cvt_pk_bf16_f32 v78, v92, v93\n"
"v_cvt_pk_bf16_f32 v79, v94, v95\n"
"s_waitcnt lgkmcnt(2)\n"
"v_mfma_f32_16x16x32_bf16 v[164:167], v[8:11], a[160:163], v[164:167]\n"
"ds_read_b64_tr_b16 v[0:1], v35 offset:8704\n"
"v_mul_f32 v96, s54, v96\n"
"v_mfma_f32_16x16x32_bf16 v[164:167], v[12:15], a[164:167], v[164:167]\n"
"ds_read_b64_tr_b16 v[2:3], v35 offset:8960\n"
"v_mul_f32 v97, s54, v97\n"
"v_mfma_f32_16x16x32_bf16 v[172:175], v[8:11], a[168:171], v[172:175]\n"
"ds_read_b64_tr_b16 v[4:5], v35 offset:12800\n"
"v_mul_f32 v98, s54, v98\n"
"v_mfma_f32_16x16x32_bf16 v[172:175], v[12:15], a[172:175], v[172:175]\n"
"ds_read_b64_tr_b16 v[6:7], v35 offset:13056\n"
"v_mul_f32 v99, s54, v99\n"
"v_mfma_f32_16x16x32_bf16 v[180:183], v[8:11], a[176:179], v[180:183]\n"
"ds_write_b128 v23, a[112:115] offset:4096\n"
"v_mul_f32 v100, s54, v100\n"
"v_mfma_f32_16x16x32_bf16 v[180:183], v[12:15], a[180:183], v[180:183]\n"
"v_mul_f32 v101, s54, v101\n"
"v_mul_f32 v102, s54, v102\n"
"v_mfma_f32_16x16x32_bf16 v[188:191], v[8:11], a[184:187], v[188:191]\n"
"v_mul_f32 v103, s54, v103\n"
"v_cvt_pk_bf16_f32 v80, v96, v97\n"
"v_mfma_f32_16x16x32_bf16 v[188:191], v[12:15], a[188:191], v[188:191]\n"
"v_cvt_pk_bf16_f32 v81, v98, v99\n"
"v_cvt_pk_bf16_f32 v82, v100, v101\n"
"v_mfma_f32_16x16x32_bf16 v[196:199], v[8:11], a[192:195], v[196:199]\n"
"ds_write_b128 v23, a[116:119] offset:5120\n"
"v_cvt_pk_bf16_f32 v83, v102, v103\n"
"v_mfma_f32_16x16x32_bf16 v[196:199], v[12:15], a[196:199], v[196:199]\n"
"v_mul_f32 v104, s54, v104\n"
"v_mul_f32 v105, s54, v105\n"
"v_mfma_f32_16x16x32_bf16 v[204:207], v[8:11], a[200:203], v[204:207]\n"
"v_mul_f32 v106, s54, v106\n"
"v_mul_f32 v107, s54, v107\n"
"v_mfma_f32_16x16x32_bf16 v[204:207], v[12:15], a[204:207], v[204:207]\n"
"v_mul_f32 v108, s54, v108\n"
"v_mul_f32 v109, s54, v109\n"
"s_waitcnt lgkmcnt(2)\n"
"v_mfma_f32_16x16x32_bf16 v[208:211], v[0:3], a[160:163], v[208:211]\n"
"ds_read_b64_tr_b16 v[8:9], v35 offset:8712\n"
"v_mfma_f32_16x16x32_bf16 v[208:211], v[4:7], a[164:167], v[208:211]\n"
"ds_read_b64_tr_b16 v[10:11], v35 offset:8968\n"
"v_mfma_f32_16x16x32_bf16 v[216:219], v[0:3], a[168:171], v[216:219]\n"
"ds_read_b64_tr_b16 v[12:13], v35 offset:12808\n"
"v_mul_f32 v110, s54, v110\n"
"v_mfma_f32_16x16x32_bf16 v[216:219], v[4:7], a[172:175], v[216:219]\n"
"ds_read_b64_tr_b16 v[14:15], v35 offset:13064\n"
"v_mul_f32 v111, s54, v111\n"
"v_mfma_f32_16x16x32_bf16 v[224:227], v[0:3], a[176:179], v[224:227]\n"
"ds_write_b128 v23, a[120:123] offset:6144\n"
"v_cvt_pk_bf16_f32 v84, v104, v105\n"
"v_mfma_f32_16x16x32_bf16 v[224:227], v[4:7], a[180:183], v[224:227]\n"
"v_cvt_pk_bf16_f32 v85, v106, v107\n"
"v_cvt_pk_bf16_f32 v86, v108, v109\n"
"v_mfma_f32_16x16x32_bf16 v[232:235], v[0:3], a[184:187], v[232:235]\n"
"v_cvt_pk_bf16_f32 v87, v110, v111\n"
"v_mul_f32 v112, s54, v112\n"
"v_mfma_f32_16x16x32_bf16 v[232:235], v[4:7], a[188:191], v[232:235]\n"
"v_mul_f32 v113, s54, v113\n"
"v_mul_f32 v114, s54, v114\n"
"v_mfma_f32_16x16x32_bf16 v[240:243], v[0:3], a[192:195], v[240:243]\n"
"ds_write_b128 v23, a[124:127] offset:7168\n"
"v_mul_f32 v115, s54, v115\n"
"v_mfma_f32_16x16x32_bf16 v[240:243], v[4:7], a[196:199], v[240:243]\n"
"v_mul_f32 v116, s54, v116\n"
"v_mul_f32 v117, s54, v117\n"
"v_mfma_f32_16x16x32_bf16 v[248:251], v[0:3], a[200:203], v[248:251]\n"
"v_mul_f32 v118, s54, v118\n"
"v_mul_f32 v119, s54, v119\n"
"v_mfma_f32_16x16x32_bf16 v[248:251], v[4:7], a[204:207], v[248:251]\n"
"v_cvt_pk_bf16_f32 v88, v112, v113\n"
"v_cvt_pk_bf16_f32 v89, v114, v115\n"
"s_waitcnt vmcnt(3) lgkmcnt(0)\n"
"s_barrier\n"
"v_mfma_f32_16x16x32_bf16 v[212:215], v[8:11], a[160:163], v[212:215]\n"
"ds_read_b128 a[224:227], v38 offset:16384\n"
"v_cvt_pk_bf16_f32 v90, v116, v117\n"
"v_mfma_f32_16x16x32_bf16 v[212:215], v[12:15], a[164:167], v[212:215]\n"
"ds_read_b128 a[228:231], v38 offset:16896\n"
"v_cvt_pk_bf16_f32 v91, v118, v119\n"
"v_mfma_f32_16x16x32_bf16 v[220:223], v[8:11], a[168:171], v[220:223]\n"
"ds_read_b128 a[232:235], v38 offset:17408\n"
"v_mul_f32 v120, s54, v120\n"
"v_mfma_f32_16x16x32_bf16 v[220:223], v[12:15], a[172:175], v[220:223]\n"
"ds_read_b128 a[236:239], v38 offset:17920\n"
"v_mul_f32 v121, s54, v121\n"
"v_mfma_f32_16x16x32_bf16 v[228:231], v[8:11], a[176:179], v[228:231]\n"
"ds_read_b128 a[240:243], v38 offset:20480\n"
"v_mul_f32 v122, s54, v122\n"
"v_mfma_f32_16x16x32_bf16 v[228:231], v[12:15], a[180:183], v[228:231]\n"
"ds_read_b128 a[244:247], v38 offset:20992\n"
"v_mul_f32 v123, s54, v123\n"
"v_mfma_f32_16x16x32_bf16 v[236:239], v[8:11], a[184:187], v[236:239]\n"
"ds_read_b128 a[248:251], v38 offset:21504\n"
"v_mul_f32 v124, s54, v124\n"
"v_mfma_f32_16x16x32_bf16 v[236:239], v[12:15], a[188:191], v[236:239]\n"
"ds_read_b128 a[252:255], v38 offset:22016\n"
"v_mul_f32 v125, s54, v125\n"
"v_mfma_f32_16x16x32_bf16 v[244:247], v[8:11], a[192:195], v[244:247]\n"
"v_mul_f32 v126, s54, v126\n"
"v_mul_f32 v127, s54, v127\n"
"v_mfma_f32_16x16x32_bf16 v[244:247], v[12:15], a[196:199], v[244:247]\n"
"v_cvt_pk_bf16_f32 v92, v120, v121\n"
"v_cvt_pk_bf16_f32 v93, v122, v123\n"
"v_mfma_f32_16x16x32_bf16 v[252:255], v[8:11], a[200:203], v[252:255]\n"
"v_cvt_pk_bf16_f32 v94, v124, v125\n"
"v_cvt_pk_bf16_f32 v95, v126, v127\n"
"v_mfma_f32_16x16x32_bf16 v[252:255], v[12:15], a[204:207], v[252:255]\n"
"v_mul_f32 v128, s54, v128\n"
"v_mul_f32 v129, s54, v129\n"
"K1_ATT:\n"
"ds_read_b64_tr_b16 a[192:193], v39 offset:16384\n"
"ds_read_b64_tr_b16 a[194:195], v39 offset:20480\n"
"ds_read_b64_tr_b16 a[196:197], v40 offset:16384\n"
"ds_read_b64_tr_b16 a[198:199], v40 offset:20480\n"
"ds_read_b64_tr_b16 a[200:201], v39 offset:16896\n"
"ds_read_b64_tr_b16 a[202:203], v39 offset:20992\n"
"ds_read_b64_tr_b16 a[204:205], v40 offset:16896\n"
"ds_read_b64_tr_b16 a[206:207], v40 offset:20992\n"
"ds_read_b64_tr_b16 a[208:209], v39 offset:17408\n"
"ds_read_b64_tr_b16 a[210:211], v39 offset:21504\n"
"ds_read_b64_tr_b16 a[212:213], v40 offset:17408\n"
"ds_read_b64_tr_b16 a[214:215], v40 offset:21504\n"
"ds_read_b64_tr_b16 a[216:217], v39 offset:17920\n"
"ds_read_b64_tr_b16 a[218:219], v39 offset:22016\n"
"ds_read_b64_tr_b16 a[220:221], v40 offset:17920\n"
"ds_read_b64_tr_b16 a[222:223], v40 offset:22016\n"
"s_nop 1\n"
"v_mul_f32 v130, s54, v130\n"
"v_mul_f32 v131, s54, v131\n"
"v_mul_f32 v132, s54, v132\n"
"v_mul_f32 v133, s54, v133\n"
"v_mul_f32 v134, s54, v134\n"
"v_mul_f32 v135, s54, v135\n"
"v_cvt_pk_bf16_f32 v96, v128, v129\n"
"v_cvt_pk_bf16_f32 v97, v130, v131\n"
"v_cvt_pk_bf16_f32 v98, v132, v133\n"
"v_cvt_pk_bf16_f32 v99, v134, v135\n"
"v_pk_mul_f32 v[136:137], v[136:137], s[54:55]\n"
"v_pk_mul_f32 v[138:139], v[138:139], s[54:55]\n"
"v_pk_mul_f32 v[140:141], v[140:141], s[54:55]\n"
"v_pk_mul_f32 v[142:143], v[142:143], s[54:55]\n"
"v_cvt_pk_bf16_f32 v100, v136, v137\n"
"v_cvt_pk_bf16_f32 v101, v138, v139\n"
"v_cvt_pk_bf16_f32 v102, v140, v141\n"
"v_cvt_pk_bf16_f32 v103, v142, v143\n"
"v_pk_mul_f32 v[144:145], v[144:145], s[54:55]\n"
"v_pk_mul_f32 v[146:147], v[146:147], s[54:55]\n"
"v_pk_mul_f32 v[148:149], v[148:149], s[54:55]\n"
"v_pk_mul_f32 v[150:151], v[150:151], s[54:55]\n"
"v_cvt_pk_bf16_f32 v104, v144, v145\n"
"v_cvt_pk_bf16_f32 v105, v146, v147\n"
"v_cvt_pk_bf16_f32 v106, v148, v149\n"
"v_cvt_pk_bf16_f32 v107, v150, v151\n"
"v_pk_mul_f32 v[152:153], v[152:153], s[54:55]\n"
"v_pk_mul_f32 v[154:155], v[154:155], s[54:55]\n"
"v_pk_mul_f32 v[156:157], v[156:157], s[54:55]\n"
"v_pk_mul_f32 v[158:159], v[158:159], s[54:55]\n"
"v_cvt_pk_bf16_f32 v108, v152, v153\n"
"v_cvt_pk_bf16_f32 v109, v154, v155\n"
"v_cvt_pk_bf16_f32 v110, v156, v157\n"
"v_cvt_pk_bf16_f32 v111, v158, v159\n"
"v_pk_mul_f32 v[160:161], v[160:161], s[54:55]\n"
"v_pk_mul_f32 v[162:163], v[162:163], s[54:55]\n"
"v_pk_mul_f32 v[164:165], v[164:165], s[54:55]\n"
"v_pk_mul_f32 v[166:167], v[166:167], s[54:55]\n"
"v_cvt_pk_bf16_f32 v112, v160, v161\n"
"v_cvt_pk_bf16_f32 v113, v162, v163\n"
"v_cvt_pk_bf16_f32 v114, v164, v165\n"
"v_cvt_pk_bf16_f32 v115, v166, v167\n"
"v_pk_mul_f32 v[168:169], v[168:169], s[54:55]\n"
"v_pk_mul_f32 v[170:171], v[170:171], s[54:55]\n"
"v_pk_mul_f32 v[172:173], v[172:173], s[54:55]\n"
"v_pk_mul_f32 v[174:175], v[174:175], s[54:55]\n"
"v_cvt_pk_bf16_f32 v116, v168, v169\n"
"v_cvt_pk_bf16_f32 v117, v170, v171\n"
"v_cvt_pk_bf16_f32 v118, v172, v173\n"
"v_cvt_pk_bf16_f32 v119, v174, v175\n"
"v_pk_mul_f32 v[176:177], v[176:177], s[54:55]\n"
"v_pk_mul_f32 v[178:179], v[178:179], s[54:55]\n"
"v_pk_mul_f32 v[180:181], v[180:181], s[54:55]\n"
"v_pk_mul_f32 v[182:183], v[182:183], s[54:55]\n"
"v_cvt_pk_bf16_f32 v120, v176, v177\n"
"v_cvt_pk_bf16_f32 v121, v178, v179\n"
"v_cvt_pk_bf16_f32 v122, v180, v181\n"
"v_cvt_pk_bf16_f32 v123, v182, v183\n"
"v_pk_mul_f32 v[184:185], v[184:185], s[54:55]\n"
"v_pk_mul_f32 v[186:187], v[186:187], s[54:55]\n"
"v_pk_mul_f32 v[188:189], v[188:189], s[54:55]\n"
"v_pk_mul_f32 v[190:191], v[190:191], s[54:55]\n"
"v_cvt_pk_bf16_f32 v124, v184, v185\n"
"v_cvt_pk_bf16_f32 v125, v186, v187\n"
"v_cvt_pk_bf16_f32 v126, v188, v189\n"
"v_cvt_pk_bf16_f32 v127, v190, v191\n"
"v_pk_mul_f32 v[192:193], v[192:193], s[54:55]\n"
"v_pk_mul_f32 v[194:195], v[194:195], s[54:55]\n"
"v_pk_mul_f32 v[196:197], v[196:197], s[54:55]\n"
"v_pk_mul_f32 v[198:199], v[198:199], s[54:55]\n"
"v_cvt_pk_bf16_f32 v128, v192, v193\n"
"v_cvt_pk_bf16_f32 v129, v194, v195\n"
"v_cvt_pk_bf16_f32 v130, v196, v197\n"
"v_cvt_pk_bf16_f32 v131, v198, v199\n"
"v_pk_mul_f32 v[200:201], v[200:201], s[54:55]\n"
"v_pk_mul_f32 v[202:203], v[202:203], s[54:55]\n"
"v_pk_mul_f32 v[204:205], v[204:205], s[54:55]\n"
"v_pk_mul_f32 v[206:207], v[206:207], s[54:55]\n"
"v_cvt_pk_bf16_f32 v132, v200, v201\n"
"v_cvt_pk_bf16_f32 v133, v202, v203\n"
"v_cvt_pk_bf16_f32 v134, v204, v205\n"
"v_cvt_pk_bf16_f32 v135, v206, v207\n"
"v_pk_mul_f32 v[208:209], v[208:209], s[54:55]\n"
"v_pk_mul_f32 v[210:211], v[210:211], s[54:55]\n"
"v_pk_mul_f32 v[212:213], v[212:213], s[54:55]\n"
"v_pk_mul_f32 v[214:215], v[214:215], s[54:55]\n"
"v_cvt_pk_bf16_f32 v136, v208, v209\n"
"v_cvt_pk_bf16_f32 v137, v210, v211\n"
"v_cvt_pk_bf16_f32 v138, v212, v213\n"
"v_cvt_pk_bf16_f32 v139, v214, v215\n"
"v_pk_mul_f32 v[216:217], v[216:217], s[54:55]\n"
"v_pk_mul_f32 v[218:219], v[218:219], s[54:55]\n"
"v_pk_mul_f32 v[220:221], v[220:221], s[54:55]\n"
"v_pk_mul_f32 v[222:223], v[222:223], s[54:55]\n"
"v_cvt_pk_bf16_f32 v140, v216, v217\n"
"v_cvt_pk_bf16_f32 v141, v218, v219\n"
"v_cvt_pk_bf16_f32 v142, v220, v221\n"
"v_cvt_pk_bf16_f32 v143, v222, v223\n"
"s_waitcnt lgkmcnt(15)\n"
"v_mfma_f32_16x16x32_bf16 v[160:163], a[224:227], v[64:67], 0\n"
"v_mul_f32 v224, s54, v224\n"
"v_mul_f32 v225, s54, v225\n"
"v_mul_f32 v226, s54, v226\n"
"v_mfma_f32_16x16x32_bf16 v[160:163], a[228:231], v[88:91], v[160:163]\n"
"v_mul_f32 v227, s54, v227\n"
"v_mul_f32 v228, s54, v228\n"
"v_mul_f32 v229, s54, v229\n"
"v_mfma_f32_16x16x32_bf16 v[160:163], a[232:235], v[112:115], v[160:163]\n"
"v_mul_f32 v230, s54, v230\n"
"v_mul_f32 v231, s54, v231\n"
"v_cvt_pk_bf16_f32 v144, v224, v225\n"
"v_mfma_f32_16x16x32_bf16 v[160:163], a[236:239], v[136:139], v[160:163]\n"
"v_cvt_pk_bf16_f32 v145, v226, v227\n"
"v_cvt_pk_bf16_f32 v146, v228, v229\n"
"v_cvt_pk_bf16_f32 v147, v230, v231\n"
"v_mfma_f32_16x16x32_bf16 v[164:167], a[240:243], v[64:67], 0\n"
"v_mul_f32 v232, s54, v232\n"
"v_mul_f32 v233, s54, v233\n"
"v_mul_f32 v234, s54, v234\n"
"v_mul_f32 v235, s54, v235\n"
"v_mfma_f32_16x16x32_bf16 v[164:167], a[244:247], v[88:91], v[164:167]\n"
"v_mul_f32 v236, s54, v236\n"
"v_mul_f32 v237, s54, v237\n"
"v_mul_f32 v238, s54, v238\n"
"v_mfma_f32_16x16x32_bf16 v[164:167], a[248:251], v[112:115], v[164:167]\n"
"v_mul_f32 v239, s54, v239\n"
"v_cvt_pk_bf16_f32 v148, v232, v233\n"
"v_cvt_pk_bf16_f32 v149, v234, v235\n"
"v_mfma_f32_16x16x32_bf16 v[164:167], a[252:255], v[136:139], v[164:167]\n"
"v_cvt_pk_bf16_f32 v150, v236, v237\n"
"v_cvt_pk_bf16_f32 v151, v238, v239\n"
"v_mul_f32 v240, s54, v240\n"
"v_mfma_f32_16x16x32_bf16 v[168:171], a[224:227], v[68:71], 0\n"
"v_mul_f32 v241, s54, v241\n"
"v_mul_f32 v242, s54, v242\n"
"v_mul_f32 v243, s54, v243\n"
"v_mfma_f32_16x16x32_bf16 v[168:171], a[228:231], v[92:95], v[168:171]\n"
"v_mul_f32 v244, s54, v244\n"
"v_mul_f32 v245, s54, v245\n"
"v_mul_f32 v246, s54, v246\n"
"v_mfma_f32_16x16x32_bf16 v[168:171], a[232:235], v[116:119], v[168:171]\n"
"v_mul_f32 v247, s54, v247\n"
"v_cvt_pk_bf16_f32 v152, v240, v241\n"
"v_cvt_pk_bf16_f32 v153, v242, v243\n"
"v_mfma_f32_16x16x32_bf16 v[168:171], a[236:239], v[140:143], v[168:171]\n"
"v_cvt_pk_bf16_f32 v154, v244, v245\n"
"v_cvt_pk_bf16_f32 v155, v246, v247\n"
"v_mul_f32 v248, s54, v248\n"
"v_mfma_f32_16x16x32_bf16 v[172:175], a[240:243], v[68:71], 0\n"
"v_mul_f32 v249, s54, v249\n"
"v_mul_f32 v250, s54, v250\n"
"v_mul_f32 v251, s54, v251\n"
"v_mfma_f32_16x16x32_bf16 v[172:175], a[244:247], v[92:95], v[172:175]\n"
"v_mul_f32 v252, s54, v252\n"
"v_mul_f32 v253, s54, v253\n"
"v_mul_f32 v254, s54, v254\n"
"v_mul_f32 v255, s54, v255\n"
"v_mfma_f32_16x16x32_bf16 v[172:175], a[248:251], v[116:119], v[172:175]\n"
"s_bitcmp1_b32 2, s94\n"
"s_cbranch_scc0 K1_W16\n"
"s_waitcnt vmcnt(7)\n"
"K1_W16:\n"
"s_waitcnt vmcnt(26)\n"
"ds_write_b128 v23, a[128:131] offset:8192\n"
"v_cvt_pk_bf16_f32 v156, v248, v249\n"
"v_cvt_pk_bf16_f32 v157, v250, v251\n"
"v_mfma_f32_16x16x32_bf16 v[172:175], a[252:255], v[140:143], v[172:175]\n"
"v_cvt_pk_bf16_f32 v158, v252, v253\n"
"v_cvt_pk_bf16_f32 v159, v254, v255\n"
"v_mov_b32 v32, 0\n"
"v_mfma_f32_16x16x32_bf16 v[176:179], a[224:227], v[72:75], 0\n"
"v_mov_b32 v33, 0\n"
"v_mov_b32 v34, 0\n"
"v_mov_b32 v35, 0\n"
"v_mfma_f32_16x16x32_bf16 v[176:179], a[228:231], v[96:99], v[176:179]\n"
"ds_write_b128 v23, a[132:135] offset:9216\n"
"v_mov_b32 v36, 0\n"
"v_mov_b32 v37, 0\n"
"v_mfma_f32_16x16x32_bf16 v[176:179], a[232:235], v[120:123], v[176:179]\n"
"v_max3_f32 v48, v160, v161, v162\n"
"v_max3_f32 v50, v168, v169, v170\n"
"v_max3_f32 v48, v48, v163, v164\n"
"v_mfma_f32_16x16x32_bf16 v[176:179], a[236:239], v[144:147], v[176:179]\n"
"v_max3_f32 v50, v50, v171, v172\n"
"v_max3_f32 v48, v48, v165, v166\n"
"v_max3_f32 v50, v50, v173, v174\n"
"v_max_f32 v48, v48, v167\n"
"v_mfma_f32_16x16x32_bf16 v[180:183], a[240:243], v[72:75], 0\n"
"ds_write_b128 v23, a[136:139] offset:10240\n"
"v_max_f32 v50, v50, v175\n"
"v_mov_b32 v49, v48\n"
"v_mfma_f32_16x16x32_bf16 v[180:183], a[244:247], v[96:99], v[180:183]\n"
"v_mov_b32 v51, v50\n"
"s_nop 0\n"
"v_permlane32_swap_b32 v48, v49\n"
"v_mfma_f32_16x16x32_bf16 v[180:183], a[248:251], v[120:123], v[180:183]\n"
"v_permlane32_swap_b32 v50, v51\n"
"v_max_f32 v48, v48, v49\n"
"v_max_f32 v50, v50, v51\n"
"v_mfma_f32_16x16x32_bf16 v[180:183], a[252:255], v[144:147], v[180:183]\n"
"ds_write_b128 v23, a[140:143] offset:11264\n"
"v_mov_b32 v49, v48\n"
"v_mov_b32 v51, v50\n"
"s_nop 0\n"
"v_mfma_f32_16x16x32_bf16 v[184:187], a[224:227], v[76:79], 0\n"
"v_permlane16_swap_b32 v48, v49\n"
"v_permlane16_swap_b32 v50, v51\n"
"v_mfma_f32_16x16x32_bf16 v[184:187], a[228:231], v[100:103], v[184:187]\n"
"v_max_f32 v48, v48, v49\n"
"v_max_f32 v50, v50, v51\n"
"v_sub_f32 v160, v160, v48\n"
"v_sub_f32 v161, v161, v48\n"
"v_mfma_f32_16x16x32_bf16 v[184:187], a[232:235], v[124:127], v[184:187]\n"
"ds_write_b128 v23, a[144:147] offset:12288\n"
"v_sub_f32 v162, v162, v48\n"
"v_sub_f32 v163, v163, v48\n"
"v_mfma_f32_16x16x32_bf16 v[184:187], a[236:239], v[148:151], v[184:187]\n"
"v_sub_f32 v164, v164, v48\n"
"v_sub_f32 v165, v165, v48\n"
"v_sub_f32 v166, v166, v48\n"
"v_sub_f32 v167, v167, v48\n"
"v_mfma_f32_16x16x32_bf16 v[188:191], a[240:243], v[76:79], 0\n"
"v_exp_f32 v160, v160\n"
"v_exp_f32 v161, v161\n"
"v_mfma_f32_16x16x32_bf16 v[188:191], a[244:247], v[100:103], v[188:191]\n"
"ds_write_b128 v23, a[148:151] offset:13312\n"
"v_exp_f32 v162, v162\n"
"v_mfma_f32_16x16x32_bf16 v[188:191], a[248:251], v[124:127], v[188:191]\n"
"v_exp_f32 v163, v163\n"
"v_exp_f32 v164, v164\n"
"v_mfma_f32_16x16x32_bf16 v[188:191], a[252:255], v[148:151], v[188:191]\n"
"v_exp_f32 v165, v165\n"
"v_mfma_f32_16x16x32_bf16 v[192:195], a[224:227], v[80:83], 0\n"
"ds_write_b128 v23, a[152:155] offset:14336\n"
"v_exp_f32 v166, v166\n"
"v_exp_f32 v167, v167\n"
"v_mfma_f32_16x16x32_bf16 v[192:195], a[228:231], v[104:107], v[192:195]\n"
"v_sub_f32 v208, 0, v48\n"
"v_sub_f32 v209, 0, v48\n"
"v_sub_f32 v210, 0, v48\n"
"v_mfma_f32_16x16x32_bf16 v[192:195], a[232:235], v[128:131], v[192:195]\n"
"v_sub_f32 v211, 0, v48\n"
"v_cvt_pk_bf16_f32 v0, v160, v161\n"
"v_cvt_pk_bf16_f32 v1, v162, v163\n"
"v_mfma_f32_16x16x32_bf16 v[192:195], a[236:239], v[152:155], v[192:195]\n"
"ds_write_b128 v23, a[156:159] offset:15360\n"
"v_cvt_pk_bf16_f32 v2, v164, v165\n"
"v_cvt_pk_bf16_f32 v3, v166, v167\n"
"v_sub_f32 v168, v168, v50\n"
"v_mfma_f32_16x16x32_bf16 v[196:199], a[240:243], v[80:83], 0\n"
"v_sub_f32 v169, v169, v50\n"
"v_sub_f32 v170, v170, v50\n"
"v_sub_f32 v171, v171, v50\n"
"v_mfma_f32_16x16x32_bf16 v[196:199], a[244:247], v[104:107], v[196:199]\n"
"v_sub_f32 v172, v172, v50\n"
"v_sub_f32 v173, v173, v50\n"
"v_sub_f32 v174, v174, v50\n"
"v_sub_f32 v175, v175, v50\n"
"v_mfma_f32_16x16x32_bf16 v[196:199], a[248:251], v[128:131], v[196:199]\n"
"v_exp_f32 v168, v168\n"
"v_exp_f32 v169, v169\n"
"v_mfma_f32_16x16x32_bf16 v[196:199], a[252:255], v[152:155], v[196:199]\n"
"v_exp_f32 v170, v170\n"
"v_mfma_f32_16x16x32_bf16 v[200:203], a[224:227], v[84:87], 0\n"
"ds_read_b128 a[224:227], v38 offset:24576\n"
"v_exp_f32 v171, v171\n"
"v_exp_f32 v172, v172\n"
"v_mfma_f32_16x16x32_bf16 v[200:203], a[228:231], v[108:111], v[200:203]\n"
"ds_read_b128 a[228:231], v38 offset:25088\n"
"v_exp_f32 v173, v173\n"
"v_mfma_f32_16x16x32_bf16 v[200:203], a[232:235], v[132:135], v[200:203]\n"
"ds_read_b128 a[232:235], v38 offset:25600\n"
"v_exp_f32 v174, v174\n"
"v_mfma_f32_16x16x32_bf16 v[200:203], a[236:239], v[156:159], v[200:203]\n"
"ds_read_b128 a[236:239], v38 offset:26112\n"
"v_exp_f32 v175, v175\n"
"v_sub_f32 v212, 0, v50\n"
"v_mfma_f32_16x16x32_bf16 v[204:207], a[240:243], v[84:87], 0\n"
"ds_read_b128 a[240:243], v38 offset:28672\n"
"v_sub_f32 v213, 0, v50\n"
"v_sub_f32 v214, 0, v50\n"
"v_mfma_f32_16x16x32_bf16 v[204:207], a[244:247], v[108:111], v[204:207]\n"
"ds_read_b128 a[244:247], v38 offset:29184\n"
"v_sub_f32 v215, 0, v50\n"
"v_cvt_pk_bf16_f32 v4, v168, v169\n"
"v_cvt_pk_bf16_f32 v5, v170, v171\n"
"v_mfma_f32_16x16x32_bf16 v[204:207], a[248:251], v[132:135], v[204:207]\n"
"ds_read_b128 a[248:251], v38 offset:29696\n"
"v_cvt_pk_bf16_f32 v6, v172, v173\n"
"v_cvt_pk_bf16_f32 v7, v174, v175\n"
"v_mfma_f32_16x16x32_bf16 v[204:207], a[252:255], v[156:159], v[204:207]\n"
"ds_read_b128 a[252:255], v38 offset:30208\n"
"s_waitcnt lgkmcnt(8)\n"
"v_mfma_f32_16x16x32_bf16 a[0:3], a[192:195], v[0:3], 0\n"
"v_max3_f32 v48, v176, v177, v178\n"
"v_max3_f32 v50, v184, v185, v186\n"
"v_max3_f32 v48, v48, v179, v180\n"
"v_max3_f32 v50, v50, v187, v188\n"
"v_mfma_f32_16x16x32_bf16 a[32:35], a[192:195], v[4:7], 0\n"
"v_max3_f32 v48, v48, v181, v182\n"
"v_max3_f32 v50, v50, v189, v190\n"
"v_max_f32 v48, v48, v183\n"
"v_mfma_f32_16x16x32_bf16 v[232:235], v[58:61], v[0:3], 0\n"
"v_max_f32 v50, v50, v191\n"
"v_mov_b32 v49, v48\n"
"v_mov_b32 v51, v50\n"
"s_nop 0\n"
"v_permlane32_swap_b32 v48, v49\n"
"v_mfma_f32_16x16x32_bf16 v[236:239], v[58:61], v[4:7], 0\n"
"v_permlane32_swap_b32 v50, v51\n"
"v_max_f32 v48, v48, v49\n"
"v_mfma_f32_16x16x32_bf16 a[4:7], a[196:199], v[0:3], 0\n"
"v_max_f32 v50, v50, v51\n"
"v_mov_b32 v49, v48\n"
"v_mov_b32 v51, v50\n"
"s_nop 0\n"
"v_mfma_f32_16x16x32_bf16 a[36:39], a[196:199], v[4:7], 0\n"
"v_permlane16_swap_b32 v48, v49\n"
"v_permlane16_swap_b32 v50, v51\n"
"v_mfma_f32_16x16x32_bf16 a[8:11], a[200:203], v[0:3], 0\n"
"v_max_f32 v48, v48, v49\n"
"v_max_f32 v50, v50, v51\n"
"v_sub_f32 v176, v176, v48\n"
"v_sub_f32 v177, v177, v48\n"
"v_mfma_f32_16x16x32_bf16 a[40:43], a[200:203], v[4:7], 0\n"
"v_sub_f32 v178, v178, v48\n"
"v_sub_f32 v179, v179, v48\n"
"v_sub_f32 v180, v180, v48\n"
"v_sub_f32 v181, v181, v48\n"
"v_mfma_f32_16x16x32_bf16 a[12:15], a[204:207], v[0:3], 0\n"
"v_sub_f32 v182, v182, v48\n"
"v_sub_f32 v183, v183, v48\n"
"v_exp_f32 v176, v176\n"
"v_mfma_f32_16x16x32_bf16 a[44:47], a[204:207], v[4:7], 0\n"
"v_exp_f32 v177, v177\n"
"v_mfma_f32_16x16x32_bf16 a[16:19], a[208:211], v[0:3], 0\n"
"v_exp_f32 v178, v178\n"
"v_exp_f32 v179, v179\n"
"v_mfma_f32_16x16x32_bf16 a[48:51], a[208:211], v[4:7], 0\n"
"v_exp_f32 v180, v180\n"
"v_exp_f32 v181, v181\n"
"v_mfma_f32_16x16x32_bf16 a[20:23], a[212:215], v[0:3], 0\n"
"v_add_f32 v32, v32, v232\n"
"v_add_f32 v33, v33, v236\n"
"v_exp_f32 v182, v182\n"
"v_mfma_f32_16x16x32_bf16 a[52:55], a[212:215], v[4:7], 0\n"
"v_exp_f32 v183, v183\n"
"v_sub_f32 v216, 0, v48\n"
"v_sub_f32 v217, 0, v48\n"
"v_mfma_f32_16x16x32_bf16 a[24:27], a[216:219], v[0:3], 0\n"
"v_sub_f32 v218, 0, v48\n"
"v_sub_f32 v219, 0, v48\n"
"v_cvt_pk_bf16_f32 v8, v176, v177\n"
"v_mfma_f32_16x16x32_bf16 a[56:59], a[216:219], v[4:7], 0\n"
"v_cvt_pk_bf16_f32 v9, v178, v179\n"
"v_cvt_pk_bf16_f32 v10, v180, v181\n"
"v_cvt_pk_bf16_f32 v11, v182, v183\n"
"v_sub_f32 v184, v184, v50\n"
"v_mfma_f32_16x16x32_bf16 a[28:31], a[220:223], v[0:3], 0\n"
"v_sub_f32 v185, v185, v50\n"
"v_sub_f32 v186, v186, v50\n"
"v_sub_f32 v187, v187, v50\n"
"v_sub_f32 v188, v188, v50\n"
"v_mfma_f32_16x16x32_bf16 a[60:63], a[220:223], v[4:7], 0\n"
"v_sub_f32 v189, v189, v50\n"
"v_sub_f32 v190, v190, v50\n"
"v_sub_f32 v191, v191, v50\n"
"s_waitcnt lgkmcnt(0)\n"
"v_mfma_f32_16x16x32_bf16 v[160:163], a[224:227], v[64:67], v[208:211]\n"
"v_exp_f32 v184, v184\n"
"v_exp_f32 v185, v185\n"
"v_mfma_f32_16x16x32_bf16 v[160:163], a[228:231], v[88:91], v[160:163]\n"
"v_exp_f32 v186, v186\n"
"v_exp_f32 v187, v187\n"
"v_mfma_f32_16x16x32_bf16 v[160:163], a[232:235], v[112:115], v[160:163]\n"
"v_exp_f32 v188, v188\n"
"v_exp_f32 v189, v189\n"
"v_mfma_f32_16x16x32_bf16 v[160:163], a[236:239], v[136:139], v[160:163]\n"
"v_exp_f32 v190, v190\n"
"v_exp_f32 v191, v191\n"
"v_mfma_f32_16x16x32_bf16 v[164:167], a[240:243], v[64:67], v[208:211]\n"
"v_sub_f32 v220, 0, v50\n"
"v_sub_f32 v221, 0, v50\n"
"v_sub_f32 v222, 0, v50\n"
"v_mfma_f32_16x16x32_bf16 v[164:167], a[244:247], v[88:91], v[164:167]\n"
"v_sub_f32 v223, 0, v50\n"
"v_cvt_pk_bf16_f32 v12, v184, v185\n"
"v_cvt_pk_bf16_f32 v13, v186, v187\n"
"v_cvt_pk_bf16_f32 v14, v188, v189\n"
"v_mfma_f32_16x16x32_bf16 v[164:167], a[248:251], v[112:115], v[164:167]\n"
"v_cvt_pk_bf16_f32 v15, v190, v191\n"
"v_max3_f32 v48, v192, v193, v194\n"
"v_max3_f32 v50, v200, v201, v202\n"
"v_mfma_f32_16x16x32_bf16 v[164:167], a[252:255], v[136:139], v[164:167]\n"
"v_max3_f32 v48, v48, v195, v196\n"
"v_max3_f32 v50, v50, v203, v204\n"
"v_max3_f32 v48, v48, v197, v198\n"
"v_max3_f32 v50, v50, v205, v206\n"
"v_mfma_f32_16x16x32_bf16 v[168:171], a[224:227], v[68:71], v[212:215]\n"
"v_max_f32 v48, v48, v199\n"
"v_max_f32 v50, v50, v207\n"
"v_mov_b32 v49, v48\n"
"v_mov_b32 v51, v50\n"
"v_mfma_f32_16x16x32_bf16 v[168:171], a[228:231], v[92:95], v[168:171]\n"
"s_nop 0\n"
"v_permlane32_swap_b32 v48, v49\n"
"v_permlane32_swap_b32 v50, v51\n"
"v_mfma_f32_16x16x32_bf16 v[168:171], a[232:235], v[116:119], v[168:171]\n"
"v_max_f32 v48, v48, v49\n"
"v_max_f32 v50, v50, v51\n"
"v_mov_b32 v49, v48\n"
"v_mfma_f32_16x16x32_bf16 v[168:171], a[236:239], v[140:143], v[168:171]\n"
"v_mov_b32 v51, v50\n"
"s_nop 0\n"
"v_permlane16_swap_b32 v48, v49\n"
"v_mfma_f32_16x16x32_bf16 v[172:175], a[240:243], v[68:71], v[212:215]\n"
"v_permlane16_swap_b32 v50, v51\n"
"v_max_f32 v48, v48, v49\n"
"v_max_f32 v50, v50, v51\n"
"v_sub_f32 v192, v192, v48\n"
"v_mfma_f32_16x16x32_bf16 v[172:175], a[244:247], v[92:95], v[172:175]\n"
"v_sub_f32 v193, v193, v48\n"
"v_sub_f32 v194, v194, v48\n"
"v_sub_f32 v195, v195, v48\n"
"v_mfma_f32_16x16x32_bf16 v[172:175], a[248:251], v[116:119], v[172:175]\n"
"v_sub_f32 v196, v196, v48\n"
"v_sub_f32 v197, v197, v48\n"
"v_sub_f32 v198, v198, v48\n"
"v_sub_f32 v199, v199, v48\n"
"v_mfma_f32_16x16x32_bf16 v[172:175], a[252:255], v[140:143], v[172:175]\n"
"v_mfma_f32_16x16x32_bf16 a[64:67], a[192:195], v[8:11], 0\n"
"v_exp_f32 v192, v192\n"
"v_exp_f32 v193, v193\n"
"v_mfma_f32_16x16x32_bf16 a[96:99], a[192:195], v[12:15], 0\n"
"s_mov_b32 s59, 24576\n"
"v_exp_f32 v24, v160\n"
"v_exp_f32 v194, v194\n"
"v_mfma_f32_16x16x32_bf16 v[240:243], v[58:61], v[8:11], 0\n"
"v_exp_f32 v195, v195\n"
"v_exp_f32 v196, v196\n"
"v_mfma_f32_16x16x32_bf16 v[244:247], v[58:61], v[12:15], 0\n"
"s_mov_b32 s58, 32768\n"
"v_exp_f32 v25, v161\n"
"v_exp_f32 v197, v197\n"
"v_mfma_f32_16x16x32_bf16 a[68:71], a[196:199], v[8:11], 0\n"
"v_cvt_pk_bf16_f32 v0, v24, v25\n"
"v_exp_f32 v198, v198\n"
"v_mfma_f32_16x16x32_bf16 a[100:103], a[196:199], v[12:15], 0\n"
"s_add_u32 s60, s61, 16384\n"
"v_exp_f32 v26, v162\n"
"v_exp_f32 v199, v199\n"
"v_mfma_f32_16x16x32_bf16 a[72:75], a[200:203], v[8:11], 0\n"
"v_sub_f32 v224, 0, v48\n"
"v_sub_f32 v225, 0, v48\n"
"v_sub_f32 v226, 0, v48\n"
"v_sub_f32 v227, 0, v48\n"
"v_mfma_f32_16x16x32_bf16 a[104:107], a[200:203], v[12:15], 0\n"
"s_add_u32 s56, s82, 40960\n"
"v_exp_f32 v27, v163\n"
"v_cvt_pk_bf16_f32 v16, v192, v193\n"
"v_cvt_pk_bf16_f32 v17, v194, v195\n"
"v_mfma_f32_16x16x32_bf16 a[76:79], a[204:207], v[8:11], 0\n"
"v_cvt_pk_bf16_f32 v1, v26, v27\n"
"v_cvt_pk_bf16_f32 v18, v196, v197\n"
"v_cvt_pk_bf16_f32 v19, v198, v199\n"
"v_sub_f32 v200, v200, v50\n"
"v_mfma_f32_16x16x32_bf16 a[108:111], a[204:207], v[12:15], 0\n"
"s_and_b32 s56, s56, 0x3ffff\n"
"v_exp_f32 v28, v164\n"
"v_sub_f32 v201, v201, v50\n"
"v_sub_f32 v202, v202, v50\n"
"v_mfma_f32_16x16x32_bf16 a[80:83], a[208:211], v[8:11], 0\n"
"v_sub_f32 v203, v203, v50\n"
"v_sub_f32 v204, v204, v50\n"
"v_sub_f32 v205, v205, v50\n"
"v_mfma_f32_16x16x32_bf16 a[112:115], a[208:211], v[12:15], 0\n"
"s_add_u32 s57, s82, 45056\n"
"v_exp_f32 v29, v165\n"
"v_sub_f32 v206, v206, v50\n"
"v_sub_f32 v207, v207, v50\n"
"v_mfma_f32_16x16x32_bf16 a[84:87], a[212:215], v[8:11], 0\n"
"v_cvt_pk_bf16_f32 v2, v28, v29\n"
"v_exp_f32 v200, v200\n"
"v_exp_f32 v201, v201\n"
"v_mfma_f32_16x16x32_bf16 a[116:119], a[212:215], v[12:15], 0\n"
"s_and_b32 s57, s57, 0x3ffff\n"
"v_exp_f32 v30, v166\n"
"v_exp_f32 v202, v202\n"
"v_mfma_f32_16x16x32_bf16 a[88:91], a[216:219], v[8:11], 0\n"
"v_add_f32 v34, v34, v240\n"
"v_add_f32 v35, v35, v244\n"
"v_mfma_f32_16x16x32_bf16 a[120:123], a[216:219], v[12:15], 0\n"
"s_mov_b32 s64, 30\n"
"v_exp_f32 v31, v167\n"
"v_exp_f32 v203, v203\n"
"v_mfma_f32_16x16x32_bf16 a[92:95], a[220:223], v[8:11], 0\n"
"v_cvt_pk_bf16_f32 v3, v30, v31\n"
"v_exp_f32 v204, v204\n"
"v_exp_f32 v205, v205\n"
"v_mfma_f32_16x16x32_bf16 a[124:127], a[220:223], v[12:15], 0\n"
"s_mov_b32 s83, 108\n"
"v_exp_f32 v24, v168\n"
"v_exp_f32 v206, v206\n"
"v_mfma_f32_16x16x32_bf16 v[176:179], a[224:227], v[72:75], v[216:219]\n"
"v_exp_f32 v207, v207\n"
"v_sub_f32 v228, 0, v50\n"
"v_mfma_f32_16x16x32_bf16 v[176:179], a[228:231], v[96:99], v[176:179]\n"
"s_memrealtime s[84:85]\n"
"v_exp_f32 v25, v169\n"
"v_sub_f32 v229, 0, v50\n"
"v_sub_f32 v230, 0, v50\n"
"v_mfma_f32_16x16x32_bf16 v[176:179], a[232:235], v[120:123], v[176:179]\n"
"v_cvt_pk_bf16_f32 v4, v24, v25\n"
"v_sub_f32 v231, 0, v50\n"
"v_cvt_pk_bf16_f32 v20, v200, v201\n"
"v_cvt_pk_bf16_f32 v21, v202, v203\n"
"v_mfma_f32_16x16x32_bf16 v[176:179], a[236:239], v[144:147], v[176:179]\n"
"s_nop 0\n"
"v_exp_f32 v26, v170\n"
"v_cvt_pk_bf16_f32 v22, v204, v205\n"
"v_cvt_pk_bf16_f32 v23, v206, v207\n"
"v_mfma_f32_16x16x32_bf16 v[180:183], a[240:243], v[72:75], v[216:219]\n"
"v_mfma_f32_16x16x32_bf16 v[180:183], a[244:247], v[96:99], v[180:183]\n"
"s_nop 0\n"
"v_exp_f32 v27, v171\n"
"v_mfma_f32_16x16x32_bf16 v[248:251], v[58:61], v[16:19], 0\n"
"v_add_u32 v42, s59, v39\n"
"v_add_u32 v43, s59, v40\n"
"v_mfma_f32_16x16x32_bf16 v[252:255], v[58:61], v[20:23], 0\n"
"v_add_u32 v41, s58, v38\n"
"v_mfma_f32_16x16x32_bf16 v[180:183], a[248:251], v[120:123], v[180:183]\n"
"v_cvt_pk_bf16_f32 v5, v26, v27\n"
"v_mfma_f32_16x16x32_bf16 v[180:183], a[252:255], v[144:147], v[180:183]\n"
"s_nop 0\n"
"v_exp_f32 v28, v172\n"
"v_mfma_f32_16x16x32_bf16 v[184:187], a[224:227], v[76:79], v[220:223]\n"
"v_mfma_f32_16x16x32_bf16 v[184:187], a[228:231], v[100:103], v[184:187]\n"
"s_waitcnt lgkmcnt(0)\n"
"v_exp_f32 v29, v173\n"
"v_mfma_f32_16x16x32_bf16 v[184:187], a[232:235], v[124:127], v[184:187]\n"
"v_cvt_pk_bf16_f32 v6, v28, v29\n"
"v_mfma_f32_16x16x32_bf16 v[184:187], a[236:239], v[148:151], v[184:187]\n"
"s_sub_u32 s86, s84, -124\n"
"v_exp_f32 v30, v174\n"
"v_mfma_f32_16x16x32_bf16 v[188:191], a[240:243], v[76:79], v[220:223]\n"
"v_mfma_f32_16x16x32_bf16 v[188:191], a[244:247], v[100:103], v[188:191]\n"
"s_movk_i32 s91, 23\n"
"v_exp_f32 v31, v175\n"
"v_mfma_f32_16x16x32_bf16 v[188:191], a[248:251], v[124:127], v[188:191]\n"
"v_cvt_pk_bf16_f32 v7, v30, v31\n"
"v_mfma_f32_16x16x32_bf16 v[188:191], a[252:255], v[148:151], v[188:191]\n"
"v_add_f32 v36, v36, v248\n"
"v_add_f32 v37, v37, v252\n"
"s_waitcnt vmcnt(16)\n"
"s_barrier\n"
"v_mfma_f32_16x16x32_bf16 a[128:131], a[192:195], v[16:19], 0\n"
"v_mfma_f32_16x16x32_bf16 a[160:163], a[192:195], v[20:23], 0\n"
"v_exp_f32 v24, v176\n"
"v_mfma_f32_16x16x32_bf16 v[232:235], v[58:61], v[0:3], 0\n"
"ds_read_b64_tr_b16 a[192:193], v42 offset:0\n"
"ds_read_b64_tr_b16 a[194:195], v42 offset:4096\n"
"v_mfma_f32_16x16x32_bf16 v[236:239], v[58:61], v[4:7], 0\n"
"v_exp_f32 v25, v177\n"
"v_mfma_f32_16x16x32_bf16 a[132:135], a[196:199], v[16:19], 0\n"
"v_cvt_pk_bf16_f32 v8, v24, v25\n"
"v_mfma_f32_16x16x32_bf16 a[164:167], a[196:199], v[20:23], 0\n"
"v_exp_f32 v26, v178\n"
"v_mfma_f32_16x16x32_bf16 a[136:139], a[200:203], v[16:19], 0\n"
"ds_read_b64_tr_b16 a[196:197], v43 offset:0\n"
"ds_read_b64_tr_b16 a[198:199], v43 offset:4096\n"
"v_mfma_f32_16x16x32_bf16 a[168:171], a[200:203], v[20:23], 0\n"
"v_exp_f32 v27, v179\n"
"v_mfma_f32_16x16x32_bf16 a[140:143], a[204:207], v[16:19], 0\n"
"ds_read_b64_tr_b16 a[200:201], v42 offset:512\n"
"v_cvt_pk_bf16_f32 v9, v26, v27\n"
"v_mfma_f32_16x16x32_bf16 a[172:175], a[204:207], v[20:23], 0\n"
"v_exp_f32 v28, v180\n"
"v_mfma_f32_16x16x32_bf16 a[144:147], a[208:211], v[16:19], 0\n"
"ds_read_b64_tr_b16 a[202:203], v42 offset:4608\n"
"v_max_f32 v54, v232, v236\n"
"v_mfma_f32_16x16x32_bf16 a[176:179], a[208:211], v[20:23], 0\n"
"v_exp_f32 v29, v181\n"
"v_mfma_f32_16x16x32_bf16 a[148:151], a[212:215], v[16:19], 0\n"
"ds_read_b64_tr_b16 a[204:205], v43 offset:512\n"
"v_cvt_pk_bf16_f32 v10, v28, v29\n"
"v_mfma_f32_16x16x32_bf16 a[180:183], a[212:215], v[20:23], 0\n"
"v_exp_f32 v30, v182\n"
"v_mfma_f32_16x16x32_bf16 a[152:155], a[216:219], v[16:19], 0\n"
"ds_read_b64_tr_b16 a[206:207], v43 offset:4608\n"
"v_cmp_nge_f32 vcc, s53, v54\n"
"v_mfma_f32_16x16x32_bf16 a[184:187], a[216:219], v[20:23], 0\n"
"v_exp_f32 v31, v183\n"
"v_mfma_f32_16x16x32_bf16 a[156:159], a[220:223], v[16:19], 0\n"
"ds_read_b64_tr_b16 a[208:209], v42 offset:1024\n"
"v_cvt_pk_bf16_f32 v11, v30, v31\n"
"v_mfma_f32_16x16x32_bf16 a[188:191], a[220:223], v[20:23], 0\n"
"v_exp_f32 v24, v184\n"
"v_mfma_f32_16x16x32_bf16 v[192:195], a[224:227], v[80:83], v[224:227]\n"
"s_cbranch_vccnz K1_RARE0\n"
"K1_BACK0:\n"
"v_mfma_f32_16x16x32_bf16 v[192:195], a[228:231], v[104:107], v[192:195]\n"
"v_exp_f32 v25, v185\n"
"v_mfma_f32_16x16x32_bf16 v[192:195], a[232:235], v[128:131], v[192:195]\n"
"ds_read_b64_tr_b16 a[210:211], v42 offset:5120\n"
"v_cvt_pk_bf16_f32 v12, v24, v25\n"
"v_mfma_f32_16x16x32_bf16 v[192:195], a[236:239], v[152:155], v[192:195]\n"
"v_exp_f32 v26, v186\n"
"v_mfma_f32_16x16x32_bf16 v[196:199], a[240:243], v[80:83], v[224:227]\n"
"v_add_f32 v32, v32, v232\n"
"v_add_f32 v33, v33, v236\n"
"v_mfma_f32_16x16x32_bf16 v[196:199], a[244:247], v[104:107], v[196:199]\n"
"v_exp_f32 v27, v187\n"
"v_mfma_f32_16x16x32_bf16 v[196:199], a[248:251], v[128:131], v[196:199]\n"
"ds_read_b64_tr_b16 a[212:213], v43 offset:1024\n"
"v_cvt_pk_bf16_f32 v13, v26, v27\n"
"v_mfma_f32_16x16x32_bf16 v[196:199], a[252:255], v[152:155], v[196:199]\n"
"v_exp_f32 v28, v188\n"
"v_mfma_f32_16x16x32_bf16 v[200:203], a[224:227], v[84:87], v[228:231]\n"
"ds_read_b64_tr_b16 a[214:215], v43 offset:5120\n"
"ds_read_b64_tr_b16 a[216:217], v42 offset:1536\n"
"v_mfma_f32_16x16x32_bf16 v[200:203], a[228:231], v[108:111], v[200:203]\n"
"v_exp_f32 v29, v189\n"
"v_mfma_f32_16x16x32_bf16 v[200:203], a[232:235], v[132:135], v[200:203]\n"
"ds_read_b64_tr_b16 a[218:219], v42 offset:5632\n"
"v_cvt_pk_bf16_f32 v14, v28, v29\n"
"v_mfma_f32_16x16x32_bf16 v[200:203], a[236:239], v[156:159], v[200:203]\n"
"v_exp_f32 v30, v190\n"
"v_mfma_f32_16x16x32_bf16 v[204:207], a[240:243], v[84:87], v[228:231]\n"
"ds_read_b64_tr_b16 a[220:221], v43 offset:1536\n"
"ds_read_b64_tr_b16 a[222:223], v43 offset:5632\n"
"v_mfma_f32_16x16x32_bf16 v[204:207], a[244:247], v[108:111], v[204:207]\n"
"v_exp_f32 v31, v191\n"
"v_mfma_f32_16x16x32_bf16 v[204:207], a[248:251], v[132:135], v[204:207]\n"
"v_cvt_pk_bf16_f32 v15, v30, v31\n"
"v_mfma_f32_16x16x32_bf16 v[204:207], a[252:255], v[156:159], v[204:207]\n"
"s_waitcnt lgkmcnt(6)\n"
"v_mfma_f32_16x16x32_bf16 a[0:3], a[192:195], v[0:3], a[0:3]\n"
"ds_read_b128 a[224:227], v41 offset:0\n"
"v_mfma_f32_16x16x32_bf16 a[32:35], a[192:195], v[4:7], a[32:35]\n"
"v_exp_f32 v24, v192\n"
"v_mfma_f32_16x16x32_bf16 v[240:243], v[58:61], v[8:11], 0\n"
"ds_read_b128 a[228:231], v41 offset:512\n"
"v_mfma_f32_16x16x32_bf16 v[244:247], v[58:61], v[12:15], 0\n"
"v_exp_f32 v25, v193\n"
"v_mfma_f32_16x16x32_bf16 a[4:7], a[196:199], v[0:3], a[4:7]\n"
"ds_read_b128 a[232:235], v41 offset:1024\n"
"v_cvt_pk_bf16_f32 v16, v24, v25\n"
"v_mfma_f32_16x16x32_bf16 a[36:39], a[196:199], v[4:7], a[36:39]\n"
"v_exp_f32 v26, v194\n"
"v_mfma_f32_16x16x32_bf16 a[8:11], a[200:203], v[0:3], a[8:11]\n"
"ds_read_b128 a[236:239], v41 offset:1536\n"
"v_mfma_f32_16x16x32_bf16 a[40:43], a[200:203], v[4:7], a[40:43]\n"
"v_exp_f32 v27, v195\n"
"v_mfma_f32_16x16x32_bf16 a[12:15], a[204:207], v[0:3], a[12:15]\n"
"ds_read_b128 a[240:243], v41 offset:4096\n"
"v_cvt_pk_bf16_f32 v17, v26, v27\n"
"v_mfma_f32_16x16x32_bf16 a[44:47], a[204:207], v[4:7], a[44:47]\n"
"v_exp_f32 v28, v196\n"
"v_mfma_f32_16x16x32_bf16 a[16:19], a[208:211], v[0:3], a[16:19]\n"
"ds_read_b128 a[244:247], v41 offset:4608\n"
"v_max_f32 v54, v240, v244\n"
"v_mfma_f32_16x16x32_bf16 a[48:51], a[208:211], v[4:7], a[48:51]\n"
"s_waitcnt lgkmcnt(6)\n"
"v_exp_f32 v29, v197\n"
"v_mfma_f32_16x16x32_bf16 a[20:23], a[212:215], v[0:3], a[20:23]\n"
"ds_read_b128 a[248:251], v41 offset:5120\n"
"v_cvt_pk_bf16_f32 v18, v28, v29\n"
"v_mfma_f32_16x16x32_bf16 a[52:55], a[212:215], v[4:7], a[52:55]\n"
"v_exp_f32 v30, v198\n"
"v_mfma_f32_16x16x32_bf16 a[24:27], a[216:219], v[0:3], a[24:27]\n"
"ds_read_b128 a[252:255], v41 offset:5632\n"
"v_cmp_nge_f32 vcc, s53, v54\n"
"v_mfma_f32_16x16x32_bf16 a[56:59], a[216:219], v[4:7], a[56:59]\n"
"v_exp_f32 v31, v199\n"
"v_mfma_f32_16x16x32_bf16 a[28:31], a[220:223], v[0:3], a[28:31]\n"
"v_cvt_pk_bf16_f32 v19, v30, v31\n"
"v_mfma_f32_16x16x32_bf16 a[60:63], a[220:223], v[4:7], a[60:63]\n"
"s_waitcnt lgkmcnt(4)\n"
"v_exp_f32 v24, v200\n"
"v_mfma_f32_16x16x32_bf16 v[160:163], a[224:227], v[64:67], v[208:211]\n"
"s_cbranch_vccnz K1_RARE1\n"
"K1_BACK1:\n"
"v_mfma_f32_16x16x32_bf16 v[160:163], a[228:231], v[88:91], v[160:163]\n"
"v_exp_f32 v25, v201\n"
"v_mfma_f32_16x16x32_bf16 v[160:163], a[232:235], v[112:115], v[160:163]\n"
"v_cvt_pk_bf16_f32 v20, v24, v25\n"
"v_mfma_f32_16x16x32_bf16 v[160:163], a[236:239], v[136:139], v[160:163]\n"
"s_waitcnt lgkmcnt(0)\n"
"v_exp_f32 v26, v202\n"
"v_mfma_f32_16x16x32_bf16 v[164:167], a[240:243], v[64:67], v[208:211]\n"
"v_add_f32 v34, v34, v240\n"
"v_add_f32 v35, v35, v244\n"
"v_mfma_f32_16x16x32_bf16 v[164:167], a[244:247], v[88:91], v[164:167]\n"
"s_mov_b32 m0, s60\n"
"v_exp_f32 v27, v203\n"
"v_mfma_f32_16x16x32_bf16 v[164:167], a[248:251], v[112:115], v[164:167]\n"
"v_cvt_pk_bf16_f32 v21, v26, v27\n"
"v_mfma_f32_16x16x32_bf16 v[164:167], a[252:255], v[136:139], v[164:167]\n"
"v_exp_f32 v28, v204\n"
"v_mfma_f32_16x16x32_bf16 v[168:171], a[224:227], v[68:71], v[212:215]\n"
"buffer_load_dwordx4 v44, s[48:51], s56 offen offset:0 nt lds\n"
"v_mfma_f32_16x16x32_bf16 v[168:171], a[228:231], v[92:95], v[168:171]\n"
"v_exp_f32 v29, v205\n"
"v_mfma_f32_16x16x32_bf16 v[168:171], a[232:235], v[116:119], v[168:171]\n"
"v_cvt_pk_bf16_f32 v22, v28, v29\n"
"v_mfma_f32_16x16x32_bf16 v[168:171], a[236:239], v[140:143], v[168:171]\n"
"v_exp_f32 v30, v206\n"
"v_mfma_f32_16x16x32_bf16 v[172:175], a[240:243], v[68:71], v[212:215]\n"
"buffer_load_dwordx4 v45, s[48:51], s56 offen offset:1024 nt lds\n"
"v_mfma_f32_16x16x32_bf16 v[172:175], a[244:247], v[92:95], v[172:175]\n"
"v_exp_f32 v31, v207\n"
"v_mfma_f32_16x16x32_bf16 v[172:175], a[248:251], v[116:119], v[172:175]\n"
"v_cvt_pk_bf16_f32 v23, v30, v31\n"
"v_mfma_f32_16x16x32_bf16 v[172:175], a[252:255], v[140:143], v[172:175]\n"
"buffer_load_dwordx4 v46, s[48:51], s56 offen offset:2048 nt lds\n"
"v_mfma_f32_16x16x32_bf16 a[64:67], a[192:195], v[8:11], a[64:67]\n"
"buffer_load_dwordx4 v47, s[48:51], s56 offen offset:3072 nt lds\n"
"v_mfma_f32_16x16x32_bf16 a[96:99], a[192:195], v[12:15], a[96:99]\n"
"s_add_u32 m0, s60, 4096\n"
"v_exp_f32 v24, v160\n"
"v_mfma_f32_16x16x32_bf16 v[248:251], v[58:61], v[16:19], 0\n"
"buffer_load_dwordx4 v44, s[48:51], s57 offen offset:0 nt lds\n"
"v_mfma_f32_16x16x32_bf16 v[252:255], v[58:61], v[20:23], 0\n"
"s_add_u32 s60, s61, s59\n"
"v_exp_f32 v25, v161\n"
"v_mfma_f32_16x16x32_bf16 a[68:71], a[196:199], v[8:11], a[68:71]\n"
"v_cvt_pk_bf16_f32 v0, v24, v25\n"
"v_mfma_f32_16x16x32_bf16 a[100:103], a[196:199], v[12:15], a[100:103]\n"
"s_mov_b32 s59, s58\n"
"v_exp_f32 v26, v162\n"
"v_mfma_f32_16x16x32_bf16 a[72:75], a[200:203], v[8:11], a[72:75]\n"
"buffer_load_dwordx4 v45, s[48:51], s57 offen offset:1024 nt lds\n"
"v_mfma_f32_16x16x32_bf16 a[104:107], a[200:203], v[12:15], a[104:107]\n"
"s_add_u32 s58, s58, 8192\n"
"v_exp_f32 v27, v163\n"
"v_mfma_f32_16x16x32_bf16 a[76:79], a[204:207], v[8:11], a[76:79]\n"
"v_cvt_pk_bf16_f32 v1, v26, v27\n"
"v_mfma_f32_16x16x32_bf16 a[108:111], a[204:207], v[12:15], a[108:111]\n"
"s_cmp_eq_u32 s58, 40960\n"
"v_exp_f32 v28, v164\n"
"v_mfma_f32_16x16x32_bf16 a[80:83], a[208:211], v[8:11], a[80:83]\n"
"v_max_f32 v54, v248, v252\n"
"v_mfma_f32_16x16x32_bf16 a[112:115], a[208:211], v[12:15], a[112:115]\n"
"s_cselect_b32 s58, 0, s58\n"
"v_exp_f32 v29, v165\n"
"v_mfma_f32_16x16x32_bf16 a[84:87], a[212:215], v[8:11], a[84:87]\n"
"v_add_u32 v42, s59, v39\n"
"v_cvt_pk_bf16_f32 v2, v28, v29\n"
"v_mfma_f32_16x16x32_bf16 a[116:119], a[212:215], v[12:15], a[116:119]\n"
"s_add_u32 s56, s57, 4096\n"
"v_exp_f32 v30, v166\n"
"v_mfma_f32_16x16x32_bf16 a[88:91], a[216:219], v[8:11], a[88:91]\n"
"v_cmp_nge_f32 vcc, s53, v54\n"
"v_mfma_f32_16x16x32_bf16 a[120:123], a[216:219], v[12:15], a[120:123]\n"
"s_and_b32 s56, s56, 0x3ffff\n"
"v_exp_f32 v31, v167\n"
"v_mfma_f32_16x16x32_bf16 a[92:95], a[220:223], v[8:11], a[92:95]\n"
"v_add_u32 v43, s59, v40\n"
"v_cvt_pk_bf16_f32 v3, v30, v31\n"
"v_mfma_f32_16x16x32_bf16 a[124:127], a[220:223], v[12:15], a[124:127]\n"
"s_memrealtime s[84:85]\n"
"v_exp_f32 v24, v168\n"
"v_mfma_f32_16x16x32_bf16 v[176:179], a[224:227], v[72:75], v[216:219]\n"
"s_cbranch_vccnz K1_RARE2\n"
"K1_BACK2:\n"
"v_mfma_f32_16x16x32_bf16 v[176:179], a[228:231], v[96:99], v[176:179]\n"
"v_exp_f32 v25, v169\n"
"v_mfma_f32_16x16x32_bf16 v[176:179], a[232:235], v[120:123], v[176:179]\n"
"v_add_u32 v41, s58, v38\n"
"v_cvt_pk_bf16_f32 v4, v24, v25\n"
"v_mfma_f32_16x16x32_bf16 v[176:179], a[236:239], v[144:147], v[176:179]\n"
"v_exp_f32 v26, v170\n"
"v_mfma_f32_16x16x32_bf16 v[180:183], a[240:243], v[72:75], v[216:219]\n"
"v_add_f32 v36, v36, v248\n"
"v_add_f32 v37, v37, v252\n"
"v_mfma_f32_16x16x32_bf16 v[180:183], a[244:247], v[96:99], v[180:183]\n"
"s_waitcnt lgkmcnt(0)\n"
"v_exp_f32 v27, v171\n"
"v_mfma_f32_16x16x32_bf16 v[180:183], a[248:251], v[120:123], v[180:183]\n"
"s_sub_u32 s91, s91, 2\n"
"s_max_i32 s91, s91, 0\n"
"v_cvt_pk_bf16_f32 v5, v26, v27\n"
"v_mfma_f32_16x16x32_bf16 v[180:183], a[252:255], v[144:147], v[180:183]\n"
"s_sub_u32 s87, s86, s84\n"
"v_exp_f32 v28, v172\n"
"v_mfma_f32_16x16x32_bf16 v[184:187], a[224:227], v[76:79], v[220:223]\n"
"buffer_load_dwordx4 v46, s[48:51], s57 offen offset:2048 nt lds\n"
"v_mfma_f32_16x16x32_bf16 v[184:187], a[228:231], v[100:103], v[184:187]\n"
"s_max_i32 s86, s87, -10\n"
"v_exp_f32 v29, v173\n"
"v_mfma_f32_16x16x32_bf16 v[184:187], a[232:235], v[124:127], v[184:187]\n"
"s_cmp_lt_i32 s87, -30\n"
"s_cselect_b32 s91, 23, s91\n"
"v_cvt_pk_bf16_f32 v6, v28, v29\n"
"v_mfma_f32_16x16x32_bf16 v[184:187], a[236:239], v[148:151], v[184:187]\n"
"s_add_u32 s86, s86, s84\n"
"v_exp_f32 v30, v174\n"
"v_mfma_f32_16x16x32_bf16 v[188:191], a[240:243], v[76:79], v[220:223]\n"
"buffer_load_dwordx4 v47, s[48:51], s57 offen offset:3072 nt lds\n"
"s_add_u32 s86, s86, s91\n"
"v_mfma_f32_16x16x32_bf16 v[188:191], a[244:247], v[100:103], v[188:191]\n"
"s_add_u32 s57, s56, 4096\n"
"v_exp_f32 v31, v175\n"
"v_mfma_f32_16x16x32_bf16 v[188:191], a[248:251], v[124:127], v[188:191]\n"
"s_and_b32 s57, s57, 0x3ffff\n"
"v_cvt_pk_bf16_f32 v7, v30, v31\n"
"v_mfma_f32_16x16x32_bf16 v[188:191], a[252:255], v[148:151], v[188:191]\n"
"s_add_u32 s86, s86, s83\n"
"s_cmp_gt_i32 s87, 0\n"
"s_cbranch_scc1 K1_SLP0\n"
"K1_SLB0:\n"
"s_sub_u32 s64, s64, 1\n"
"s_cmp_lg_u32 s64, 3\n"
"K1_LOOP:\n"
"s_waitcnt vmcnt(16)\n"
"s_barrier\n"
"v_mfma_f32_16x16x32_bf16 a[128:131], a[192:195], v[16:19], a[128:131]\n"
"v_mfma_f32_16x16x32_bf16 a[160:163], a[192:195], v[20:23], a[160:163]\n"
"v_exp_f32 v24, v176\n"
"v_mfma_f32_16x16x32_bf16 v[232:235], v[58:61], v[0:3], 0\n"
"ds_read_b64_tr_b16 a[192:193], v42 offset:0\n"
"ds_read_b64_tr_b16 a[194:195], v42 offset:4096\n"
"v_mfma_f32_16x16x32_bf16 v[236:239], v[58:61], v[4:7], 0\n"
"v_exp_f32 v25, v177\n"
"v_mfma_f32_16x16x32_bf16 a[132:135], a[196:199], v[16:19], a[132:135]\n"
"v_cvt_pk_bf16_f32 v8, v24, v25\n"
"v_mfma_f32_16x16x32_bf16 a[164:167], a[196:199], v[20:23], a[164:167]\n"
"v_exp_f32 v26, v178\n"
"v_mfma_f32_16x16x32_bf16 a[136:139], a[200:203], v[16:19], a[136:139]\n"
"ds_read_b64_tr_b16 a[196:197], v43 offset:0\n"
"ds_read_b64_tr_b16 a[198:199], v43 offset:4096\n"
"v_mfma_f32_16x16x32_bf16 a[168:171], a[200:203], v[20:23], a[168:171]\n"
"v_exp_f32 v27, v179\n"
"v_mfma_f32_16x16x32_bf16 a[140:143], a[204:207], v[16:19], a[140:143]\n"
"ds_read_b64_tr_b16 a[200:201], v42 offset:512\n"
"v_cvt_pk_bf16_f32 v9, v26, v27\n"
"v_mfma_f32_16x16x32_bf16 a[172:175], a[204:207], v[20:23], a[172:175]\n"
"v_exp_f32 v28, v180\n"
"v_mfma_f32_16x16x32_bf16 a[144:147], a[208:211], v[16:19], a[144:147]\n"
"ds_read_b64_tr_b16 a[202:203], v42 offset:4608\n"
"v_max_f32 v54, v232, v236\n"
"v_mfma_f32_16x16x32_bf16 a[176:179], a[208:211], v[20:23], a[176:179]\n"
"v_exp_f32 v29, v181\n"
"v_mfma_f32_16x16x32_bf16 a[148:151], a[212:215], v[16:19], a[148:151]\n"
"ds_read_b64_tr_b16 a[204:205], v43 offset:512\n"
"v_cvt_pk_bf16_f32 v10, v28, v29\n"
"v_mfma_f32_16x16x32_bf16 a[180:183], a[212:215], v[20:23], a[180:183]\n"
"v_exp_f32 v30, v182\n"
"v_mfma_f32_16x16x32_bf16 a[152:155], a[216:219], v[16:19], a[152:155]\n"
"ds_read_b64_tr_b16 a[206:207], v43 offset:4608\n"
"v_cmp_nge_f32 vcc, s53, v54\n"
"v_mfma_f32_16x16x32_bf16 a[184:187], a[216:219], v[20:23], a[184:187]\n"
"v_exp_f32 v31, v183\n"
"v_mfma_f32_16x16x32_bf16 a[156:159], a[220:223], v[16:19], a[156:159]\n"
"ds_read_b64_tr_b16 a[208:209], v42 offset:1024\n"
"v_cvt_pk_bf16_f32 v11, v30, v31\n"
"v_mfma_f32_16x16x32_bf16 a[188:191], a[220:223], v[20:23], a[188:191]\n"
"v_exp_f32 v24, v184\n"
"v_mfma_f32_16x16x32_bf16 v[192:195], a[224:227], v[80:83], v[224:227]\n"
"s_cbranch_vccnz K1_RARE3\n"
"K1_BACK3:\n"
"v_mfma_f32_16x16x32_bf16 v[192:195], a[228:231], v[104:107], v[192:195]\n"
"v_exp_f32 v25, v185\n"
"v_mfma_f32_16x16x32_bf16 v[192:195], a[232:235], v[128:131], v[192:195]\n"
"ds_read_b64_tr_b16 a[210:211], v42 offset:5120\n"
"v_cvt_pk_bf16_f32 v12, v24, v25\n"
"v_mfma_f32_16x16x32_bf16 v[192:195], a[236:239], v[152:155], v[192:195]\n"
"v_exp_f32 v26, v186\n"
"v_mfma_f32_16x16x32_bf16 v[196:199], a[240:243], v[80:83], v[224:227]\n"
"v_add_f32 v32, v32, v232\n"
"v_add_f32 v33, v33, v236\n"
"v_mfma_f32_16x16x32_bf16 v[196:199], a[244:247], v[104:107], v[196:199]\n"
"v_exp_f32 v27, v187\n"
"v_mfma_f32_16x16x32_bf16 v[196:199], a[248:251], v[128:131], v[196:199]\n"
"ds_read_b64_tr_b16 a[212:213], v43 offset:1024\n"
"v_cvt_pk_bf16_f32 v13, v26, v27\n"
"v_mfma_f32_16x16x32_bf16 v[196:199], a[252:255], v[152:155], v[196:199]\n"
"v_exp_f32 v28, v188\n"
"v_mfma_f32_16x16x32_bf16 v[200:203], a[224:227], v[84:87], v[228:231]\n"
"ds_read_b64_tr_b16 a[214:215], v43 offset:5120\n"
"ds_read_b64_tr_b16 a[216:217], v42 offset:1536\n"
"v_mfma_f32_16x16x32_bf16 v[200:203], a[228:231], v[108:111], v[200:203]\n"
"v_exp_f32 v29, v189\n"
"v_mfma_f32_16x16x32_bf16 v[200:203], a[232:235], v[132:135], v[200:203]\n"
"ds_read_b64_tr_b16 a[218:219], v42 offset:5632\n"
"v_cvt_pk_bf16_f32 v14, v28, v29\n"
"v_mfma_f32_16x16x32_bf16 v[200:203], a[236:239], v[156:159], v[200:203]\n"
"v_exp_f32 v30, v190\n"
"v_mfma_f32_16x16x32_bf16 v[204:207], a[240:243], v[84:87], v[228:231]\n"
"ds_read_b64_tr_b16 a[220:221], v43 offset:1536\n"
"ds_read_b64_tr_b16 a[222:223], v43 offset:5632\n"
"v_mfma_f32_16x16x32_bf16 v[204:207], a[244:247], v[108:111], v[204:207]\n"
"v_exp_f32 v31, v191\n"
"v_mfma_f32_16x16x32_bf16 v[204:207], a[248:251], v[132:135], v[204:207]\n"
"v_cvt_pk_bf16_f32 v15, v30, v31\n"
"v_mfma_f32_16x16x32_bf16 v[204:207], a[252:255], v[156:159], v[204:207]\n"
"s_waitcnt lgkmcnt(6)\n"
"v_mfma_f32_16x16x32_bf16 a[0:3], a[192:195], v[0:3], a[0:3]\n"
"ds_read_b128 a[224:227], v41 offset:0\n"
"v_mfma_f32_16x16x32_bf16 a[32:35], a[192:195], v[4:7], a[32:35]\n"
"v_exp_f32 v24, v192\n"
"v_mfma_f32_16x16x32_bf16 v[240:243], v[58:61], v[8:11], 0\n"
"ds_read_b128 a[228:231], v41 offset:512\n"
"v_mfma_f32_16x16x32_bf16 v[244:247], v[58:61], v[12:15], 0\n"
"v_exp_f32 v25, v193\n"
"v_mfma_f32_16x16x32_bf16 a[4:7], a[196:199], v[0:3], a[4:7]\n"
"ds_read_b128 a[232:235], v41 offset:1024\n"
"v_cvt_pk_bf16_f32 v16, v24, v25\n"
"v_mfma_f32_16x16x32_bf16 a[36:39], a[196:199], v[4:7], a[36:39]\n"
"v_exp_f32 v26, v194\n"
"v_mfma_f32_16x16x32_bf16 a[8:11], a[200:203], v[0:3], a[8:11]\n"
"ds_read_b128 a[236:239], v41 offset:1536\n"
"v_mfma_f32_16x16x32_bf16 a[40:43], a[200:203], v[4:7], a[40:43]\n"
"v_exp_f32 v27, v195\n"
"v_mfma_f32_16x16x32_bf16 a[12:15], a[204:207], v[0:3], a[12:15]\n"
"ds_read_b128 a[240:243], v41 offset:4096\n"
"v_cvt_pk_bf16_f32 v17, v26, v27\n"
"v_mfma_f32_16x16x32_bf16 a[44:47], a[204:207], v[4:7], a[44:47]\n"
"v_exp_f32 v28, v196\n"
"v_mfma_f32_16x16x32_bf16 a[16:19], a[208:211], v[0:3], a[16:19]\n"
"ds_read_b128 a[244:247], v41 offset:4608\n"
"v_max_f32 v54, v240, v244\n"
"v_mfma_f32_16x16x32_bf16 a[48:51], a[208:211], v[4:7], a[48:51]\n"
"s_waitcnt lgkmcnt(6)\n"
"v_exp_f32 v29, v197\n"
"v_mfma_f32_16x16x32_bf16 a[20:23], a[212:215], v[0:3], a[20:23]\n"
"ds_read_b128 a[248:251], v41 offset:5120\n"
"v_cvt_pk_bf16_f32 v18, v28, v29\n"
"v_mfma_f32_16x16x32_bf16 a[52:55], a[212:215], v[4:7], a[52:55]\n"
"v_exp_f32 v30, v198\n"
"v_mfma_f32_16x16x32_bf16 a[24:27], a[216:219], v[0:3], a[24:27]\n"
"ds_read_b128 a[252:255], v41 offset:5632\n"
"v_cmp_nge_f32 vcc, s53, v54\n"
"v_mfma_f32_16x16x32_bf16 a[56:59], a[216:219], v[4:7], a[56:59]\n"
"v_exp_f32 v31, v199\n"
"v_mfma_f32_16x16x32_bf16 a[28:31], a[220:223], v[0:3], a[28:31]\n"
"v_cvt_pk_bf16_f32 v19, v30, v31\n"
"v_mfma_f32_16x16x32_bf16 a[60:63], a[220:223], v[4:7], a[60:63]\n"
"s_waitcnt lgkmcnt(4)\n"
"v_exp_f32 v24, v200\n"
"v_mfma_f32_16x16x32_bf16 v[160:163], a[224:227], v[64:67], v[208:211]\n"
"s_cbranch_vccnz K1_RARE4\n"
"K1_BACK4:\n"
"v_mfma_f32_16x16x32_bf16 v[160:163], a[228:231], v[88:91], v[160:163]\n"
"v_exp_f32 v25, v201\n"
"v_mfma_f32_16x16x32_bf16 v[160:163], a[232:235], v[112:115], v[160:163]\n"
"v_cvt_pk_bf16_f32 v20, v24, v25\n"
"v_mfma_f32_16x16x32_bf16 v[160:163], a[236:239], v[136:139], v[160:163]\n"
"s_waitcnt lgkmcnt(0)\n"
"v_exp_f32 v26, v202\n"
"v_mfma_f32_16x16x32_bf16 v[164:167], a[240:243], v[64:67], v[208:211]\n"
"v_add_f32 v34, v34, v240\n"
"v_add_f32 v35, v35, v244\n"
"v_mfma_f32_16x16x32_bf16 v[164:167], a[244:247], v[88:91], v[164:167]\n"
"s_mov_b32 m0, s60\n"
"v_exp_f32 v27, v203\n"
"v_mfma_f32_16x16x32_bf16 v[164:167], a[248:251], v[112:115], v[164:167]\n"
"v_cvt_pk_bf16_f32 v21, v26, v27\n"
"v_mfma_f32_16x16x32_bf16 v[164:167], a[252:255], v[136:139], v[164:167]\n"
"v_exp_f32 v28, v204\n"
"v_mfma_f32_16x16x32_bf16 v[168:171], a[224:227], v[68:71], v[212:215]\n"
"buffer_load_dwordx4 v44, s[48:51], s56 offen offset:0 nt lds\n"
"v_mfma_f32_16x16x32_bf16 v[168:171], a[228:231], v[92:95], v[168:171]\n"
"v_exp_f32 v29, v205\n"
"v_mfma_f32_16x16x32_bf16 v[168:171], a[232:235], v[116:119], v[168:171]\n"
"v_cvt_pk_bf16_f32 v22, v28, v29\n"
"v_mfma_f32_16x16x32_bf16 v[168:171], a[236:239], v[140:143], v[168:171]\n"
"v_exp_f32 v30, v206\n"
"v_mfma_f32_16x16x32_bf16 v[172:175], a[240:243], v[68:71], v[212:215]\n"
"buffer_load_dwordx4 v45, s[48:51], s56 offen offset:1024 nt lds\n"
"v_mfma_f32_16x16x32_bf16 v[172:175], a[244:247], v[92:95], v[172:175]\n"
"v_exp_f32 v31, v207\n"
"v_mfma_f32_16x16x32_bf16 v[172:175], a[248:251], v[116:119], v[172:175]\n"
"v_cvt_pk_bf16_f32 v23, v30, v31\n"
"v_mfma_f32_16x16x32_bf16 v[172:175], a[252:255], v[140:143], v[172:175]\n"
"buffer_load_dwordx4 v46, s[48:51], s56 offen offset:2048 nt lds\n"
"v_mfma_f32_16x16x32_bf16 a[64:67], a[192:195], v[8:11], a[64:67]\n"
"buffer_load_dwordx4 v47, s[48:51], s56 offen offset:3072 nt lds\n"
"v_mfma_f32_16x16x32_bf16 a[96:99], a[192:195], v[12:15], a[96:99]\n"
"s_add_u32 m0, s60, 4096\n"
"v_exp_f32 v24, v160\n"
"v_mfma_f32_16x16x32_bf16 v[248:251], v[58:61], v[16:19], 0\n"
"buffer_load_dwordx4 v44, s[48:51], s57 offen offset:0 nt lds\n"
"v_mfma_f32_16x16x32_bf16 v[252:255], v[58:61], v[20:23], 0\n"
"s_add_u32 s60, s61, s59\n"
"v_exp_f32 v25, v161\n"
"v_mfma_f32_16x16x32_bf16 a[68:71], a[196:199], v[8:11], a[68:71]\n"
"v_cvt_pk_bf16_f32 v0, v24, v25\n"
"v_mfma_f32_16x16x32_bf16 a[100:103], a[196:199], v[12:15], a[100:103]\n"
"s_mov_b32 s59, s58\n"
"v_exp_f32 v26, v162\n"
"v_mfma_f32_16x16x32_bf16 a[72:75], a[200:203], v[8:11], a[72:75]\n"
"buffer_load_dwordx4 v45, s[48:51], s57 offen offset:1024 nt lds\n"
"v_mfma_f32_16x16x32_bf16 a[104:107], a[200:203], v[12:15], a[104:107]\n"
"s_add_u32 s58, s58, 8192\n"
"v_exp_f32 v27, v163\n"
"v_mfma_f32_16x16x32_bf16 a[76:79], a[204:207], v[8:11], a[76:79]\n"
"v_cvt_pk_bf16_f32 v1, v26, v27\n"
"v_mfma_f32_16x16x32_bf16 a[108:111], a[204:207], v[12:15], a[108:111]\n"
"s_cmp_eq_u32 s58, 40960\n"
"v_exp_f32 v28, v164\n"
"v_mfma_f32_16x16x32_bf16 a[80:83], a[208:211], v[8:11], a[80:83]\n"
"v_max_f32 v54, v248, v252\n"
"v_mfma_f32_16x16x32_bf16 a[112:115], a[208:211], v[12:15], a[112:115]\n"
"s_cselect_b32 s58, 0, s58\n"
"v_exp_f32 v29, v165\n"
"v_mfma_f32_16x16x32_bf16 a[84:87], a[212:215], v[8:11], a[84:87]\n"
"v_add_u32 v42, s59, v39\n"
"v_cvt_pk_bf16_f32 v2, v28, v29\n"
"v_mfma_f32_16x16x32_bf16 a[116:119], a[212:215], v[12:15], a[116:119]\n"
"s_add_u32 s56, s57, 4096\n"
"v_exp_f32 v30, v166\n"
"v_mfma_f32_16x16x32_bf16 a[88:91], a[216:219], v[8:11], a[88:91]\n"
"v_cmp_nge_f32 vcc, s53, v54\n"
"v_mfma_f32_16x16x32_bf16 a[120:123], a[216:219], v[12:15], a[120:123]\n"
"s_and_b32 s56, s56, 0x3ffff\n"
"v_exp_f32 v31, v167\n"
"v_mfma_f32_16x16x32_bf16 a[92:95], a[220:223], v[8:11], a[92:95]\n"
"v_add_u32 v43, s59, v40\n"
"v_cvt_pk_bf16_f32 v3, v30, v31\n"
"v_mfma_f32_16x16x32_bf16 a[124:127], a[220:223], v[12:15], a[124:127]\n"
"s_memrealtime s[84:85]\n"
"v_exp_f32 v24, v168\n"
"v_mfma_f32_16x16x32_bf16 v[176:179], a[224:227], v[72:75], v[216:219]\n"
"s_cbranch_vccnz K1_RARE5\n"
"K1_BACK5:\n"
"v_mfma_f32_16x16x32_bf16 v[176:179], a[228:231], v[96:99], v[176:179]\n"
"v_exp_f32 v25, v169\n"
"v_mfma_f32_16x16x32_bf16 v[176:179], a[232:235], v[120:123], v[176:179]\n"
"v_add_u32 v41, s58, v38\n"
"v_cvt_pk_bf16_f32 v4, v24, v25\n"
"v_mfma_f32_16x16x32_bf16 v[176:179], a[236:239], v[144:147], v[176:179]\n"
"v_exp_f32 v26, v170\n"
"v_mfma_f32_16x16x32_bf16 v[180:183], a[240:243], v[72:75], v[216:219]\n"
"v_add_f32 v36, v36, v248\n"
"v_add_f32 v37, v37, v252\n"
"v_mfma_f32_16x16x32_bf16 v[180:183], a[244:247], v[96:99], v[180:183]\n"
"s_waitcnt lgkmcnt(0)\n"
"v_exp_f32 v27, v171\n"
"v_mfma_f32_16x16x32_bf16 v[180:183], a[248:251], v[120:123], v[180:183]\n"
"s_sub_u32 s91, s91, 2\n"
"s_max_i32 s91, s91, 0\n"
"v_cvt_pk_bf16_f32 v5, v26, v27\n"
"v_mfma_f32_16x16x32_bf16 v[180:183], a[252:255], v[144:147], v[180:183]\n"
"s_sub_u32 s87, s86, s84\n"
"v_exp_f32 v28, v172\n"
"v_mfma_f32_16x16x32_bf16 v[184:187], a[224:227], v[76:79], v[220:223]\n"
"buffer_load_dwordx4 v46, s[48:51], s57 offen offset:2048 nt lds\n"
"v_mfma_f32_16x16x32_bf16 v[184:187], a[228:231], v[100:103], v[184:187]\n"
"s_max_i32 s86, s87, -10\n"
"v_exp_f32 v29, v173\n"
"v_mfma_f32_16x16x32_bf16 v[184:187], a[232:235], v[124:127], v[184:187]\n"
"s_cmp_lt_i32 s87, -30\n"
"s_cselect_b32 s91, 23, s91\n"
"v_cvt_pk_bf16_f32 v6, v28, v29\n"
"v_mfma_f32_16x16x32_bf16 v[184:187], a[236:239], v[148:151], v[184:187]\n"
"s_add_u32 s86, s86, s84\n"
"v_exp_f32 v30, v174\n"
"v_mfma_f32_16x16x32_bf16 v[188:191], a[240:243], v[76:79], v[220:223]\n"
"buffer_load_dwordx4 v47, s[48:51], s57 offen offset:3072 nt lds\n"
"s_add_u32 s86, s86, s91\n"
"v_mfma_f32_16x16x32_bf16 v[188:191], a[244:247], v[100:103], v[188:191]\n"
"s_add_u32 s57, s56, 4096\n"
"v_exp_f32 v31, v175\n"
"v_mfma_f32_16x16x32_bf16 v[188:191], a[248:251], v[124:127], v[188:191]\n"
"s_and_b32 s57, s57, 0x3ffff\n"
"v_cvt_pk_bf16_f32 v7, v30, v31\n"
"v_mfma_f32_16x16x32_bf16 v[188:191], a[252:255], v[148:151], v[188:191]\n"
"s_add_u32 s86, s86, s83\n"
"s_cmp_gt_i32 s87, 0\n"
"s_cbranch_scc1 K1_SLP1\n"
"K1_SLB1:\n"
"s_sub_u32 s64, s64, 1\n"
"s_cmp_lg_u32 s64, 3\n"
"s_cbranch_scc1 K1_LOOP\n"
"v_lshlrev_b32 v53, 7, v57\n"
"global_load_dword v55, v53, s[28:29]\n"
"s_lshl_b32 s93, s52, 7\n"
"v_lshl_add_u32 v52, v57, 9, s93\n"
"v_min_u32 v52, P2_C-K1_LOOPE-4, v52\n"
"v_add_u32 v52, K1_LOOPE-K1_PC, v52\n"
"global_load_dword v55, v52, s[72:73]\n"
"s_mov_b32 s36, s33\n"
"s_and_b32 s37, s71, 0xffff\n"
"s_mov_b32 s38, 780288\n"
"s_mov_b32 s40, s16\n"
"s_and_b32 s41, s17, 0xffff\n"
"s_mov_b32 s42, 8321280\n"
"s_mul_i32 s67, s52, 6144\n"
"s_add_u32 s67, s67, 16384\n"
"s_lshl_b32 s68, s52, 12\n"
"s_lshr_b32 s83, s23, 2\n"
"s_xor_b32 s65, s83, 3\n"
"s_lshr_b32 s81, s65, 1\n"
"s_lshl_b32 s81, s81, 7\n"
"s_and_b32 s65, s65, 1\n"
"s_lshl_b32 s65, s65, 6\n"
"v_bfe_u32 v52, v57, 2, 3\n"
"v_lshrrev_b32 v51, 5, v57\n"
"v_lshlrev_b32 v51, 6, v51\n"
"v_xor_b32 v51, s65, v51\n"
"v_lshl_add_u32 v51, v52, 11, v51\n"
"v_and_b32 v52, 3, v57\n"
"v_lshl_add_u32 v51, v52, 4, v51\n"
"v_bfe_u32 v52, v57, 4, 1\n"
"v_lshlrev_b32 v52, 5, v52\n"
"v_xor_b32 v51, v51, v52\n"
"v_add_u32 v44, 0, v51\n"
"v_add_u32 v45, 15360, v51\n"
"v_add_u32 v46, 30720, v51\n"
"v_add_u32 v47, 46080, v51\n"
"s_and_b32 s80, s23, 3\n"
"s_lshl_b32 s80, s80, 2\n"
"s_lshl_b32 s82, s83, 4\n"
"K1_LOOPE:\n"
"s_cmp_eq_u32 s64, 3\n"
"s_cbranch_scc1 K1_L3\n"
"s_cmp_eq_u32 s64, 2\n"
"s_cbranch_scc1 K1_L2\n"
"s_waitcnt vmcnt(2)\n"
"K1_L2:\n"
"s_waitcnt vmcnt(10)\n"
"K1_L3:\n"
"s_waitcnt vmcnt(18)\n"
"s_barrier\n"
"v_mfma_f32_16x16x32_bf16 a[128:131], a[192:195], v[16:19], a[128:131]\n"
"v_mfma_f32_16x16x32_bf16 a[160:163], a[192:195], v[20:23], a[160:163]\n"
"v_exp_f32 v24, v176\n"
"v_mfma_f32_16x16x32_bf16 v[232:235], v[58:61], v[0:3], 0\n"
"ds_read_b64_tr_b16 a[192:193], v42 offset:0\n"
"ds_read_b64_tr_b16 a[194:195], v42 offset:4096\n"
"v_mfma_f32_16x16x32_bf16 v[236:239], v[58:61], v[4:7], 0\n"
"v_exp_f32 v25, v177\n"
"v_mfma_f32_16x16x32_bf16 a[132:135], a[196:199], v[16:19], a[132:135]\n"
"v_cvt_pk_bf16_f32 v8, v24, v25\n"
"v_mfma_f32_16x16x32_bf16 a[164:167], a[196:199], v[20:23], a[164:167]\n"
"v_exp_f32 v26, v178\n"
"v_mfma_f32_16x16x32_bf16 a[136:139], a[200:203], v[16:19], a[136:139]\n"
"ds_read_b64_tr_b16 a[196:197], v43 offset:0\n"
"ds_read_b64_tr_b16 a[198:199], v43 offset:4096\n"
"v_mfma_f32_16x16x32_bf16 a[168:171], a[200:203], v[20:23], a[168:171]\n"
"v_exp_f32 v27, v179\n"
"v_mfma_f32_16x16x32_bf16 a[140:143], a[204:207], v[16:19], a[140:143]\n"
"ds_read_b64_tr_b16 a[200:201], v42 offset:512\n"
"v_cvt_pk_bf16_f32 v9, v26, v27\n"
"v_mfma_f32_16x16x32_bf16 a[172:175], a[204:207], v[20:23], a[172:175]\n"
"v_exp_f32 v28, v180\n"
"v_mfma_f32_16x16x32_bf16 a[144:147], a[208:211], v[16:19], a[144:147]\n"
"ds_read_b64_tr_b16 a[202:203], v42 offset:4608\n"
"v_max_f32 v54, v232, v236\n"
"v_mfma_f32_16x16x32_bf16 a[176:179], a[208:211], v[20:23], a[176:179]\n"
"v_exp_f32 v29, v181\n"
"v_mfma_f32_16x16x32_bf16 a[148:151], a[212:215], v[16:19], a[148:151]\n"
"ds_read_b64_tr_b16 a[204:205], v43 offset:512\n"
"v_cvt_pk_bf16_f32 v10, v28, v29\n"
"v_mfma_f32_16x16x32_bf16 a[180:183], a[212:215], v[20:23], a[180:183]\n"
"v_exp_f32 v30, v182\n"
"v_mfma_f32_16x16x32_bf16 a[152:155], a[216:219], v[16:19], a[152:155]\n"
"ds_read_b64_tr_b16 a[206:207], v43 offset:4608\n"
"v_cmp_nge_f32 vcc, s53, v54\n"
"v_mfma_f32_16x16x32_bf16 a[184:187], a[216:219], v[20:23], a[184:187]\n"
"v_exp_f32 v31, v183\n"
"v_mfma_f32_16x16x32_bf16 a[156:159], a[220:223], v[16:19], a[156:159]\n"
"ds_read_b64_tr_b16 a[208:209], v42 offset:1024\n"
"v_cvt_pk_bf16_f32 v11, v30, v31\n"
"v_mfma_f32_16x16x32_bf16 a[188:191], a[220:223], v[20:23], a[188:191]\n"
"v_exp_f32 v24, v184\n"
"v_mfma_f32_16x16x32_bf16 v[192:195], a[224:227], v[80:83], v[224:227]\n"
"s_cbranch_vccnz K1_RARE6\n"
"K1_BACK6:\n"
"v_mfma_f32_16x16x32_bf16 v[192:195], a[228:231], v[104:107], v[192:195]\n"
"v_exp_f32 v25, v185\n"
"v_mfma_f32_16x16x32_bf16 v[192:195], a[232:235], v[128:131], v[192:195]\n"
"ds_read_b64_tr_b16 a[210:211], v42 offset:5120\n"
"v_cvt_pk_bf16_f32 v12, v24, v25\n"
"v_mfma_f32_16x16x32_bf16 v[192:195], a[236:239], v[152:155], v[192:195]\n"
"v_exp_f32 v26, v186\n"
"v_mfma_f32_16x16x32_bf16 v[196:199], a[240:243], v[80:83], v[224:227]\n"
"v_add_f32 v32, v32, v232\n"
"v_add_f32 v33, v33, v236\n"
"v_mfma_f32_16x16x32_bf16 v[196:199], a[244:247], v[104:107], v[196:199]\n"
"v_exp_f32 v27, v187\n"
"v_mfma_f32_16x16x32_bf16 v[196:199], a[248:251], v[128:131], v[196:199]\n"
"ds_read_b64_tr_b16 a[212:213], v43 offset:1024\n"
"v_cvt_pk_bf16_f32 v13, v26, v27\n"
"v_mfma_f32_16x16x32_bf16 v[196:199], a[252:255], v[152:155], v[196:199]\n"
"v_exp_f32 v28, v188\n"
"v_mfma_f32_16x16x32_bf16 v[200:203], a[224:227], v[84:87], v[228:231]\n"
"ds_read_b64_tr_b16 a[214:215], v43 offset:5120\n"
"ds_read_b64_tr_b16 a[216:217], v42 offset:1536\n"
"v_mfma_f32_16x16x32_bf16 v[200:203], a[228:231], v[108:111], v[200:203]\n"
"v_exp_f32 v29, v189\n"
"v_mfma_f32_16x16x32_bf16 v[200:203], a[232:235], v[132:135], v[200:203]\n"
"ds_read_b64_tr_b16 a[218:219], v42 offset:5632\n"
"v_cvt_pk_bf16_f32 v14, v28, v29\n"
"v_mfma_f32_16x16x32_bf16 v[200:203], a[236:239], v[156:159], v[200:203]\n"
"v_exp_f32 v30, v190\n"
"v_mfma_f32_16x16x32_bf16 v[204:207], a[240:243], v[84:87], v[228:231]\n"
"ds_read_b64_tr_b16 a[220:221], v43 offset:1536\n"
"ds_read_b64_tr_b16 a[222:223], v43 offset:5632\n"
"v_mfma_f32_16x16x32_bf16 v[204:207], a[244:247], v[108:111], v[204:207]\n"
"v_exp_f32 v31, v191\n"
"v_mfma_f32_16x16x32_bf16 v[204:207], a[248:251], v[132:135], v[204:207]\n"
"v_cvt_pk_bf16_f32 v15, v30, v31\n"
"v_mfma_f32_16x16x32_bf16 v[204:207], a[252:255], v[156:159], v[204:207]\n"
"s_waitcnt lgkmcnt(6)\n"
"v_mfma_f32_16x16x32_bf16 a[0:3], a[192:195], v[0:3], a[0:3]\n"
"ds_read_b128 a[224:227], v41 offset:0\n"
"v_mfma_f32_16x16x32_bf16 a[32:35], a[192:195], v[4:7], a[32:35]\n"
"v_exp_f32 v24, v192\n"
"v_mfma_f32_16x16x32_bf16 v[240:243], v[58:61], v[8:11], 0\n"
"ds_read_b128 a[228:231], v41 offset:512\n"
"v_mfma_f32_16x16x32_bf16 v[244:247], v[58:61], v[12:15], 0\n"
"v_exp_f32 v25, v193\n"
"v_mfma_f32_16x16x32_bf16 a[4:7], a[196:199], v[0:3], a[4:7]\n"
"ds_read_b128 a[232:235], v41 offset:1024\n"
"v_cvt_pk_bf16_f32 v16, v24, v25\n"
"v_mfma_f32_16x16x32_bf16 a[36:39], a[196:199], v[4:7], a[36:39]\n"
"v_exp_f32 v26, v194\n"
"v_mfma_f32_16x16x32_bf16 a[8:11], a[200:203], v[0:3], a[8:11]\n"
"ds_read_b128 a[236:239], v41 offset:1536\n"
"v_mfma_f32_16x16x32_bf16 a[40:43], a[200:203], v[4:7], a[40:43]\n"
"v_exp_f32 v27, v195\n"
"v_mfma_f32_16x16x32_bf16 a[12:15], a[204:207], v[0:3], a[12:15]\n"
"ds_read_b128 a[240:243], v41 offset:4096\n"
"v_cvt_pk_bf16_f32 v17, v26, v27\n"
"v_mfma_f32_16x16x32_bf16 a[44:47], a[204:207], v[4:7], a[44:47]\n"
"v_exp_f32 v28, v196\n"
"v_mfma_f32_16x16x32_bf16 a[16:19], a[208:211], v[0:3], a[16:19]\n"
"ds_read_b128 a[244:247], v41 offset:4608\n"
"v_max_f32 v54, v240, v244\n"
"v_mfma_f32_16x16x32_bf16 a[48:51], a[208:211], v[4:7], a[48:51]\n"
"s_waitcnt lgkmcnt(6)\n"
"v_exp_f32 v29, v197\n"
"v_mfma_f32_16x16x32_bf16 a[20:23], a[212:215], v[0:3], a[20:23]\n"
"ds_read_b128 a[248:251], v41 offset:5120\n"
"v_cvt_pk_bf16_f32 v18, v28, v29\n"
"v_mfma_f32_16x16x32_bf16 a[52:55], a[212:215], v[4:7], a[52:55]\n"
"v_exp_f32 v30, v198\n"
"v_mfma_f32_16x16x32_bf16 a[24:27], a[216:219], v[0:3], a[24:27]\n"
"ds_read_b128 a[252:255], v41 offset:5632\n"
"v_cmp_nge_f32 vcc, s53, v54\n"
"v_mfma_f32_16x16x32_bf16 a[56:59], a[216:219], v[4:7], a[56:59]\n"
"v_exp_f32 v31, v199\n"
"v_mfma_f32_16x16x32_bf16 a[28:31], a[220:223], v[0:3], a[28:31]\n"
"v_cvt_pk_bf16_f32 v19, v30, v31\n"
"v_mfma_f32_16x16x32_bf16 a[60:63], a[220:223], v[4:7], a[60:63]\n"
"s_waitcnt lgkmcnt(4)\n"
"v_exp_f32 v24, v200\n"
"v_mfma_f32_16x16x32_bf16 v[160:163], a[224:227], v[64:67], v[208:211]\n"
"s_cbranch_vccnz K1_RARE7\n"
"K1_BACK7:\n"
"v_mfma_f32_16x16x32_bf16 v[160:163], a[228:231], v[88:91], v[160:163]\n"
"v_exp_f32 v25, v201\n"
"v_mfma_f32_16x16x32_bf16 v[160:163], a[232:235], v[112:115], v[160:163]\n"
"v_cvt_pk_bf16_f32 v20, v24, v25\n"
"v_mfma_f32_16x16x32_bf16 v[160:163], a[236:239], v[136:139], v[160:163]\n"
"s_waitcnt lgkmcnt(0)\n"
"v_exp_f32 v26, v202\n"
"v_mfma_f32_16x16x32_bf16 v[164:167], a[240:243], v[64:67], v[208:211]\n"
"v_add_f32 v34, v34, v240\n"
"v_add_f32 v35, v35, v244\n"
"v_mfma_f32_16x16x32_bf16 v[164:167], a[244:247], v[88:91], v[164:167]\n"
"v_exp_f32 v27, v203\n"
"v_mfma_f32_16x16x32_bf16 v[164:167], a[248:251], v[112:115], v[164:167]\n"
"v_cvt_pk_bf16_f32 v21, v26, v27\n"
"v_mfma_f32_16x16x32_bf16 v[164:167], a[252:255], v[136:139], v[164:167]\n"
"v_exp_f32 v28, v204\n"
"v_mfma_f32_16x16x32_bf16 v[168:171], a[224:227], v[68:71], v[212:215]\n"
"v_mfma_f32_16x16x32_bf16 v[168:171], a[228:231], v[92:95], v[168:171]\n"
"v_exp_f32 v29, v205\n"
"v_mfma_f32_16x16x32_bf16 v[168:171], a[232:235], v[116:119], v[168:171]\n"
"v_cvt_pk_bf16_f32 v22, v28, v29\n"
"v_mfma_f32_16x16x32_bf16 v[168:171], a[236:239], v[140:143], v[168:171]\n"
"v_exp_f32 v30, v206\n"
"v_mfma_f32_16x16x32_bf16 v[172:175], a[240:243], v[68:71], v[212:215]\n"
"v_mfma_f32_16x16x32_bf16 v[172:175], a[244:247], v[92:95], v[172:175]\n"
"v_exp_f32 v31, v207\n"
"v_mfma_f32_16x16x32_bf16 v[172:175], a[248:251], v[116:119], v[172:175]\n"
"v_cvt_pk_bf16_f32 v23, v30, v31\n"
"v_mfma_f32_16x16x32_bf16 v[172:175], a[252:255], v[140:143], v[172:175]\n"
"v_mfma_f32_16x16x32_bf16 a[64:67], a[192:195], v[8:11], a[64:67]\n"
"v_mfma_f32_16x16x32_bf16 a[96:99], a[192:195], v[12:15], a[96:99]\n"
"v_exp_f32 v24, v160\n"
"v_mfma_f32_16x16x32_bf16 v[248:251], v[58:61], v[16:19], 0\n"
"v_mfma_f32_16x16x32_bf16 v[252:255], v[58:61], v[20:23], 0\n"
"s_add_u32 s60, s61, s59\n"
"v_exp_f32 v25, v161\n"
"v_mfma_f32_16x16x32_bf16 a[68:71], a[196:199], v[8:11], a[68:71]\n"
"v_cvt_pk_bf16_f32 v0, v24, v25\n"
"v_mfma_f32_16x16x32_bf16 a[100:103], a[196:199], v[12:15], a[100:103]\n"
"s_mov_b32 s59, s58\n"
"v_exp_f32 v26, v162\n"
"v_mfma_f32_16x16x32_bf16 a[72:75], a[200:203], v[8:11], a[72:75]\n"
"v_mfma_f32_16x16x32_bf16 a[104:107], a[200:203], v[12:15], a[104:107]\n"
"s_add_u32 s58, s58, 8192\n"
"v_exp_f32 v27, v163\n"
"v_mfma_f32_16x16x32_bf16 a[76:79], a[204:207], v[8:11], a[76:79]\n"
"v_cvt_pk_bf16_f32 v1, v26, v27\n"
"v_mfma_f32_16x16x32_bf16 a[108:111], a[204:207], v[12:15], a[108:111]\n"
"s_cmp_eq_u32 s58, 40960\n"
"v_exp_f32 v28, v164\n"
"v_mfma_f32_16x16x32_bf16 a[80:83], a[208:211], v[8:11], a[80:83]\n"
"v_max_f32 v54, v248, v252\n"
"v_mfma_f32_16x16x32_bf16 a[112:115], a[208:211], v[12:15], a[112:115]\n"
"s_cselect_b32 s58, 0, s58\n"
"v_exp_f32 v29, v165\n"
"v_mfma_f32_16x16x32_bf16 a[84:87], a[212:215], v[8:11], a[84:87]\n"
"v_add_u32 v42, s59, v39\n"
"v_cvt_pk_bf16_f32 v2, v28, v29\n"
"v_mfma_f32_16x16x32_bf16 a[116:119], a[212:215], v[12:15], a[116:119]\n"
"s_add_u32 s56, s57, 4096\n"
"v_exp_f32 v30, v166\n"
"v_mfma_f32_16x16x32_bf16 a[88:91], a[216:219], v[8:11], a[88:91]\n"
"v_cmp_nge_f32 vcc, s53, v54\n"
"v_mfma_f32_16x16x32_bf16 a[120:123], a[216:219], v[12:15], a[120:123]\n"
"s_and_b32 s56, s56, 0x3ffff\n"
"v_exp_f32 v31, v167\n"
"v_mfma_f32_16x16x32_bf16 a[92:95], a[220:223], v[8:11], a[92:95]\n"
"v_add_u32 v43, s59, v40\n"
"v_cvt_pk_bf16_f32 v3, v30, v31\n"
"v_mfma_f32_16x16x32_bf16 a[124:127], a[220:223], v[12:15], a[124:127]\n"
"v_exp_f32 v24, v168\n"
"v_mfma_f32_16x16x32_bf16 v[176:179], a[224:227], v[72:75], v[216:219]\n"
"s_cbranch_vccnz K1_RARE8\n"
"K1_BACK8:\n"
"v_mfma_f32_16x16x32_bf16 v[176:179], a[228:231], v[96:99], v[176:179]\n"
"v_exp_f32 v25, v169\n"
"v_mfma_f32_16x16x32_bf16 v[176:179], a[232:235], v[120:123], v[176:179]\n"
"v_add_u32 v41, s58, v38\n"
"v_cvt_pk_bf16_f32 v4, v24, v25\n"
"v_mfma_f32_16x16x32_bf16 v[176:179], a[236:239], v[144:147], v[176:179]\n"
"v_exp_f32 v26, v170\n"
"v_mfma_f32_16x16x32_bf16 v[180:183], a[240:243], v[72:75], v[216:219]\n"
"v_add_f32 v36, v36, v248\n"
"v_add_f32 v37, v37, v252\n"
"v_mfma_f32_16x16x32_bf16 v[180:183], a[244:247], v[96:99], v[180:183]\n"
"v_exp_f32 v27, v171\n"
"v_mfma_f32_16x16x32_bf16 v[180:183], a[248:251], v[120:123], v[180:183]\n"
"v_cvt_pk_bf16_f32 v5, v26, v27\n"
"v_mfma_f32_16x16x32_bf16 v[180:183], a[252:255], v[144:147], v[180:183]\n"
"v_exp_f32 v28, v172\n"
"v_mfma_f32_16x16x32_bf16 v[184:187], a[224:227], v[76:79], v[220:223]\n"
"v_mfma_f32_16x16x32_bf16 v[184:187], a[228:231], v[100:103], v[184:187]\n"
"v_exp_f32 v29, v173\n"
"v_mfma_f32_16x16x32_bf16 v[184:187], a[232:235], v[124:127], v[184:187]\n"
"v_cvt_pk_bf16_f32 v6, v28, v29\n"
"v_mfma_f32_16x16x32_bf16 v[184:187], a[236:239], v[148:151], v[184:187]\n"
"v_exp_f32 v30, v174\n"
"v_mfma_f32_16x16x32_bf16 v[188:191], a[240:243], v[76:79], v[220:223]\n"
"v_mfma_f32_16x16x32_bf16 v[188:191], a[244:247], v[100:103], v[188:191]\n"
"s_add_u32 s57, s56, 4096\n"
"v_exp_f32 v31, v175\n"
"v_mfma_f32_16x16x32_bf16 v[188:191], a[248:251], v[124:127], v[188:191]\n"
"s_and_b32 s57, s57, 0x3ffff\n"
"v_cvt_pk_bf16_f32 v7, v30, v31\n"
"v_mfma_f32_16x16x32_bf16 v[188:191], a[252:255], v[148:151], v[188:191]\n"
"s_sub_u32 s64, s64, 1\n"
"s_cmp_lg_u32 s64, 0\n"
"s_cbranch_scc1 K1_LOOPE\n"
"v_mfma_f32_16x16x32_bf16 a[128:131], a[192:195], v[16:19], a[128:131]\n"
"v_mfma_f32_16x16x32_bf16 a[160:163], a[192:195], v[20:23], a[160:163]\n"
"v_exp_f32 v24, v176\n"
"v_mfma_f32_16x16x32_bf16 v[232:235], v[58:61], v[0:3], 0\n"
"ds_read_b64_tr_b16 a[192:193], v42 offset:0\n"
"ds_read_b64_tr_b16 a[194:195], v42 offset:4096\n"
"v_mfma_f32_16x16x32_bf16 v[236:239], v[58:61], v[4:7], 0\n"
"v_exp_f32 v25, v177\n"
"v_mfma_f32_16x16x32_bf16 a[132:135], a[196:199], v[16:19], a[132:135]\n"
"v_cvt_pk_bf16_f32 v8, v24, v25\n"
"v_mfma_f32_16x16x32_bf16 a[164:167], a[196:199], v[20:23], a[164:167]\n"
"v_exp_f32 v26, v178\n"
"v_mfma_f32_16x16x32_bf16 a[136:139], a[200:203], v[16:19], a[136:139]\n"
"ds_read_b64_tr_b16 a[196:197], v43 offset:0\n"
"ds_read_b64_tr_b16 a[198:199], v43 offset:4096\n"
"v_mfma_f32_16x16x32_bf16 a[168:171], a[200:203], v[20:23], a[168:171]\n"
"v_exp_f32 v27, v179\n"
"v_mfma_f32_16x16x32_bf16 a[140:143], a[204:207], v[16:19], a[140:143]\n"
"ds_read_b64_tr_b16 a[200:201], v42 offset:512\n"
"v_cvt_pk_bf16_f32 v9, v26, v27\n"
"v_mfma_f32_16x16x32_bf16 a[172:175], a[204:207], v[20:23], a[172:175]\n"
"v_exp_f32 v28, v180\n"
"v_mfma_f32_16x16x32_bf16 a[144:147], a[208:211], v[16:19], a[144:147]\n"
"ds_read_b64_tr_b16 a[202:203], v42 offset:4608\n"
"v_max_f32 v54, v232, v236\n"
"v_mfma_f32_16x16x32_bf16 a[176:179], a[208:211], v[20:23], a[176:179]\n"
"v_exp_f32 v29, v181\n"
"v_mfma_f32_16x16x32_bf16 a[148:151], a[212:215], v[16:19], a[148:151]\n"
"ds_read_b64_tr_b16 a[204:205], v43 offset:512\n"
"v_cvt_pk_bf16_f32 v10, v28, v29\n"
"v_mfma_f32_16x16x32_bf16 a[180:183], a[212:215], v[20:23], a[180:183]\n"
"v_exp_f32 v30, v182\n"
"v_mfma_f32_16x16x32_bf16 a[152:155], a[216:219], v[16:19], a[152:155]\n"
"ds_read_b64_tr_b16 a[206:207], v43 offset:4608\n"
"v_cmp_nge_f32 vcc, s53, v54\n"
"v_mfma_f32_16x16x32_bf16 a[184:187], a[216:219], v[20:23], a[184:187]\n"
"v_exp_f32 v31, v183\n"
"v_mfma_f32_16x16x32_bf16 a[156:159], a[220:223], v[16:19], a[156:159]\n"
"ds_read_b64_tr_b16 a[208:209], v42 offset:1024\n"
"v_cvt_pk_bf16_f32 v11, v30, v31\n"
"v_mfma_f32_16x16x32_bf16 a[188:191], a[220:223], v[20:23], a[188:191]\n"
"v_exp_f32 v24, v184\n"
"v_mfma_f32_16x16x32_bf16 v[192:195], a[224:227], v[80:83], v[224:227]\n"
"s_cbranch_vccnz K1_RARE9\n"
"K1_BACK9:\n"
"v_mfma_f32_16x16x32_bf16 v[192:195], a[228:231], v[104:107], v[192:195]\n"
"v_exp_f32 v25, v185\n"
"v_mfma_f32_16x16x32_bf16 v[192:195], a[232:235], v[128:131], v[192:195]\n"
"ds_read_b64_tr_b16 a[210:211], v42 offset:5120\n"
"v_cvt_pk_bf16_f32 v12, v24, v25\n"
"v_mfma_f32_16x16x32_bf16 v[192:195], a[236:239], v[152:155], v[192:195]\n"
"v_exp_f32 v26, v186\n"
"v_mfma_f32_16x16x32_bf16 v[196:199], a[240:243], v[80:83], v[224:227]\n"
"v_add_f32 v32, v32, v232\n"
"v_add_f32 v33, v33, v236\n"
"v_mfma_f32_16x16x32_bf16 v[196:199], a[244:247], v[104:107], v[196:199]\n"
"v_exp_f32 v27, v187\n"
"v_mfma_f32_16x16x32_bf16 v[196:199], a[248:251], v[128:131], v[196:199]\n"
"ds_read_b64_tr_b16 a[212:213], v43 offset:1024\n"
"v_cvt_pk_bf16_f32 v13, v26, v27\n"
"v_mfma_f32_16x16x32_bf16 v[196:199], a[252:255], v[152:155], v[196:199]\n"
"v_exp_f32 v28, v188\n"
"v_mfma_f32_16x16x32_bf16 v[200:203], a[224:227], v[84:87], v[228:231]\n"
"ds_read_b64_tr_b16 a[214:215], v43 offset:5120\n"
"ds_read_b64_tr_b16 a[216:217], v42 offset:1536\n"
"v_mfma_f32_16x16x32_bf16 v[200:203], a[228:231], v[108:111], v[200:203]\n"
"v_exp_f32 v29, v189\n"
"v_mfma_f32_16x16x32_bf16 v[200:203], a[232:235], v[132:135], v[200:203]\n"
"ds_read_b64_tr_b16 a[218:219], v42 offset:5632\n"
"v_cvt_pk_bf16_f32 v14, v28, v29\n"
"v_mfma_f32_16x16x32_bf16 v[200:203], a[236:239], v[156:159], v[200:203]\n"
"v_exp_f32 v30, v190\n"
"v_mfma_f32_16x16x32_bf16 v[204:207], a[240:243], v[84:87], v[228:231]\n"
"ds_read_b64_tr_b16 a[220:221], v43 offset:1536\n"
"ds_read_b64_tr_b16 a[222:223], v43 offset:5632\n"
"v_mfma_f32_16x16x32_bf16 v[204:207], a[244:247], v[108:111], v[204:207]\n"
"v_exp_f32 v31, v191\n"
"v_mfma_f32_16x16x32_bf16 v[204:207], a[248:251], v[132:135], v[204:207]\n"
"v_cvt_pk_bf16_f32 v15, v30, v31\n"
"v_mfma_f32_16x16x32_bf16 v[204:207], a[252:255], v[156:159], v[204:207]\n"
"s_waitcnt lgkmcnt(0)\n"
"s_barrier\n"
"v_mfma_f32_16x16x32_bf16 a[0:3], a[192:195], v[0:3], a[0:3]\n"
"s_mul_i32 s65, s70, 81920\n"
"s_mul_i32 s66, s69, 12288\n"
"s_add_u32 s65, s65, s66\n"
"s_add_u32 s65, s65, 16384\n"
"v_add_u32 v41, s65, v56\n"
"v_add_u32 v42, 40960, v41\n"
"v_mfma_f32_16x16x32_bf16 a[32:35], a[192:195], v[4:7], a[32:35]\n"
"v_exp_f32 v24, v192\n"
"v_mfma_f32_16x16x32_bf16 v[240:243], v[58:61], v[8:11], 0\n"
"v_exp_f32 v25, v193\n"
"v_mfma_f32_16x16x32_bf16 v[244:247], v[58:61], v[12:15], 0\n"
"v_exp_f32 v26, v194\n"
"v_cvt_pk_bf16_f32 v16, v24, v25\n"
"v_mfma_f32_16x16x32_bf16 a[4:7], a[196:199], v[0:3], a[4:7]\n"
"v_exp_f32 v27, v195\n"
"v_mfma_f32_16x16x32_bf16 a[36:39], a[196:199], v[4:7], a[36:39]\n"
"v_exp_f32 v28, v196\n"
"v_cvt_pk_bf16_f32 v17, v26, v27\n"
"v_mfma_f32_16x16x32_bf16 a[8:11], a[200:203], v[0:3], a[8:11]\n"
"v_exp_f32 v29, v197\n"
"v_mfma_f32_16x16x32_bf16 a[40:43], a[200:203], v[4:7], a[40:43]\n"
"v_exp_f32 v30, v198\n"
"v_cvt_pk_bf16_f32 v18, v28, v29\n"
"v_mfma_f32_16x16x32_bf16 a[12:15], a[204:207], v[0:3], a[12:15]\n"
"v_exp_f32 v31, v199\n"
"v_mfma_f32_16x16x32_bf16 a[44:47], a[204:207], v[4:7], a[44:47]\n"
"v_exp_f32 v24, v200\n"
"v_cvt_pk_bf16_f32 v19, v30, v31\n"
"v_max_f32 v54, v240, v244\n"
"v_mfma_f32_16x16x32_bf16 a[16:19], a[208:211], v[0:3], a[16:19]\n"
"v_exp_f32 v25, v201\n"
"v_mfma_f32_16x16x32_bf16 a[48:51], a[208:211], v[4:7], a[48:51]\n"
"v_exp_f32 v26, v202\n"
"v_cvt_pk_bf16_f32 v20, v24, v25\n"
"v_cmp_nge_f32 vcc, s53, v54\n"
"v_mfma_f32_16x16x32_bf16 a[20:23], a[212:215], v[0:3], a[20:23]\n"
"v_exp_f32 v27, v203\n"
"v_mfma_f32_16x16x32_bf16 a[52:55], a[212:215], v[4:7], a[52:55]\n"
"v_exp_f32 v28, v204\n"
"v_cvt_pk_bf16_f32 v21, v26, v27\n"
"s_cbranch_vccnz K1_RARE10\n"
"K1_BACK10:\n"
"v_mfma_f32_16x16x32_bf16 a[24:27], a[216:219], v[0:3], a[24:27]\n"
"v_exp_f32 v29, v205\n"
"v_mfma_f32_16x16x32_bf16 a[56:59], a[216:219], v[4:7], a[56:59]\n"
"v_exp_f32 v30, v206\n"
"v_cvt_pk_bf16_f32 v22, v28, v29\n"
"v_add_f32 v34, v34, v240\n"
"v_add_f32 v35, v35, v244\n"
"v_mfma_f32_16x16x32_bf16 a[28:31], a[220:223], v[0:3], a[28:31]\n"
"v_exp_f32 v31, v207\n"
"v_mfma_f32_16x16x32_bf16 a[60:63], a[220:223], v[4:7], a[60:63]\n"
"v_cvt_pk_bf16_f32 v23, v30, v31\n"
"v_mfma_f32_16x16x32_bf16 a[64:67], a[192:195], v[8:11], a[64:67]\n"
"s_add_u32 s66, s80, 0\n"
"s_add_u32 s66, s66, s82\n"
"s_lshl_b32 s66, s66, 17\n"
"s_or_b32 s66, s66, s81\n"
"s_add_u32 m0, s68, 0\n"
"s_nop 0\n"
"buffer_load_dwordx4 v44, s[40:43], s66 offen offset:0 lds\n"
"buffer_load_dwordx4 v45, s[40:43], s66 offen offset:1024 lds\n"
"v_mfma_f32_16x16x32_bf16 a[96:99], a[192:195], v[12:15], a[96:99]\n"
"buffer_load_dwordx4 v46, s[40:43], s66 offen offset:2048 lds\n"
"v_mfma_f32_16x16x32_bf16 v[248:251], v[58:61], v[16:19], 0\n"
"buffer_load_dwordx4 v47, s[40:43], s66 offen offset:3072 lds\n"
"s_add_u32 s66, s80, 0\n"
"s_add_u32 s66, s66, s82\n"
"s_lshl_b32 s66, s66, 17\n"
"s_or_b32 s66, s66, s81\n"
"s_xor_b32 s66, s66, 128\n"
"s_add_u32 m0, s68, 40960\n"
"s_nop 0\n"
"buffer_load_dwordx4 v44, s[40:43], s66 offen offset:0 lds\n"
"v_mfma_f32_16x16x32_bf16 v[252:255], v[58:61], v[20:23], 0\n"
"buffer_load_dwordx4 v45, s[40:43], s66 offen offset:1024 lds\n"
"v_mfma_f32_16x16x32_bf16 a[68:71], a[196:199], v[8:11], a[68:71]\n"
"buffer_load_dwordx4 v46, s[40:43], s66 offen offset:2048 lds\n"
"buffer_load_dwordx4 v47, s[40:43], s66 offen offset:3072 lds\n"
"v_mfma_f32_16x16x32_bf16 a[100:103], a[196:199], v[12:15], a[100:103]\n"
"s_add_u32 s66, s80, 1\n"
"s_add_u32 s66, s66, s82\n"
"s_lshl_b32 s66, s66, 17\n"
"s_or_b32 s66, s66, s81\n"
"s_add_u32 m0, s68, 81920\n"
"s_nop 0\n"
"buffer_load_dwordx4 v44, s[40:43], s66 offen offset:0 lds\n"
"v_mfma_f32_16x16x32_bf16 a[72:75], a[200:203], v[8:11], a[72:75]\n"
"buffer_load_dwordx4 v45, s[40:43], s66 offen offset:1024 lds\n"
"buffer_load_dwordx4 v46, s[40:43], s66 offen offset:2048 lds\n"
"v_mfma_f32_16x16x32_bf16 a[104:107], a[200:203], v[12:15], a[104:107]\n"
"buffer_load_dwordx4 v47, s[40:43], s66 offen offset:3072 lds\n"
"v_mfma_f32_16x16x32_bf16 a[76:79], a[204:207], v[8:11], a[76:79]\n"
"s_add_u32 s66, s80, 1\n"
"s_add_u32 s66, s66, s82\n"
"s_lshl_b32 s66, s66, 17\n"
"s_or_b32 s66, s66, s81\n"
"s_xor_b32 s66, s66, 128\n"
"s_add_u32 m0, s68, 122880\n"
"s_nop 0\n"
"buffer_load_dwordx4 v44, s[40:43], s66 offen offset:0 lds\n"
"buffer_load_dwordx4 v45, s[40:43], s66 offen offset:1024 lds\n"
"v_mfma_f32_16x16x32_bf16 a[108:111], a[204:207], v[12:15], a[108:111]\n"
"buffer_load_dwordx4 v46, s[40:43], s66 offen offset:2048 lds\n"
"v_mfma_f32_16x16x32_bf16 a[80:83], a[208:211], v[8:11], a[80:83]\n"
"buffer_load_dwordx4 v47, s[40:43], s66 offen offset:3072 lds\n"
"v_mfma_f32_16x16x32_bf16 a[112:115], a[208:211], v[12:15], a[112:115]\n"
"v_rcp_f32 v24, v32\n"
"v_mfma_f32_16x16x32_bf16 a[84:87], a[212:215], v[8:11], a[84:87]\n"
"v_max_f32 v54, v248, v252\n"
"v_accvgpr_read_b32 v64, a0\n"
"v_mfma_f32_16x16x32_bf16 a[116:119], a[212:215], v[12:15], a[116:119]\n"
"v_cmp_nge_f32 vcc, s53, v54\n"
"v_accvgpr_read_b32 v65, a1\n"
"v_mfma_f32_16x16x32_bf16 a[88:91], a[216:219], v[8:11], a[88:91]\n"
"v_accvgpr_read_b32 v66, a2\n"
"v_accvgpr_read_b32 v67, a3\n"
"v_accvgpr_read_b32 v68, a4\n"
"v_mfma_f32_16x16x32_bf16 a[120:123], a[216:219], v[12:15], a[120:123]\n"
"s_cbranch_vccnz K1_RARE11\n"
"K1_BACK11:\n"
"v_accvgpr_read_b32 v69, a5\n"
"v_accvgpr_read_b32 v70, a6\n"
"v_mfma_f32_16x16x32_bf16 a[92:95], a[220:223], v[8:11], a[92:95]\n"
"v_add_f32 v36, v36, v248\n"
"v_add_f32 v37, v37, v252\n"
"v_mfma_f32_16x16x32_bf16 a[124:127], a[220:223], v[12:15], a[124:127]\n"
"v_accvgpr_read_b32 v71, a7\n"
"v_mul_f32 v64, v64, v24\n"
"v_mfma_f32_16x16x32_bf16 a[128:131], a[192:195], v[16:19], a[128:131]\n"
"v_mul_f32 v65, v65, v24\n"
"v_mul_f32 v66, v66, v24\n"
"v_mfma_f32_16x16x32_bf16 a[160:163], a[192:195], v[20:23], a[160:163]\n"
"v_mul_f32 v67, v67, v24\n"
"v_mul_f32 v68, v68, v24\n"
"v_mfma_f32_16x16x32_bf16 a[132:135], a[196:199], v[16:19], a[132:135]\n"
"v_mul_f32 v69, v69, v24\n"
"v_mul_f32 v70, v70, v24\n"
"v_mfma_f32_16x16x32_bf16 a[164:167], a[196:199], v[20:23], a[164:167]\n"
"v_mul_f32 v71, v71, v24\n"
"v_cvt_pk_bf16_f32 v128, v64, v65\n"
"v_mfma_f32_16x16x32_bf16 a[136:139], a[200:203], v[16:19], a[136:139]\n"
"v_cvt_pk_bf16_f32 v129, v66, v67\n"
"v_cvt_pk_bf16_f32 v130, v68, v69\n"
"v_cvt_pk_bf16_f32 v131, v70, v71\n"
"v_mfma_f32_16x16x32_bf16 a[168:171], a[200:203], v[20:23], a[168:171]\n"
"ds_write_b128 v41, v[128:131] offset:0\n"
"v_accvgpr_read_b32 v72, a8\n"
"v_mfma_f32_16x16x32_bf16 a[140:143], a[204:207], v[16:19], a[140:143]\n"
"v_accvgpr_read_b32 v73, a9\n"
"v_accvgpr_read_b32 v74, a10\n"
"v_mfma_f32_16x16x32_bf16 a[172:175], a[204:207], v[20:23], a[172:175]\n"
"v_accvgpr_read_b32 v75, a11\n"
"v_accvgpr_read_b32 v76, a12\n"
"v_mfma_f32_16x16x32_bf16 a[144:147], a[208:211], v[16:19], a[144:147]\n"
"v_accvgpr_read_b32 v77, a13\n"
"v_accvgpr_read_b32 v78, a14\n"
"v_mfma_f32_16x16x32_bf16 a[176:179], a[208:211], v[20:23], a[176:179]\n"
"v_accvgpr_read_b32 v79, a15\n"
"v_mul_f32 v72, v72, v24\n"
"v_mfma_f32_16x16x32_bf16 a[148:151], a[212:215], v[16:19], a[148:151]\n"
"v_mul_f32 v73, v73, v24\n"
"v_mul_f32 v74, v74, v24\n"
"v_mfma_f32_16x16x32_bf16 a[180:183], a[212:215], v[20:23], a[180:183]\n"
"v_mul_f32 v75, v75, v24\n"
"v_mul_f32 v76, v76, v24\n"
"v_mfma_f32_16x16x32_bf16 a[152:155], a[216:219], v[16:19], a[152:155]\n"
"v_mul_f32 v77, v77, v24\n"
"v_mul_f32 v78, v78, v24\n"
"v_mul_f32 v79, v79, v24\n"
"v_mfma_f32_16x16x32_bf16 a[184:187], a[216:219], v[20:23], a[184:187]\n"
"v_cvt_pk_bf16_f32 v132, v72, v73\n"
"v_cvt_pk_bf16_f32 v133, v74, v75\n"
"v_mfma_f32_16x16x32_bf16 a[156:159], a[220:223], v[16:19], a[156:159]\n"
"v_cvt_pk_bf16_f32 v134, v76, v77\n"
"v_cvt_pk_bf16_f32 v135, v78, v79\n"
"v_mfma_f32_16x16x32_bf16 a[188:191], a[220:223], v[20:23], a[188:191]\n"
"s_movk_i32 s65, 0\n"
"buffer_store_dwordx4 v[128:131], v56, s[44:47], s65 offen sc1\n"
"ds_write_b128 v41, v[132:135] offset:6144\n"
"v_accvgpr_read_b32 v80, a16\n"
"v_accvgpr_read_b32 v81, a17\n"
"v_accvgpr_read_b32 v82, a18\n"
"v_accvgpr_read_b32 v83, a19\n"
"v_accvgpr_read_b32 v84, a20\n"
"v_accvgpr_read_b32 v85, a21\n"
"v_accvgpr_read_b32 v86, a22\n"
"v_accvgpr_read_b32 v87, a23\n"
"v_mul_f32 v80, v80, v24\n"
"v_mul_f32 v81, v81, v24\n"
"v_mul_f32 v82, v82, v24\n"
"v_mul_f32 v83, v83, v24\n"
"v_mul_f32 v84, v84, v24\n"
"v_mul_f32 v85, v85, v24\n"
"v_mul_f32 v86, v86, v24\n"
"v_mul_f32 v87, v87, v24\n"
"v_cvt_pk_bf16_f32 v136, v80, v81\n"
"v_cvt_pk_bf16_f32 v137, v82, v83\n"
"v_cvt_pk_bf16_f32 v138, v84, v85\n"
"v_cvt_pk_bf16_f32 v139, v86, v87\n"
"ds_write_b128 v42, v[136:139] offset:0\n"
"v_accvgpr_read_b32 v88, a24\n"
"v_accvgpr_read_b32 v89, a25\n"
"v_accvgpr_read_b32 v90, a26\n"
"v_accvgpr_read_b32 v91, a27\n"
"v_accvgpr_read_b32 v92, a28\n"
"v_accvgpr_read_b32 v93, a29\n"
"v_accvgpr_read_b32 v94, a30\n"
"v_accvgpr_read_b32 v95, a31\n"
"v_mul_f32 v88, v88, v24\n"
"v_mul_f32 v89, v89, v24\n"
"v_mul_f32 v90, v90, v24\n"
"v_mul_f32 v91, v91, v24\n"
"v_mul_f32 v92, v92, v24\n"
"v_mul_f32 v93, v93, v24\n"
"v_mul_f32 v94, v94, v24\n"
"v_mul_f32 v95, v95, v24\n"
"v_cvt_pk_bf16_f32 v140, v88, v89\n"
"v_cvt_pk_bf16_f32 v141, v90, v91\n"
"v_cvt_pk_bf16_f32 v142, v92, v93\n"
"v_cvt_pk_bf16_f32 v143, v94, v95\n"
"s_movk_i32 s65, 6144\n"
"buffer_store_dwordx4 v[132:135], v56, s[44:47], s65 offen sc1\n"
"ds_write_b128 v42, v[140:143] offset:6144\n"
"v_rcp_f32 v26, v33\n"
"v_accvgpr_read_b32 v96, a32\n"
"v_accvgpr_read_b32 v97, a33\n"
"v_accvgpr_read_b32 v98, a34\n"
"v_accvgpr_read_b32 v99, a35\n"
"v_accvgpr_read_b32 v100, a36\n"
"v_accvgpr_read_b32 v101, a37\n"
"v_accvgpr_read_b32 v102, a38\n"
"v_accvgpr_read_b32 v103, a39\n"
"v_pk_mul_f32 v[96:97], v[96:97], v[26:27] op_sel_hi:[1,0]\n"
"v_pk_mul_f32 v[98:99], v[98:99], v[26:27] op_sel_hi:[1,0]\n"
"v_pk_mul_f32 v[100:101], v[100:101], v[26:27] op_sel_hi:[1,0]\n"
"v_pk_mul_f32 v[102:103], v[102:103], v[26:27] op_sel_hi:[1,0]\n"
"v_cvt_pk_bf16_f32 v176, v96, v97\n"
"v_cvt_pk_bf16_f32 v177, v98, v99\n"
"v_cvt_pk_bf16_f32 v178, v100, v101\n"
"v_cvt_pk_bf16_f32 v179, v102, v103\n"
"ds_write_b128 v41, v[176:179] offset:1024\n"
"v_accvgpr_read_b32 v104, a40\n"
"v_accvgpr_read_b32 v105, a41\n"
"v_accvgpr_read_b32 v106, a42\n"
"v_accvgpr_read_b32 v107, a43\n"
"v_accvgpr_read_b32 v108, a44\n"
"v_accvgpr_read_b32 v109, a45\n"
"v_accvgpr_read_b32 v110, a46\n"
"v_accvgpr_read_b32 v111, a47\n"
"v_pk_mul_f32 v[104:105], v[104:105], v[26:27] op_sel_hi:[1,0]\n"
"v_pk_mul_f32 v[106:107], v[106:107], v[26:27] op_sel_hi:[1,0]\n"
"v_pk_mul_f32 v[108:109], v[108:109], v[26:27] op_sel_hi:[1,0]\n"
"v_pk_mul_f32 v[110:111], v[110:111], v[26:27] op_sel_hi:[1,0]\n"
"v_cvt_pk_bf16_f32 v180, v104, v105\n"
"v_cvt_pk_bf16_f32 v181, v106, v107\n"
"v_cvt_pk_bf16_f32 v182, v108, v109\n"
"v_cvt_pk_bf16_f32 v183, v110, v111\n"
"s_movk_i32 s65, 12288\n"
"buffer_store_dwordx4 v[136:139], v56, s[44:47], s65 offen sc1\n"
"ds_write_b128 v41, v[180:183] offset:7168\n"
"v_accvgpr_read_b32 v112, a48\n"
"v_accvgpr_read_b32 v113, a49\n"
"v_accvgpr_read_b32 v114, a50\n"
"v_accvgpr_read_b32 v115, a51\n"
"v_accvgpr_read_b32 v116, a52\n"
"v_accvgpr_read_b32 v117, a53\n"
"v_accvgpr_read_b32 v118, a54\n"
"v_accvgpr_read_b32 v119, a55\n"
"v_pk_mul_f32 v[112:113], v[112:113], v[26:27] op_sel_hi:[1,0]\n"
"v_pk_mul_f32 v[114:115], v[114:115], v[26:27] op_sel_hi:[1,0]\n"
"v_pk_mul_f32 v[116:117], v[116:117], v[26:27] op_sel_hi:[1,0]\n"
"v_pk_mul_f32 v[118:119], v[118:119], v[26:27] op_sel_hi:[1,0]\n"
"v_cvt_pk_bf16_f32 v184, v112, v113\n"
"v_cvt_pk_bf16_f32 v185, v114, v115\n"
"v_cvt_pk_bf16_f32 v186, v116, v117\n"
"v_cvt_pk_bf16_f32 v187, v118, v119\n"
"s_movk_i32 s65, 18432\n"
"buffer_store_dwordx4 v[140:143], v56, s[44:47], s65 offen sc1\n"
"ds_write_b128 v42, v[184:187] offset:1024\n"
"v_accvgpr_read_b32 v120, a56\n"
"v_accvgpr_read_b32 v121, a57\n"
"v_accvgpr_read_b32 v122, a58\n"
"v_accvgpr_read_b32 v123, a59\n"
"v_accvgpr_read_b32 v124, a60\n"
"v_accvgpr_read_b32 v125, a61\n"
"v_accvgpr_read_b32 v126, a62\n"
"v_accvgpr_read_b32 v127, a63\n"
"v_pk_mul_f32 v[120:121], v[120:121], v[26:27] op_sel_hi:[1,0]\n"
"v_pk_mul_f32 v[122:123], v[122:123], v[26:27] op_sel_hi:[1,0]\n"
"v_pk_mul_f32 v[124:125], v[124:125], v[26:27] op_sel_hi:[1,0]\n"
"v_pk_mul_f32 v[126:127], v[126:127], v[26:27] op_sel_hi:[1,0]\n"
"v_cvt_pk_bf16_f32 v188, v120, v121\n"
"v_cvt_pk_bf16_f32 v189, v122, v123\n"
"v_cvt_pk_bf16_f32 v190, v124, v125\n"
"v_cvt_pk_bf16_f32 v191, v126, v127\n"
"ds_write_b128 v42, v[188:191] offset:7168\n"
"v_rcp_f32 v24, v34\n"
"v_accvgpr_read_b32 v64, a64\n"
"v_accvgpr_read_b32 v65, a65\n"
"v_accvgpr_read_b32 v66, a66\n"
"v_accvgpr_read_b32 v67, a67\n"
"v_accvgpr_read_b32 v68, a68\n"
"v_accvgpr_read_b32 v69, a69\n"
"v_accvgpr_read_b32 v70, a70\n"
"v_accvgpr_read_b32 v71, a71\n"
"v_pk_mul_f32 v[64:65], v[64:65], v[24:25] op_sel_hi:[1,0]\n"
"v_pk_mul_f32 v[66:67], v[66:67], v[24:25] op_sel_hi:[1,0]\n"
"v_pk_mul_f32 v[68:69], v[68:69], v[24:25] op_sel_hi:[1,0]\n"
"v_pk_mul_f32 v[70:71], v[70:71], v[24:25] op_sel_hi:[1,0]\n"
"v_cvt_pk_bf16_f32 v192, v64, v65\n"
"v_cvt_pk_bf16_f32 v193, v66, v67\n"
"v_cvt_pk_bf16_f32 v194, v68, v69\n"
"v_cvt_pk_bf16_f32 v195, v70, v71\n"
"s_movk_i32 s65, 1024\n"
"buffer_store_dwordx4 v[176:179], v56, s[44:47], s65 offen sc1\n"
"ds_write_b128 v41, v[192:195] offset:2048\n"
"v_accvgpr_read_b32 v72, a72\n"
"v_accvgpr_read_b32 v73, a73\n"
"v_accvgpr_read_b32 v74, a74\n"
"v_accvgpr_read_b32 v75, a75\n"
"v_accvgpr_read_b32 v76, a76\n"
"v_accvgpr_read_b32 v77, a77\n"
"v_accvgpr_read_b32 v78, a78\n"
"v_accvgpr_read_b32 v79, a79\n"
"v_pk_mul_f32 v[72:73], v[72:73], v[24:25] op_sel_hi:[1,0]\n"
"v_pk_mul_f32 v[74:75], v[74:75], v[24:25] op_sel_hi:[1,0]\n"
"v_pk_mul_f32 v[76:77], v[76:77], v[24:25] op_sel_hi:[1,0]\n"
"v_pk_mul_f32 v[78:79], v[78:79], v[24:25] op_sel_hi:[1,0]\n"
"v_cvt_pk_bf16_f32 v196, v72, v73\n"
"v_cvt_pk_bf16_f32 v197, v74, v75\n"
"v_cvt_pk_bf16_f32 v198, v76, v77\n"
"v_cvt_pk_bf16_f32 v199, v78, v79\n"
"ds_write_b128 v41, v[196:199] offset:8192\n"
"v_accvgpr_read_b32 v80, a80\n"
"v_accvgpr_read_b32 v81, a81\n"
"v_accvgpr_read_b32 v82, a82\n"
"v_accvgpr_read_b32 v83, a83\n"
"v_accvgpr_read_b32 v84, a84\n"
"v_accvgpr_read_b32 v85, a85\n"
"v_accvgpr_read_b32 v86, a86\n"
"v_accvgpr_read_b32 v87, a87\n"
"v_pk_mul_f32 v[80:81], v[80:81], v[24:25] op_sel_hi:[1,0]\n"
"v_pk_mul_f32 v[82:83], v[82:83], v[24:25] op_sel_hi:[1,0]\n"
"v_pk_mul_f32 v[84:85], v[84:85], v[24:25] op_sel_hi:[1,0]\n"
"v_pk_mul_f32 v[86:87], v[86:87], v[24:25] op_sel_hi:[1,0]\n"
"v_cvt_pk_bf16_f32 v200, v80, v81\n"
"v_cvt_pk_bf16_f32 v201, v82, v83\n"
"v_cvt_pk_bf16_f32 v202, v84, v85\n"
"v_cvt_pk_bf16_f32 v203, v86, v87\n"
"s_movk_i32 s65, 7168\n"
"buffer_store_dwordx4 v[180:183], v56, s[44:47], s65 offen sc1\n"
"ds_write_b128 v42, v[200:203] offset:2048\n"
"v_accvgpr_read_b32 v88, a88\n"
"v_accvgpr_read_b32 v89, a89\n"
"v_accvgpr_read_b32 v90, a90\n"
"v_accvgpr_read_b32 v91, a91\n"
"v_accvgpr_read_b32 v92, a92\n"
"v_accvgpr_read_b32 v93, a93\n"
"v_accvgpr_read_b32 v94, a94\n"
"v_accvgpr_read_b32 v95, a95\n"
"v_pk_mul_f32 v[88:89], v[88:89], v[24:25] op_sel_hi:[1,0]\n"
"v_pk_mul_f32 v[90:91], v[90:91], v[24:25] op_sel_hi:[1,0]\n"
"v_pk_mul_f32 v[92:93], v[92:93], v[24:25] op_sel_hi:[1,0]\n"
"v_pk_mul_f32 v[94:95], v[94:95], v[24:25] op_sel_hi:[1,0]\n"
"v_cvt_pk_bf16_f32 v204, v88, v89\n"
"v_cvt_pk_bf16_f32 v205, v90, v91\n"
"v_cvt_pk_bf16_f32 v206, v92, v93\n"
"v_cvt_pk_bf16_f32 v207, v94, v95\n"
"s_movk_i32 s65, 13312\n"
"buffer_store_dwordx4 v[184:187], v56, s[44:47], s65 offen sc1\n"
"ds_write_b128 v42, v[204:207] offset:8192\n"
"v_rcp_f32 v26, v35\n"
"v_accvgpr_read_b32 v96, a96\n"
"v_accvgpr_read_b32 v97, a97\n"
"v_accvgpr_read_b32 v98, a98\n"
"v_accvgpr_read_b32 v99, a99\n"
"v_accvgpr_read_b32 v100, a100\n"
"v_accvgpr_read_b32 v101, a101\n"
"v_accvgpr_read_b32 v102, a102\n"
"v_accvgpr_read_b32 v103, a103\n"
"v_pk_mul_f32 v[96:97], v[96:97], v[26:27] op_sel_hi:[1,0]\n"
"v_pk_mul_f32 v[98:99], v[98:99], v[26:27] op_sel_hi:[1,0]\n"
"v_pk_mul_f32 v[100:101], v[100:101], v[26:27] op_sel_hi:[1,0]\n"
"v_pk_mul_f32 v[102:103], v[102:103], v[26:27] op_sel_hi:[1,0]\n"
"v_cvt_pk_bf16_f32 v208, v96, v97\n"
"v_cvt_pk_bf16_f32 v209, v98, v99\n"
"v_cvt_pk_bf16_f32 v210, v100, v101\n"
"v_cvt_pk_bf16_f32 v211, v102, v103\n"
"ds_write_b128 v41, v[208:211] offset:3072\n"
"v_accvgpr_read_b32 v104, a104\n"
"v_accvgpr_read_b32 v105, a105\n"
"v_accvgpr_read_b32 v106, a106\n"
"v_accvgpr_read_b32 v107, a107\n"
"v_accvgpr_read_b32 v108, a108\n"
"v_accvgpr_read_b32 v109, a109\n"
"v_accvgpr_read_b32 v110, a110\n"
"v_accvgpr_read_b32 v111, a111\n"
"v_pk_mul_f32 v[104:105], v[104:105], v[26:27] op_sel_hi:[1,0]\n"
"v_pk_mul_f32 v[106:107], v[106:107], v[26:27] op_sel_hi:[1,0]\n"
"v_pk_mul_f32 v[108:109], v[108:109], v[26:27] op_sel_hi:[1,0]\n"
"v_pk_mul_f32 v[110:111], v[110:111], v[26:27] op_sel_hi:[1,0]\n"
"v_cvt_pk_bf16_f32 v212, v104, v105\n"
"v_cvt_pk_bf16_f32 v213, v106, v107\n"
"v_cvt_pk_bf16_f32 v214, v108, v109\n"
"v_cvt_pk_bf16_f32 v215, v110, v111\n"
"s_movk_i32 s65, 19456\n"
"buffer_store_dwordx4 v[188:191], v56, s[44:47], s65 offen sc1\n"
"ds_write_b128 v41, v[212:215] offset:9216\n"
"v_accvgpr_read_b32 v112, a112\n"
"v_accvgpr_read_b32 v113, a113\n"
"v_accvgpr_read_b32 v114, a114\n"
"v_accvgpr_read_b32 v115, a115\n"
"v_accvgpr_read_b32 v116, a116\n"
"v_accvgpr_read_b32 v117, a117\n"
"v_accvgpr_read_b32 v118, a118\n"
"v_accvgpr_read_b32 v119, a119\n"
"v_pk_mul_f32 v[112:113], v[112:113], v[26:27] op_sel_hi:[1,0]\n"
"v_pk_mul_f32 v[114:115], v[114:115], v[26:27] op_sel_hi:[1,0]\n"
"v_pk_mul_f32 v[116:117], v[116:117], v[26:27] op_sel_hi:[1,0]\n"
"v_pk_mul_f32 v[118:119], v[118:119], v[26:27] op_sel_hi:[1,0]\n"
"v_cvt_pk_bf16_f32 v216, v112, v113\n"
"v_cvt_pk_bf16_f32 v217, v114, v115\n"
"v_cvt_pk_bf16_f32 v218, v116, v117\n"
"v_cvt_pk_bf16_f32 v219, v118, v119\n"
"ds_write_b128 v42, v[216:219] offset:3072\n"
"v_accvgpr_read_b32 v120, a120\n"
"v_accvgpr_read_b32 v121, a121\n"
"v_accvgpr_read_b32 v122, a122\n"
"v_accvgpr_read_b32 v123, a123\n"
"v_accvgpr_read_b32 v124, a124\n"
"v_accvgpr_read_b32 v125, a125\n"
"v_accvgpr_read_b32 v126, a126\n"
"v_accvgpr_read_b32 v127, a127\n"
"v_pk_mul_f32 v[120:121], v[120:121], v[26:27] op_sel_hi:[1,0]\n"
"v_pk_mul_f32 v[122:123], v[122:123], v[26:27] op_sel_hi:[1,0]\n"
"v_pk_mul_f32 v[124:125], v[124:125], v[26:27] op_sel_hi:[1,0]\n"
"v_pk_mul_f32 v[126:127], v[126:127], v[26:27] op_sel_hi:[1,0]\n"
"v_cvt_pk_bf16_f32 v220, v120, v121\n"
"v_cvt_pk_bf16_f32 v221, v122, v123\n"
"v_cvt_pk_bf16_f32 v222, v124, v125\n"
"v_cvt_pk_bf16_f32 v223, v126, v127\n"
"s_movk_i32 s65, 2048\n"
"buffer_store_dwordx4 v[192:195], v56, s[44:47], s65 offen sc1\n"
"ds_write_b128 v42, v[220:223] offset:9216\n"
"v_rcp_f32 v24, v36\n"
"v_accvgpr_read_b32 v64, a128\n"
"v_accvgpr_read_b32 v65, a129\n"
"v_accvgpr_read_b32 v66, a130\n"
"v_accvgpr_read_b32 v67, a131\n"
"v_accvgpr_read_b32 v68, a132\n"
"v_accvgpr_read_b32 v69, a133\n"
"v_accvgpr_read_b32 v70, a134\n"
"v_accvgpr_read_b32 v71, a135\n"
"v_pk_mul_f32 v[64:65], v[64:65], v[24:25] op_sel_hi:[1,0]\n"
"v_pk_mul_f32 v[66:67], v[66:67], v[24:25] op_sel_hi:[1,0]\n"
"v_pk_mul_f32 v[68:69], v[68:69], v[24:25] op_sel_hi:[1,0]\n"
"v_pk_mul_f32 v[70:71], v[70:71], v[24:25] op_sel_hi:[1,0]\n"
"v_cvt_pk_bf16_f32 v224, v64, v65\n"
"v_cvt_pk_bf16_f32 v225, v66, v67\n"
"v_cvt_pk_bf16_f32 v226, v68, v69\n"
"v_cvt_pk_bf16_f32 v227, v70, v71\n"
"ds_write_b128 v41, v[224:227] offset:4096\n"
"v_accvgpr_read_b32 v72, a136\n"
"v_accvgpr_read_b32 v73, a137\n"
"v_accvgpr_read_b32 v74, a138\n"
"v_accvgpr_read_b32 v75, a139\n"
"v_accvgpr_read_b32 v76, a140\n"
"v_accvgpr_read_b32 v77, a141\n"
"v_accvgpr_read_b32 v78, a142\n"
"v_accvgpr_read_b32 v79, a143\n"
"v_pk_mul_f32 v[72:73], v[72:73], v[24:25] op_sel_hi:[1,0]\n"
"v_pk_mul_f32 v[74:75], v[74:75], v[24:25] op_sel_hi:[1,0]\n"
"v_pk_mul_f32 v[76:77], v[76:77], v[24:25] op_sel_hi:[1,0]\n"
"v_pk_mul_f32 v[78:79], v[78:79], v[24:25] op_sel_hi:[1,0]\n"
"v_cvt_pk_bf16_f32 v228, v72, v73\n"
"v_cvt_pk_bf16_f32 v229, v74, v75\n"
"v_cvt_pk_bf16_f32 v230, v76, v77\n"
"v_cvt_pk_bf16_f32 v231, v78, v79\n"
"s_movk_i32 s65, 8192\n"
"buffer_store_dwordx4 v[196:199], v56, s[44:47], s65 offen sc1\n"
"ds_write_b128 v41, v[228:231] offset:10240\n"
"v_accvgpr_read_b32 v80, a144\n"
"v_accvgpr_read_b32 v81, a145\n"
"v_accvgpr_read_b32 v82, a146\n"
"v_accvgpr_read_b32 v83, a147\n"
"v_accvgpr_read_b32 v84, a148\n"
"v_accvgpr_read_b32 v85, a149\n"
"v_accvgpr_read_b32 v86, a150\n"
"v_accvgpr_read_b32 v87, a151\n"
"v_pk_mul_f32 v[80:81], v[80:81], v[24:25] op_sel_hi:[1,0]\n"
"v_pk_mul_f32 v[82:83], v[82:83], v[24:25] op_sel_hi:[1,0]\n"
"v_pk_mul_f32 v[84:85], v[84:85], v[24:25] op_sel_hi:[1,0]\n"
"v_pk_mul_f32 v[86:87], v[86:87], v[24:25] op_sel_hi:[1,0]\n"
"v_cvt_pk_bf16_f32 v232, v80, v81\n"
"v_cvt_pk_bf16_f32 v233, v82, v83\n"
"v_cvt_pk_bf16_f32 v234, v84, v85\n"
"v_cvt_pk_bf16_f32 v235, v86, v87\n"
"s_movk_i32 s65, 14336\n"
"buffer_store_dwordx4 v[200:203], v56, s[44:47], s65 offen sc1\n"
"ds_write_b128 v42, v[232:235] offset:4096\n"
"v_accvgpr_read_b32 v88, a152\n"
"v_accvgpr_read_b32 v89, a153\n"
"v_accvgpr_read_b32 v90, a154\n"
"v_accvgpr_read_b32 v91, a155\n"
"v_accvgpr_read_b32 v92, a156\n"
"v_accvgpr_read_b32 v93, a157\n"
"v_accvgpr_read_b32 v94, a158\n"
"v_accvgpr_read_b32 v95, a159\n"
"v_pk_mul_f32 v[88:89], v[88:89], v[24:25] op_sel_hi:[1,0]\n"
"v_pk_mul_f32 v[90:91], v[90:91], v[24:25] op_sel_hi:[1,0]\n"
"v_pk_mul_f32 v[92:93], v[92:93], v[24:25] op_sel_hi:[1,0]\n"
"v_pk_mul_f32 v[94:95], v[94:95], v[24:25] op_sel_hi:[1,0]\n"
"v_cvt_pk_bf16_f32 v236, v88, v89\n"
"v_cvt_pk_bf16_f32 v237, v90, v91\n"
"v_cvt_pk_bf16_f32 v238, v92, v93\n"
"v_cvt_pk_bf16_f32 v239, v94, v95\n"
"ds_write_b128 v42, v[236:239] offset:10240\n"
"v_rcp_f32 v26, v37\n"
"v_accvgpr_read_b32 v96, a160\n"
"v_accvgpr_read_b32 v97, a161\n"
"v_accvgpr_read_b32 v98, a162\n"
"v_accvgpr_read_b32 v99, a163\n"
"v_accvgpr_read_b32 v100, a164\n"
"v_accvgpr_read_b32 v101, a165\n"
"v_accvgpr_read_b32 v102, a166\n"
"v_accvgpr_read_b32 v103, a167\n"
"v_pk_mul_f32 v[96:97], v[96:97], v[26:27] op_sel_hi:[1,0]\n"
"v_pk_mul_f32 v[98:99], v[98:99], v[26:27] op_sel_hi:[1,0]\n"
"v_pk_mul_f32 v[100:101], v[100:101], v[26:27] op_sel_hi:[1,0]\n"
"v_pk_mul_f32 v[102:103], v[102:103], v[26:27] op_sel_hi:[1,0]\n"
"v_cvt_pk_bf16_f32 v240, v96, v97\n"
"v_cvt_pk_bf16_f32 v241, v98, v99\n"
"v_cvt_pk_bf16_f32 v242, v100, v101\n"
"v_cvt_pk_bf16_f32 v243, v102, v103\n"
"s_movk_i32 s65, 20480\n"
"buffer_store_dwordx4 v[204:207], v56, s[44:47], s65 offen sc1\n"
"ds_write_b128 v41, v[240:243] offset:5120\n"
"v_accvgpr_read_b32 v104, a168\n"
"v_accvgpr_read_b32 v105, a169\n"
"v_accvgpr_read_b32 v106, a170\n"
"v_accvgpr_read_b32 v107, a171\n"
"v_accvgpr_read_b32 v108, a172\n"
"v_accvgpr_read_b32 v109, a173\n"
"v_accvgpr_read_b32 v110, a174\n"
"v_accvgpr_read_b32 v111, a175\n"
"v_pk_mul_f32 v[104:105], v[104:105], v[26:27] op_sel_hi:[1,0]\n"
"v_pk_mul_f32 v[106:107], v[106:107], v[26:27] op_sel_hi:[1,0]\n"
"v_pk_mul_f32 v[108:109], v[108:109], v[26:27] op_sel_hi:[1,0]\n"
"v_pk_mul_f32 v[110:111], v[110:111], v[26:27] op_sel_hi:[1,0]\n"
"v_cvt_pk_bf16_f32 v244, v104, v105\n"
"v_cvt_pk_bf16_f32 v245, v106, v107\n"
"v_cvt_pk_bf16_f32 v246, v108, v109\n"
"v_cvt_pk_bf16_f32 v247, v110, v111\n"
"ds_write_b128 v41, v[244:247] offset:11264\n"
"v_accvgpr_read_b32 v112, a176\n"
"v_accvgpr_read_b32 v113, a177\n"
"v_accvgpr_read_b32 v114, a178\n"
"v_accvgpr_read_b32 v115, a179\n"
"v_accvgpr_read_b32 v116, a180\n"
"v_accvgpr_read_b32 v117, a181\n"
"v_accvgpr_read_b32 v118, a182\n"
"v_accvgpr_read_b32 v119, a183\n"
"v_pk_mul_f32 v[112:113], v[112:113], v[26:27] op_sel_hi:[1,0]\n"
"v_pk_mul_f32 v[114:115], v[114:115], v[26:27] op_sel_hi:[1,0]\n"
"v_pk_mul_f32 v[116:117], v[116:117], v[26:27] op_sel_hi:[1,0]\n"
"v_pk_mul_f32 v[118:119], v[118:119], v[26:27] op_sel_hi:[1,0]\n"
"v_cvt_pk_bf16_f32 v248, v112, v113\n"
"v_cvt_pk_bf16_f32 v249, v114, v115\n"
"v_cvt_pk_bf16_f32 v250, v116, v117\n"
"v_cvt_pk_bf16_f32 v251, v118, v119\n"
"s_movk_i32 s65, 3072\n"
"buffer_store_dwordx4 v[208:211], v56, s[44:47], s65 offen sc1\n"
"ds_write_b128 v42, v[248:251] offset:5120\n"
"v_accvgpr_read_b32 v120, a184\n"
"v_accvgpr_read_b32 v121, a185\n"
"v_accvgpr_read_b32 v122, a186\n"
"v_accvgpr_read_b32 v123, a187\n"
"v_accvgpr_read_b32 v124, a188\n"
"v_accvgpr_read_b32 v125, a189\n"
"v_accvgpr_read_b32 v126, a190\n"
"v_accvgpr_read_b32 v127, a191\n"
"v_pk_mul_f32 v[120:121], v[120:121], v[26:27] op_sel_hi:[1,0]\n"
"v_pk_mul_f32 v[122:123], v[122:123], v[26:27] op_sel_hi:[1,0]\n"
"v_pk_mul_f32 v[124:125], v[124:125], v[26:27] op_sel_hi:[1,0]\n"
"v_pk_mul_f32 v[126:127], v[126:127], v[26:27] op_sel_hi:[1,0]\n"
"v_cvt_pk_bf16_f32 v252, v120, v121\n"
"v_cvt_pk_bf16_f32 v253, v122, v123\n"
"v_cvt_pk_bf16_f32 v254, v124, v125\n"
"v_cvt_pk_bf16_f32 v255, v126, v127\n"
"s_movk_i32 s65, 9216\n"
"buffer_store_dwordx4 v[212:215], v56, s[44:47], s65 offen sc1\n"
"ds_write_b128 v42, v[252:255] offset:11264\n"
"s_mul_i32 s65, s69, 12288\n"
"s_add_u32 s65, s65, 16384\n"
"v_add_u32 v16, s65, v56\n"
"v_add_u32 v17, 81920, v16\n"
"v_bfe_u32 v36, v57, 2, 2\n"
"v_lshlrev_b32 v36, 6, v36\n"
"v_and_b32 v37, 3, v57\n"
"v_lshl_add_u32 v36, v37, 3, v36\n"
"v_lshrrev_b32 v37, 5, v57\n"
"v_lshl_add_u32 v36, v37, 10, v36\n"
"v_bfe_u32 v37, v57, 4, 1\n"
"v_mul_u32_u24 v37, 288, v37\n"
"v_add_u32 v36, v36, v37\n"
"s_lshl_b32 s65, s70, 13\n"
"v_add_u32 v18, s65, v36\n"
"v_xor_b32 v19, 32, v18\n"
"v_add_u32 v20, 81920, v18\n"
"v_add_u32 v21, 81920, v19\n"
"s_waitcnt vmcnt(26) lgkmcnt(0)\n"
"s_barrier\n"
"ds_read_b64_tr_b16 v[0:1], v18 offset:0\n"
"ds_read_b64_tr_b16 v[2:3], v18 offset:2048\n"
"ds_read_b64_tr_b16 v[4:5], v18 offset:4096\n"
"ds_read_b64_tr_b16 v[6:7], v18 offset:6144\n"
"ds_read_b128 a[0:3], v16 offset:0\n"
"ds_read_b128 a[4:7], v16 offset:6144\n"
"ds_read_b128 a[8:11], v16 offset:1024\n"
"ds_read_b128 a[12:15], v16 offset:7168\n"
"ds_read_b128 a[16:19], v16 offset:2048\n"
"ds_read_b128 a[20:23], v16 offset:8192\n"
"ds_read_b128 a[24:27], v16 offset:3072\n"
"ds_read_b128 a[28:31], v16 offset:9216\n"
"ds_read_b128 a[32:35], v16 offset:4096\n"
"ds_read_b128 a[36:39], v16 offset:10240\n"
"ds_read_b128 a[40:43], v16 offset:5120\n"
"ds_read_b128 a[44:47], v16 offset:11264\n"
"s_mov_b32 s48, s18\n"
"s_and_b32 s49, s19, 0xffff\n"
"s_mov_b32 s50, 391488\n"
"s_mov_b32 s96, s34\n"
"s_and_b32 s97, s35, 0xffff\n"
"s_mov_b32 s98, 382592\n"
"s_mov_b32 s99, 0x20000\n"
"v_and_b32 v36, 15, v57\n"
"v_lshlrev_b32 v37, 11, v36\n"
"v_lshrrev_b32 v38, 4, v57\n"
"v_lshl_add_u32 v37, v38, 4, v37\n"
"s_mul_i32 s65, s69, 196608\n"
"s_lshl_b32 s87, s70, 8\n"
"s_add_u32 s65, s65, s87\n"
"v_add_u32 v22, s65, v37\n"
"v_mov_b32 v23, 0\n"
"v_not_b32 v40, s88\n"
"v_mov_b32 v42, s88\n"
"v_mov_b32 v43, s89\n"
"s_lshl_b32 s65, s22, 11\n"
"s_add_u32 s65, s65, 4096\n"
"v_lshl_add_u32 v24, v36, 7, s65\n"
"v_bfe_u32 v36, v57, 2, 2\n"
"v_cmp_eq_u32 vcc, v36, v38\n"
"v_and_b32 v36, 1, v57\n"
"v_lshlrev_b32 v36, 4, v36\n"
"v_mov_b32 v37, 0x3f80\n"
"v_lshlrev_b32 v37, v36, v37\n"
"v_cndmask_b32 v37, 0, v37, vcc\n"
"v_bfe_u32 v36, v57, 1, 1\n"
"v_cmp_eq_u32 vcc, 0, v36\n"
"s_nop 1\n"
"v_cndmask_b32 v28, 0, v37, vcc\n"
"v_cndmask_b32 v29, v37, v23, vcc\n"
"v_mov_b32 v30, 0\n"
"v_mov_b32 v31, 0\n"
"v_mov_b32 v32, 0\n"
"v_mov_b32 v33, 0\n"
"v_mov_b32 v34, v28\n"
"v_mov_b32 v35, v29\n"
"s_and_b32 s65, s23, 12\n"
"s_lshl_b32 s84, 15, s65\n"
"s_xor_b32 s65, s83, 3\n"
"s_lshl_b32 s85, s65, 2\n"
"s_add_u32 s85, s85, s83\n"
"s_mul_i32 s85, s85, 25088\n"
"s_lshl_b32 s58, s83, 2\n"
"s_add_u32 s58, s58, s65\n"
"s_mul_i32 s58, s58, 25088\n"
"s_xor_b32 s65, s83, 2\n"
"s_lshl_b32 s86, s65, 2\n"
"s_add_u32 s86, s86, s83\n"
"s_mul_i32 s86, s86, 25088\n"
"s_lshl_b32 s59, s83, 2\n"
"s_add_u32 s59, s59, s65\n"
"s_mul_i32 s59, s59, 25088\n"
"s_xor_b32 s65, s83, 1\n"
"s_lshl_b32 s95, s65, 2\n"
"s_add_u32 s95, s95, s83\n"
"s_mul_i32 s95, s95, 25088\n"
"s_lshl_b32 s60, s83, 2\n"
"s_add_u32 s60, s60, s65\n"
"s_mul_i32 s60, s60, 25088\n"
"v_mov_b32 v26, s58\n"
"v_writelane_b32 v26, s59, 1\n"
"v_writelane_b32 v26, s60, 2\n"
"v_add_u32 v26, 6144, v26\n"
"s_waitcnt lgkmcnt(0)\n"
"v_mfma_f32_16x16x32_bf16 v[64:67], v[0:3], a[0:3], 0\n"
"ds_read_b64_tr_b16 v[8:9], v19 offset:0\n"
"v_mfma_f32_16x16x32_bf16 v[64:67], v[4:7], a[4:7], v[64:67]\n"
"ds_read_b64_tr_b16 v[10:11], v19 offset:2048\n"
"v_mfma_f32_16x16x32_bf16 v[72:75], v[0:3], a[8:11], 0\n"
"ds_read_b64_tr_b16 v[12:13], v19 offset:4096\n"
"v_mfma_f32_16x16x32_bf16 v[72:75], v[4:7], a[12:15], v[72:75]\n"
"ds_read_b64_tr_b16 v[14:15], v19 offset:6144\n"
"v_mfma_f32_16x16x32_bf16 v[80:83], v[0:3], a[16:19], 0\n"
"v_mfma_f32_16x16x32_bf16 v[80:83], v[4:7], a[20:23], v[80:83]\n"
"v_mfma_f32_16x16x32_bf16 v[88:91], v[0:3], a[24:27], 0\n"
"v_mfma_f32_16x16x32_bf16 v[88:91], v[4:7], a[28:31], v[88:91]\n"
"s_movk_i32 s65, 15360\n"
"buffer_store_dwordx4 v[216:219], v56, s[44:47], s65 offen sc1\n"
"v_mfma_f32_16x16x32_bf16 v[96:99], v[0:3], a[32:35], 0\n"
"s_movk_i32 s65, 21504\n"
"buffer_store_dwordx4 v[220:223], v56, s[44:47], s65 offen sc1\n"
"v_mfma_f32_16x16x32_bf16 v[96:99], v[4:7], a[36:39], v[96:99]\n"
"s_movk_i32 s65, 4096\n"
"buffer_store_dwordx4 v[224:227], v56, s[44:47], s65 offen sc1\n"
"v_mfma_f32_16x16x32_bf16 v[104:107], v[0:3], a[40:43], 0\n"
"s_movk_i32 s65, 10240\n"
"buffer_store_dwordx4 v[228:231], v56, s[44:47], s65 offen sc1\n"
"v_mfma_f32_16x16x32_bf16 v[104:107], v[4:7], a[44:47], v[104:107]\n"
"s_waitcnt lgkmcnt(0)\n"
"v_mfma_f32_16x16x32_bf16 v[68:71], v[8:11], a[0:3], 0\n"
"ds_read_b64_tr_b16 v[0:1], v18 offset:512\n"
"v_mfma_f32_16x16x32_bf16 v[68:71], v[12:15], a[4:7], v[68:71]\n"
"ds_read_b64_tr_b16 v[2:3], v18 offset:2560\n"
"v_mfma_f32_16x16x32_bf16 v[76:79], v[8:11], a[8:11], 0\n"
"ds_read_b64_tr_b16 v[4:5], v18 offset:4608\n"
"v_mfma_f32_16x16x32_bf16 v[76:79], v[12:15], a[12:15], v[76:79]\n"
"ds_read_b64_tr_b16 v[6:7], v18 offset:6656\n"
"v_mfma_f32_16x16x32_bf16 v[84:87], v[8:11], a[16:19], 0\n"
"v_mfma_f32_16x16x32_bf16 v[84:87], v[12:15], a[20:23], v[84:87]\n"
"v_mfma_f32_16x16x32_bf16 v[92:95], v[8:11], a[24:27], 0\n"
"v_mfma_f32_16x16x32_bf16 v[92:95], v[12:15], a[28:31], v[92:95]\n"
"s_movk_i32 s65, 16384\n"
"buffer_store_dwordx4 v[232:235], v56, s[44:47], s65 offen sc1\n"
"v_mfma_f32_16x16x32_bf16 v[100:103], v[8:11], a[32:35], 0\n"
"v_mfma_f32_16x16x32_bf16 v[100:103], v[12:15], a[36:39], v[100:103]\n"
"s_movk_i32 s65, 22528\n"
"buffer_store_dwordx4 v[236:239], v56, s[44:47], s65 offen sc1\n"
"v_mfma_f32_16x16x32_bf16 v[108:111], v[8:11], a[40:43], 0\n"
"s_movk_i32 s65, 5120\n"
"buffer_store_dwordx4 v[240:243], v56, s[44:47], s65 offen sc1\n"
"v_mfma_f32_16x16x32_bf16 v[108:111], v[12:15], a[44:47], v[108:111]\n"
"s_waitcnt lgkmcnt(0)\n"
"v_mfma_f32_16x16x32_bf16 v[112:115], v[0:3], a[0:3], 0\n"
"ds_read_b64_tr_b16 v[8:9], v19 offset:512\n"
"v_mfma_f32_16x16x32_bf16 v[112:115], v[4:7], a[4:7], v[112:115]\n"
"ds_read_b64_tr_b16 v[10:11], v19 offset:2560\n"
"v_mfma_f32_16x16x32_bf16 v[120:123], v[0:3], a[8:11], 0\n"
"ds_read_b64_tr_b16 v[12:13], v19 offset:4608\n"
"v_mfma_f32_16x16x32_bf16 v[120:123], v[4:7], a[12:15], v[120:123]\n"
"ds_read_b64_tr_b16 v[14:15], v19 offset:6656\n"
"v_mfma_f32_16x16x32_bf16 v[128:131], v[0:3], a[16:19], 0\n"
"v_mfma_f32_16x16x32_bf16 v[128:131], v[4:7], a[20:23], v[128:131]\n"
"v_mfma_f32_16x16x32_bf16 v[136:139], v[0:3], a[24:27], 0\n"
"v_mfma_f32_16x16x32_bf16 v[136:139], v[4:7], a[28:31], v[136:139]\n"
"s_movk_i32 s65, 11264\n"
"buffer_store_dwordx4 v[244:247], v56, s[44:47], s65 offen sc1\n"
"v_mfma_f32_16x16x32_bf16 v[144:147], v[0:3], a[32:35], 0\n"
"s_movk_i32 s65, 17408\n"
"buffer_store_dwordx4 v[248:251], v56, s[44:47], s65 offen sc1\n"
"v_mfma_f32_16x16x32_bf16 v[144:147], v[4:7], a[36:39], v[144:147]\n"
"s_movk_i32 s65, 23552\n"
"buffer_store_dwordx4 v[252:255], v56, s[44:47], s65 offen sc1\n"
"v_mfma_f32_16x16x32_bf16 v[152:155], v[0:3], a[40:43], 0\n"
"v_mfma_f32_16x16x32_bf16 v[152:155], v[4:7], a[44:47], v[152:155]\n"
"s_waitcnt vmcnt(32) lgkmcnt(0)\n"
"s_barrier\n"
"v_mfma_f32_16x16x32_bf16 v[116:119], v[8:11], a[0:3], 0\n"
"ds_read_b64_tr_b16 v[0:1], v18 offset:40960\n"
"v_mfma_f32_16x16x32_bf16 v[116:119], v[12:15], a[4:7], v[116:119]\n"
"ds_read_b64_tr_b16 v[2:3], v18 offset:43008\n"
"v_mfma_f32_16x16x32_bf16 v[124:127], v[8:11], a[8:11], 0\n"
"s_add_u32 s66, s80, 2\n"
"ds_read_b64_tr_b16 v[4:5], v18 offset:45056\n"
"v_mfma_f32_16x16x32_bf16 v[124:127], v[12:15], a[12:15], v[124:127]\n"
"s_add_u32 s66, s66, s82\n"
"ds_read_b64_tr_b16 v[6:7], v18 offset:47104\n"
"v_mfma_f32_16x16x32_bf16 v[132:135], v[8:11], a[16:19], 0\n"
"s_lshl_b32 s66, s66, 17\n"
"ds_read_b128 a[48:51], v16 offset:40960\n"
"v_mfma_f32_16x16x32_bf16 v[132:135], v[12:15], a[20:23], v[132:135]\n"
"s_or_b32 s66, s66, s81\n"
"ds_read_b128 a[52:55], v16 offset:47104\n"
"v_mfma_f32_16x16x32_bf16 v[140:143], v[8:11], a[24:27], 0\n"
"s_add_u32 m0, s68, 0\n"
"ds_read_b128 a[56:59], v16 offset:41984\n"
"v_mfma_f32_16x16x32_bf16 v[140:143], v[12:15], a[28:31], v[140:143]\n"
"buffer_load_dwordx4 v44, s[40:43], s66 offen offset:0 lds\n"
"v_mfma_f32_16x16x32_bf16 v[148:151], v[8:11], a[32:35], 0\n"
"v_mfma_f32_16x16x32_bf16 v[148:151], v[12:15], a[36:39], v[148:151]\n"
"v_mfma_f32_16x16x32_bf16 v[156:159], v[8:11], a[40:43], 0\n"
"v_mfma_f32_16x16x32_bf16 v[156:159], v[12:15], a[44:47], v[156:159]\n"
"s_waitcnt lgkmcnt(3)\n"
"v_mfma_f32_16x16x32_bf16 v[160:163], v[0:3], a[0:3], 0\n"
"ds_read_b64_tr_b16 v[8:9], v19 offset:40960\n"
"v_mfma_f32_16x16x32_bf16 v[160:163], v[4:7], a[4:7], v[160:163]\n"
"ds_read_b64_tr_b16 v[10:11], v19 offset:43008\n"
"v_mfma_f32_16x16x32_bf16 v[168:171], v[0:3], a[8:11], 0\n"
"ds_read_b64_tr_b16 v[12:13], v19 offset:45056\n"
"v_mfma_f32_16x16x32_bf16 v[168:171], v[4:7], a[12:15], v[168:171]\n"
"ds_read_b64_tr_b16 v[14:15], v19 offset:47104\n"
"v_mfma_f32_16x16x32_bf16 v[176:179], v[0:3], a[16:19], 0\n"
"ds_read_b128 a[60:63], v16 offset:48128\n"
"v_mfma_f32_16x16x32_bf16 v[176:179], v[4:7], a[20:23], v[176:179]\n"
"ds_read_b128 a[64:67], v16 offset:43008\n"
"v_mfma_f32_16x16x32_bf16 v[184:187], v[0:3], a[24:27], 0\n"
"ds_read_b128 a[68:71], v16 offset:49152\n"
"v_mfma_f32_16x16x32_bf16 v[184:187], v[4:7], a[28:31], v[184:187]\n"
"buffer_load_dwordx4 v45, s[40:43], s66 offen offset:1024 lds\n"
"v_mfma_f32_16x16x32_bf16 v[192:195], v[0:3], a[32:35], 0\n"
"v_mfma_f32_16x16x32_bf16 v[192:195], v[4:7], a[36:39], v[192:195]\n"
"v_mfma_f32_16x16x32_bf16 v[200:203], v[0:3], a[40:43], 0\n"
"v_mfma_f32_16x16x32_bf16 v[200:203], v[4:7], a[44:47], v[200:203]\n"
"s_waitcnt lgkmcnt(3)\n"
"v_mfma_f32_16x16x32_bf16 v[164:167], v[8:11], a[0:3], 0\n"
"ds_read_b64_tr_b16 v[0:1], v18 offset:41472\n"
"v_mfma_f32_16x16x32_bf16 v[164:167], v[12:15], a[4:7], v[164:167]\n"
"ds_read_b64_tr_b16 v[2:3], v18 offset:43520\n"
"v_mfma_f32_16x16x32_bf16 v[172:175], v[8:11], a[8:11], 0\n"
"ds_read_b64_tr_b16 v[4:5], v18 offset:45568\n"
"v_mfma_f32_16x16x32_bf16 v[172:175], v[12:15], a[12:15], v[172:175]\n"
"ds_read_b64_tr_b16 v[6:7], v18 offset:47616\n"
"v_mfma_f32_16x16x32_bf16 v[180:183], v[8:11], a[16:19], 0\n"
"ds_read_b128 a[72:75], v16 offset:44032\n"
"v_mfma_f32_16x16x32_bf16 v[180:183], v[12:15], a[20:23], v[180:183]\n"
"ds_read_b128 a[76:79], v16 offset:50176\n"
"v_mfma_f32_16x16x32_bf16 v[188:191], v[8:11], a[24:27], 0\n"
"ds_read_b128 a[80:83], v16 offset:45056\n"
"v_mfma_f32_16x16x32_bf16 v[188:191], v[12:15], a[28:31], v[188:191]\n"
"buffer_load_dwordx4 v46, s[40:43], s66 offen offset:2048 lds\n"
"v_mfma_f32_16x16x32_bf16 v[196:199], v[8:11], a[32:35], 0\n"
"v_mfma_f32_16x16x32_bf16 v[196:199], v[12:15], a[36:39], v[196:199]\n"
"v_mfma_f32_16x16x32_bf16 v[204:207], v[8:11], a[40:43], 0\n"
"v_mfma_f32_16x16x32_bf16 v[204:207], v[12:15], a[44:47], v[204:207]\n"
"s_waitcnt lgkmcnt(3)\n"
"v_mfma_f32_16x16x32_bf16 v[208:211], v[0:3], a[0:3], 0\n"
"ds_read_b64_tr_b16 v[8:9], v19 offset:41472\n"
"v_mfma_f32_16x16x32_bf16 v[208:211], v[4:7], a[4:7], v[208:211]\n"
"ds_read_b64_tr_b16 v[10:11], v19 offset:43520\n"
"v_mfma_f32_16x16x32_bf16 v[216:219], v[0:3], a[8:11], 0\n"
"ds_read_b64_tr_b16 v[12:13], v19 offset:45568\n"
"v_mfma_f32_16x16x32_bf16 v[216:219], v[4:7], a[12:15], v[216:219]\n"
"ds_read_b64_tr_b16 v[14:15], v19 offset:47616\n"
"v_mfma_f32_16x16x32_bf16 v[224:227], v[0:3], a[16:19], 0\n"
"ds_read_b128 a[84:87], v16 offset:51200\n"
"v_mfma_f32_16x16x32_bf16 v[224:227], v[4:7], a[20:23], v[224:227]\n"
"ds_read_b128 a[88:91], v16 offset:46080\n"
"v_mfma_f32_16x16x32_bf16 v[232:235], v[0:3], a[24:27], 0\n"
"ds_read_b128 a[92:95], v16 offset:52224\n"
"v_mfma_f32_16x16x32_bf16 v[232:235], v[4:7], a[28:31], v[232:235]\n"
"buffer_load_dwordx4 v47, s[40:43], s66 offen offset:3072 lds\n"
"v_mfma_f32_16x16x32_bf16 v[240:243], v[0:3], a[32:35], 0\n"
"v_mfma_f32_16x16x32_bf16 v[240:243], v[4:7], a[36:39], v[240:243]\n"
"v_mfma_f32_16x16x32_bf16 v[248:251], v[0:3], a[40:43], 0\n"
"v_mfma_f32_16x16x32_bf16 v[248:251], v[4:7], a[44:47], v[248:251]\n"
"s_waitcnt vmcnt(32) lgkmcnt(0)\n"
"s_barrier\n"
"v_mfma_f32_16x16x32_bf16 v[212:215], v[8:11], a[0:3], 0\n"
"ds_read_b64_tr_b16 v[0:1], v20 offset:0\n"
"v_mfma_f32_16x16x32_bf16 v[212:215], v[12:15], a[4:7], v[212:215]\n"
"s_add_u32 s66, s80, 2\n"
"ds_read_b64_tr_b16 v[2:3], v20 offset:2048\n"
"v_mfma_f32_16x16x32_bf16 v[220:223], v[8:11], a[8:11], 0\n"
"s_add_u32 s66, s66, s82\n"
"ds_read_b64_tr_b16 v[4:5], v20 offset:4096\n"
"v_mfma_f32_16x16x32_bf16 v[220:223], v[12:15], a[12:15], v[220:223]\n"
"s_lshl_b32 s66, s66, 17\n"
"ds_read_b64_tr_b16 v[6:7], v20 offset:6144\n"
"v_mfma_f32_16x16x32_bf16 v[228:231], v[8:11], a[16:19], 0\n"
"s_or_b32 s66, s66, s81\n"
"v_mfma_f32_16x16x32_bf16 v[228:231], v[12:15], a[20:23], v[228:231]\n"
"s_xor_b32 s66, s66, 128\n"
"v_mfma_f32_16x16x32_bf16 v[236:239], v[8:11], a[24:27], 0\n"
"s_add_u32 m0, s68, 40960\n"
"v_mfma_f32_16x16x32_bf16 v[236:239], v[12:15], a[28:31], v[236:239]\n"
"buffer_load_dwordx4 v44, s[40:43], s66 offen offset:0 lds\n"
"v_mfma_f32_16x16x32_bf16 v[244:247], v[8:11], a[32:35], 0\n"
"v_mfma_f32_16x16x32_bf16 v[244:247], v[12:15], a[36:39], v[244:247]\n"
"v_mfma_f32_16x16x32_bf16 v[252:255], v[8:11], a[40:43], 0\n"
"v_mfma_f32_16x16x32_bf16 v[252:255], v[12:15], a[44:47], v[252:255]\n"
"s_waitcnt lgkmcnt(0)\n"
"v_mfma_f32_16x16x32_bf16 v[64:67], v[0:3], a[48:51], v[64:67]\n"
"ds_read_b64_tr_b16 v[8:9], v21 offset:0\n"
"v_mfma_f32_16x16x32_bf16 v[64:67], v[4:7], a[52:55], v[64:67]\n"
"ds_read_b64_tr_b16 v[10:11], v21 offset:2048\n"
"v_mfma_f32_16x16x32_bf16 v[72:75], v[0:3], a[56:59], v[72:75]\n"
"ds_read_b64_tr_b16 v[12:13], v21 offset:4096\n"
"v_mfma_f32_16x16x32_bf16 v[72:75], v[4:7], a[60:63], v[72:75]\n"
"ds_read_b64_tr_b16 v[14:15], v21 offset:6144\n"
"v_mfma_f32_16x16x32_bf16 v[80:83], v[0:3], a[64:67], v[80:83]\n"
"v_mfma_f32_16x16x32_bf16 v[80:83], v[4:7], a[68:71], v[80:83]\n"
"v_mfma_f32_16x16x32_bf16 v[88:91], v[0:3], a[72:75], v[88:91]\n"
"v_mfma_f32_16x16x32_bf16 v[88:91], v[4:7], a[76:79], v[88:91]\n"
"buffer_load_dwordx4 v45, s[40:43], s66 offen offset:1024 lds\n"
"v_mfma_f32_16x16x32_bf16 v[96:99], v[0:3], a[80:83], v[96:99]\n"
"v_mfma_f32_16x16x32_bf16 v[96:99], v[4:7], a[84:87], v[96:99]\n"
"v_mfma_f32_16x16x32_bf16 v[104:107], v[0:3], a[88:91], v[104:107]\n"
"v_mfma_f32_16x16x32_bf16 v[104:107], v[4:7], a[92:95], v[104:107]\n"
"s_waitcnt lgkmcnt(0)\n"
"v_mfma_f32_16x16x32_bf16 v[68:71], v[8:11], a[48:51], v[68:71]\n"
"ds_read_b64_tr_b16 v[0:1], v20 offset:512\n"
"v_mfma_f32_16x16x32_bf16 v[68:71], v[12:15], a[52:55], v[68:71]\n"
"ds_read_b64_tr_b16 v[2:3], v20 offset:2560\n"
"v_mfma_f32_16x16x32_bf16 v[76:79], v[8:11], a[56:59], v[76:79]\n"
"ds_read_b64_tr_b16 v[4:5], v20 offset:4608\n"
"v_mfma_f32_16x16x32_bf16 v[76:79], v[12:15], a[60:63], v[76:79]\n"
"ds_read_b64_tr_b16 v[6:7], v20 offset:6656\n"
"v_mfma_f32_16x16x32_bf16 v[84:87], v[8:11], a[64:67], v[84:87]\n"
"v_mfma_f32_16x16x32_bf16 v[84:87], v[12:15], a[68:71], v[84:87]\n"
"v_mfma_f32_16x16x32_bf16 v[92:95], v[8:11], a[72:75], v[92:95]\n"
"v_mfma_f32_16x16x32_bf16 v[92:95], v[12:15], a[76:79], v[92:95]\n"
"buffer_load_dwordx4 v46, s[40:43], s66 offen offset:2048 lds\n"
"v_mfma_f32_16x16x32_bf16 v[100:103], v[8:11], a[80:83], v[100:103]\n"
"v_mfma_f32_16x16x32_bf16 v[100:103], v[12:15], a[84:87], v[100:103]\n"
"v_mfma_f32_16x16x32_bf16 v[108:111], v[8:11], a[88:91], v[108:111]\n"
"v_mfma_f32_16x16x32_bf16 v[108:111], v[12:15], a[92:95], v[108:111]\n"
"s_waitcnt lgkmcnt(0)\n"
"v_mfma_f32_16x16x32_bf16 v[112:115], v[0:3], a[48:51], v[112:115]\n"
"ds_read_b64_tr_b16 v[8:9], v21 offset:512\n"
"v_mfma_f32_16x16x32_bf16 v[112:115], v[4:7], a[52:55], v[112:115]\n"
"ds_read_b64_tr_b16 v[10:11], v21 offset:2560\n"
"v_mfma_f32_16x16x32_bf16 v[120:123], v[0:3], a[56:59], v[120:123]\n"
"ds_read_b64_tr_b16 v[12:13], v21 offset:4608\n"
"v_mfma_f32_16x16x32_bf16 v[120:123], v[4:7], a[60:63], v[120:123]\n"
"ds_read_b64_tr_b16 v[14:15], v21 offset:6656\n"
"v_mfma_f32_16x16x32_bf16 v[128:131], v[0:3], a[64:67], v[128:131]\n"
"v_mfma_f32_16x16x32_bf16 v[128:131], v[4:7], a[68:71], v[128:131]\n"
"v_mfma_f32_16x16x32_bf16 v[136:139], v[0:3], a[72:75], v[136:139]\n"
"v_mfma_f32_16x16x32_bf16 v[136:139], v[4:7], a[76:79], v[136:139]\n"
"buffer_load_dwordx4 v47, s[40:43], s66 offen offset:3072 lds\n"
"v_mfma_f32_16x16x32_bf16 v[144:147], v[0:3], a[80:83], v[144:147]\n"
"v_mfma_f32_16x16x32_bf16 v[144:147], v[4:7], a[84:87], v[144:147]\n"
"v_mfma_f32_16x16x32_bf16 v[152:155], v[0:3], a[88:91], v[152:155]\n"
"v_mfma_f32_16x16x32_bf16 v[152:155], v[4:7], a[92:95], v[152:155]\n"
"s_waitcnt vmcnt(8)\n"
"s_waitcnt vmcnt(32) lgkmcnt(0)\n"
"s_barrier\n"
"s_cmp_lg_u32 s52, 0\n"
"s_cbranch_scc1 P2_NP\n"
"s_lshl_b32 s65, s22, 4\n"
"s_add_u32 s65, s65, s23\n"
"s_lshl_b32 s65, s65, 7\n"
"s_add_u32 s65, s65, 4096\n"
"v_mov_b32 v36, s65\n"
"global_store_dwordx2 v36, v[42:43], s[20:21] sc1\n"
"P2_NP:\n"
"v_mfma_f32_16x16x32_bf16 v[116:119], v[8:11], a[48:51], v[116:119]\n"
"ds_read_b64_tr_b16 v[0:1], v20 offset:40960\n"
"v_mfma_f32_16x16x32_bf16 v[116:119], v[12:15], a[52:55], v[116:119]\n"
"ds_read_b64_tr_b16 v[2:3], v20 offset:43008\n"
"v_mfma_f32_16x16x32_bf16 v[124:127], v[8:11], a[56:59], v[124:127]\n"
"s_add_u32 s66, s80, 3\n"
"ds_read_b64_tr_b16 v[4:5], v20 offset:45056\n"
"v_mfma_f32_16x16x32_bf16 v[124:127], v[12:15], a[60:63], v[124:127]\n"
"s_add_u32 s66, s66, s82\n"
"ds_read_b64_tr_b16 v[6:7], v20 offset:47104\n"
"v_mfma_f32_16x16x32_bf16 v[132:135], v[8:11], a[64:67], v[132:135]\n"
"s_lshl_b32 s66, s66, 17\n"
"ds_read_b128 a[96:99], v17 offset:0\n"
"v_mfma_f32_16x16x32_bf16 v[132:135], v[12:15], a[68:71], v[132:135]\n"
"s_or_b32 s66, s66, s81\n"
"ds_read_b128 a[100:103], v17 offset:6144\n"
"v_mfma_f32_16x16x32_bf16 v[140:143], v[8:11], a[72:75], v[140:143]\n"
"s_add_u32 m0, s68, 81920\n"
"ds_read_b128 a[104:107], v17 offset:1024\n"
"v_mfma_f32_16x16x32_bf16 v[140:143], v[12:15], a[76:79], v[140:143]\n"
"buffer_load_dwordx4 v44, s[40:43], s66 offen offset:0 lds\n"
"v_mfma_f32_16x16x32_bf16 v[148:151], v[8:11], a[80:83], v[148:151]\n"
"v_mfma_f32_16x16x32_bf16 v[148:151], v[12:15], a[84:87], v[148:151]\n"
"v_mfma_f32_16x16x32_bf16 v[156:159], v[8:11], a[88:91], v[156:159]\n"
"v_mfma_f32_16x16x32_bf16 v[156:159], v[12:15], a[92:95], v[156:159]\n"
"s_waitcnt lgkmcnt(3)\n"
"v_mfma_f32_16x16x32_bf16 v[160:163], v[0:3], a[48:51], v[160:163]\n"
"ds_read_b64_tr_b16 v[8:9], v21 offset:40960\n"
"v_mfma_f32_16x16x32_bf16 v[160:163], v[4:7], a[52:55], v[160:163]\n"
"ds_read_b64_tr_b16 v[10:11], v21 offset:43008\n"
"v_mfma_f32_16x16x32_bf16 v[168:171], v[0:3], a[56:59], v[168:171]\n"
"ds_read_b64_tr_b16 v[12:13], v21 offset:45056\n"
"v_mfma_f32_16x16x32_bf16 v[168:171], v[4:7], a[60:63], v[168:171]\n"
"ds_read_b64_tr_b16 v[14:15], v21 offset:47104\n"
"v_mfma_f32_16x16x32_bf16 v[176:179], v[0:3], a[64:67], v[176:179]\n"
"ds_read_b128 a[108:111], v17 offset:7168\n"
"v_mfma_f32_16x16x32_bf16 v[176:179], v[4:7], a[68:71], v[176:179]\n"
"ds_read_b128 a[112:115], v17 offset:2048\n"
"v_mfma_f32_16x16x32_bf16 v[184:187], v[0:3], a[72:75], v[184:187]\n"
"ds_read_b128 a[116:119], v17 offset:8192\n"
"v_mfma_f32_16x16x32_bf16 v[184:187], v[4:7], a[76:79], v[184:187]\n"
"buffer_load_dwordx4 v45, s[40:43], s66 offen offset:1024 lds\n"
"v_mfma_f32_16x16x32_bf16 v[192:195], v[0:3], a[80:83], v[192:195]\n"
"v_mfma_f32_16x16x32_bf16 v[192:195], v[4:7], a[84:87], v[192:195]\n"
"v_mfma_f32_16x16x32_bf16 v[200:203], v[0:3], a[88:91], v[200:203]\n"
"v_mfma_f32_16x16x32_bf16 v[200:203], v[4:7], a[92:95], v[200:203]\n"
"s_waitcnt lgkmcnt(3)\n"
"v_mfma_f32_16x16x32_bf16 v[164:167], v[8:11], a[48:51], v[164:167]\n"
"ds_read_b64_tr_b16 v[0:1], v20 offset:41472\n"
"v_mfma_f32_16x16x32_bf16 v[164:167], v[12:15], a[52:55], v[164:167]\n"
"ds_read_b64_tr_b16 v[2:3], v20 offset:43520\n"
"v_mfma_f32_16x16x32_bf16 v[172:175], v[8:11], a[56:59], v[172:175]\n"
"ds_read_b64_tr_b16 v[4:5], v20 offset:45568\n"
"v_mfma_f32_16x16x32_bf16 v[172:175], v[12:15], a[60:63], v[172:175]\n"
"ds_read_b64_tr_b16 v[6:7], v20 offset:47616\n"
"v_mfma_f32_16x16x32_bf16 v[180:183], v[8:11], a[64:67], v[180:183]\n"
"ds_read_b128 a[120:123], v17 offset:3072\n"
"v_mfma_f32_16x16x32_bf16 v[180:183], v[12:15], a[68:71], v[180:183]\n"
"ds_read_b128 a[124:127], v17 offset:9216\n"
"v_mfma_f32_16x16x32_bf16 v[188:191], v[8:11], a[72:75], v[188:191]\n"
"ds_read_b128 a[128:131], v17 offset:4096\n"
"v_mfma_f32_16x16x32_bf16 v[188:191], v[12:15], a[76:79], v[188:191]\n"
"buffer_load_dwordx4 v46, s[40:43], s66 offen offset:2048 lds\n"
"v_mfma_f32_16x16x32_bf16 v[196:199], v[8:11], a[80:83], v[196:199]\n"
"v_mfma_f32_16x16x32_bf16 v[196:199], v[12:15], a[84:87], v[196:199]\n"
"v_mfma_f32_16x16x32_bf16 v[204:207], v[8:11], a[88:91], v[204:207]\n"
"v_mfma_f32_16x16x32_bf16 v[204:207], v[12:15], a[92:95], v[204:207]\n"
"s_waitcnt lgkmcnt(3)\n"
"v_mfma_f32_16x16x32_bf16 v[208:211], v[0:3], a[48:51], v[208:211]\n"
"ds_read_b64_tr_b16 v[8:9], v21 offset:41472\n"
"v_mfma_f32_16x16x32_bf16 v[208:211], v[4:7], a[52:55], v[208:211]\n"
"ds_read_b64_tr_b16 v[10:11], v21 offset:43520\n"
"v_mfma_f32_16x16x32_bf16 v[216:219], v[0:3], a[56:59], v[216:219]\n"
"ds_read_b64_tr_b16 v[12:13], v21 offset:45568\n"
"v_mfma_f32_16x16x32_bf16 v[216:219], v[4:7], a[60:63], v[216:219]\n"
"ds_read_b64_tr_b16 v[14:15], v21 offset:47616\n"
"v_mfma_f32_16x16x32_bf16 v[224:227], v[0:3], a[64:67], v[224:227]\n"
"ds_read_b128 a[132:135], v17 offset:10240\n"
"v_mfma_f32_16x16x32_bf16 v[224:227], v[4:7], a[68:71], v[224:227]\n"
"ds_read_b128 a[136:139], v17 offset:5120\n"
"v_mfma_f32_16x16x32_bf16 v[232:235], v[0:3], a[72:75], v[232:235]\n"
"ds_read_b128 a[140:143], v17 offset:11264\n"
"v_mfma_f32_16x16x32_bf16 v[232:235], v[4:7], a[76:79], v[232:235]\n"
"buffer_load_dwordx4 v47, s[40:43], s66 offen offset:3072 lds\n"
"v_mfma_f32_16x16x32_bf16 v[240:243], v[0:3], a[80:83], v[240:243]\n"
"v_mfma_f32_16x16x32_bf16 v[240:243], v[4:7], a[84:87], v[240:243]\n"
"v_mfma_f32_16x16x32_bf16 v[248:251], v[0:3], a[88:91], v[248:251]\n"
"v_mfma_f32_16x16x32_bf16 v[248:251], v[4:7], a[92:95], v[248:251]\n"
"s_waitcnt vmcnt(8) lgkmcnt(0)\n"
"s_barrier\n"
"v_mfma_f32_16x16x32_bf16 v[212:215], v[8:11], a[48:51], v[212:215]\n"
"ds_read_b64_tr_b16 v[0:1], v18 offset:0\n"
"v_mfma_f32_16x16x32_bf16 v[212:215], v[12:15], a[52:55], v[212:215]\n"
"s_add_u32 s66, s80, 3\n"
"ds_read_b64_tr_b16 v[2:3], v18 offset:2048\n"
"v_mfma_f32_16x16x32_bf16 v[220:223], v[8:11], a[56:59], v[220:223]\n"
"s_add_u32 s66, s66, s82\n"
"ds_read_b64_tr_b16 v[4:5], v18 offset:4096\n"
"v_mfma_f32_16x16x32_bf16 v[220:223], v[12:15], a[60:63], v[220:223]\n"
"s_lshl_b32 s66, s66, 17\n"
"ds_read_b64_tr_b16 v[6:7], v18 offset:6144\n"
"v_mfma_f32_16x16x32_bf16 v[228:231], v[8:11], a[64:67], v[228:231]\n"
"s_or_b32 s66, s66, s81\n"
"v_mfma_f32_16x16x32_bf16 v[228:231], v[12:15], a[68:71], v[228:231]\n"
"s_xor_b32 s66, s66, 128\n"
"v_mfma_f32_16x16x32_bf16 v[236:239], v[8:11], a[72:75], v[236:239]\n"
"s_add_u32 m0, s68, 122880\n"
"v_mfma_f32_16x16x32_bf16 v[236:239], v[12:15], a[76:79], v[236:239]\n"
"buffer_load_dwordx4 v44, s[40:43], s66 offen offset:0 lds\n"
"v_mfma_f32_16x16x32_bf16 v[244:247], v[8:11], a[80:83], v[244:247]\n"
"v_mfma_f32_16x16x32_bf16 v[244:247], v[12:15], a[84:87], v[244:247]\n"
"v_mfma_f32_16x16x32_bf16 v[252:255], v[8:11], a[88:91], v[252:255]\n"
"v_mfma_f32_16x16x32_bf16 v[252:255], v[12:15], a[92:95], v[252:255]\n"
"s_waitcnt lgkmcnt(0)\n"
"v_mfma_f32_16x16x32_bf16 v[64:67], v[0:3], a[96:99], v[64:67]\n"
"ds_read_b64_tr_b16 v[8:9], v19 offset:0\n"
"v_mfma_f32_16x16x32_bf16 v[64:67], v[4:7], a[100:103], v[64:67]\n"
"ds_read_b64_tr_b16 v[10:11], v19 offset:2048\n"
"v_mfma_f32_16x16x32_bf16 v[72:75], v[0:3], a[104:107], v[72:75]\n"
"ds_read_b64_tr_b16 v[12:13], v19 offset:4096\n"
"v_mfma_f32_16x16x32_bf16 v[72:75], v[4:7], a[108:111], v[72:75]\n"
"ds_read_b64_tr_b16 v[14:15], v19 offset:6144\n"
"v_mfma_f32_16x16x32_bf16 v[80:83], v[0:3], a[112:115], v[80:83]\n"
"v_mfma_f32_16x16x32_bf16 v[80:83], v[4:7], a[116:119], v[80:83]\n"
"v_mfma_f32_16x16x32_bf16 v[88:91], v[0:3], a[120:123], v[88:91]\n"
"v_mfma_f32_16x16x32_bf16 v[88:91], v[4:7], a[124:127], v[88:91]\n"
"buffer_load_dwordx4 v45, s[40:43], s66 offen offset:1024 lds\n"
"v_mfma_f32_16x16x32_bf16 v[96:99], v[0:3], a[128:131], v[96:99]\n"
"v_mfma_f32_16x16x32_bf16 v[96:99], v[4:7], a[132:135], v[96:99]\n"
"v_mfma_f32_16x16x32_bf16 v[104:107], v[0:3], a[136:139], v[104:107]\n"
"v_mfma_f32_16x16x32_bf16 v[104:107], v[4:7], a[140:143], v[104:107]\n"
"s_waitcnt lgkmcnt(0)\n"
"v_mfma_f32_16x16x32_bf16 v[68:71], v[8:11], a[96:99], v[68:71]\n"
"ds_read_b64_tr_b16 v[0:1], v18 offset:512\n"
"v_mfma_f32_16x16x32_bf16 v[68:71], v[12:15], a[100:103], v[68:71]\n"
"ds_read_b64_tr_b16 v[2:3], v18 offset:2560\n"
"v_mfma_f32_16x16x32_bf16 v[76:79], v[8:11], a[104:107], v[76:79]\n"
"ds_read_b64_tr_b16 v[4:5], v18 offset:4608\n"
"v_mfma_f32_16x16x32_bf16 v[76:79], v[12:15], a[108:111], v[76:79]\n"
"ds_read_b64_tr_b16 v[6:7], v18 offset:6656\n"
"v_mfma_f32_16x16x32_bf16 v[84:87], v[8:11], a[112:115], v[84:87]\n"
"v_mfma_f32_16x16x32_bf16 v[84:87], v[12:15], a[116:119], v[84:87]\n"
"v_mfma_f32_16x16x32_bf16 v[92:95], v[8:11], a[120:123], v[92:95]\n"
"v_mfma_f32_16x16x32_bf16 v[92:95], v[12:15], a[124:127], v[92:95]\n"
"buffer_load_dwordx4 v46, s[40:43], s66 offen offset:2048 lds\n"
"v_mfma_f32_16x16x32_bf16 v[100:103], v[8:11], a[128:131], v[100:103]\n"
"v_mfma_f32_16x16x32_bf16 v[100:103], v[12:15], a[132:135], v[100:103]\n"
"v_mfma_f32_16x16x32_bf16 v[108:111], v[8:11], a[136:139], v[108:111]\n"
"v_mfma_f32_16x16x32_bf16 v[108:111], v[12:15], a[140:143], v[108:111]\n"
"s_waitcnt lgkmcnt(0)\n"
"v_mfma_f32_16x16x32_bf16 v[112:115], v[0:3], a[96:99], v[112:115]\n"
"ds_read_b64_tr_b16 v[8:9], v19 offset:512\n"
"v_mfma_f32_16x16x32_bf16 v[112:115], v[4:7], a[100:103], v[112:115]\n"
"ds_read_b64_tr_b16 v[10:11], v19 offset:2560\n"
"v_mfma_f32_16x16x32_bf16 v[120:123], v[0:3], a[104:107], v[120:123]\n"
"ds_read_b64_tr_b16 v[12:13], v19 offset:4608\n"
"v_mfma_f32_16x16x32_bf16 v[120:123], v[4:7], a[108:111], v[120:123]\n"
"ds_read_b64_tr_b16 v[14:15], v19 offset:6656\n"
"v_mfma_f32_16x16x32_bf16 v[128:131], v[0:3], a[112:115], v[128:131]\n"
"v_mfma_f32_16x16x32_bf16 v[128:131], v[4:7], a[116:119], v[128:131]\n"
"v_mfma_f32_16x16x32_bf16 v[136:139], v[0:3], a[120:123], v[136:139]\n"
"v_mfma_f32_16x16x32_bf16 v[136:139], v[4:7], a[124:127], v[136:139]\n"
"buffer_load_dwordx4 v47, s[40:43], s66 offen offset:3072 lds\n"
"v_mfma_f32_16x16x32_bf16 v[144:147], v[0:3], a[128:131], v[144:147]\n"
"v_mfma_f32_16x16x32_bf16 v[144:147], v[4:7], a[132:135], v[144:147]\n"
"v_mfma_f32_16x16x32_bf16 v[152:155], v[0:3], a[136:139], v[152:155]\n"
"v_mfma_f32_16x16x32_bf16 v[152:155], v[4:7], a[140:143], v[152:155]\n"
"s_waitcnt vmcnt(8) lgkmcnt(0)\n"
"s_barrier\n"
"v_mfma_f32_16x16x32_bf16 v[116:119], v[8:11], a[96:99], v[116:119]\n"
"ds_read_b64_tr_b16 v[0:1], v18 offset:40960\n"
"v_mfma_f32_16x16x32_bf16 v[116:119], v[12:15], a[100:103], v[116:119]\n"
"s_cmp_le_u32 s80, 0\n"
"ds_read_b64_tr_b16 v[2:3], v18 offset:43008\n"
"v_mfma_f32_16x16x32_bf16 v[124:127], v[8:11], a[104:107], v[124:127]\n"
"s_cselect_b32 s66, 4, 0\n"
"ds_read_b64_tr_b16 v[4:5], v18 offset:45056\n"
"v_mfma_f32_16x16x32_bf16 v[124:127], v[12:15], a[108:111], v[124:127]\n"
"s_add_u32 s66, s66, s82\n"
"ds_read_b64_tr_b16 v[6:7], v18 offset:47104\n"
"v_mfma_f32_16x16x32_bf16 v[132:135], v[8:11], a[112:115], v[132:135]\n"
"s_lshl_b32 s66, s66, 17\n"
"ds_read_b128 a[144:147], v17 offset:40960\n"
"v_mfma_f32_16x16x32_bf16 v[132:135], v[12:15], a[116:119], v[132:135]\n"
"s_or_b32 s66, s66, s81\n"
"ds_read_b128 a[148:151], v17 offset:47104\n"
"v_mfma_f32_16x16x32_bf16 v[140:143], v[8:11], a[120:123], v[140:143]\n"
"s_add_u32 m0, s68, 0\n"
"ds_read_b128 a[152:155], v17 offset:41984\n"
"v_mfma_f32_16x16x32_bf16 v[140:143], v[12:15], a[124:127], v[140:143]\n"
"buffer_load_dwordx4 v44, s[40:43], s66 offen offset:0 lds\n"
"v_mfma_f32_16x16x32_bf16 v[148:151], v[8:11], a[128:131], v[148:151]\n"
"v_mfma_f32_16x16x32_bf16 v[148:151], v[12:15], a[132:135], v[148:151]\n"
"buffer_load_dwordx4 v45, s[40:43], s66 offen offset:1024 lds\n"
"v_mfma_f32_16x16x32_bf16 v[156:159], v[8:11], a[136:139], v[156:159]\n"
"v_mfma_f32_16x16x32_bf16 v[156:159], v[12:15], a[140:143], v[156:159]\n"
"s_waitcnt lgkmcnt(3)\n"
"v_mfma_f32_16x16x32_bf16 v[160:163], v[0:3], a[96:99], v[160:163]\n"
"ds_read_b64_tr_b16 v[8:9], v19 offset:40960\n"
"v_mfma_f32_16x16x32_bf16 v[160:163], v[4:7], a[100:103], v[160:163]\n"
"ds_read_b64_tr_b16 v[10:11], v19 offset:43008\n"
"v_mfma_f32_16x16x32_bf16 v[168:171], v[0:3], a[104:107], v[168:171]\n"
"ds_read_b64_tr_b16 v[12:13], v19 offset:45056\n"
"v_mfma_f32_16x16x32_bf16 v[168:171], v[4:7], a[108:111], v[168:171]\n"
"ds_read_b64_tr_b16 v[14:15], v19 offset:47104\n"
"v_mfma_f32_16x16x32_bf16 v[176:179], v[0:3], a[112:115], v[176:179]\n"
"ds_read_b128 a[156:159], v17 offset:48128\n"
"v_mfma_f32_16x16x32_bf16 v[176:179], v[4:7], a[116:119], v[176:179]\n"
"ds_read_b128 a[160:163], v17 offset:43008\n"
"v_mfma_f32_16x16x32_bf16 v[184:187], v[0:3], a[120:123], v[184:187]\n"
"ds_read_b128 a[164:167], v17 offset:49152\n"
"v_mfma_f32_16x16x32_bf16 v[184:187], v[4:7], a[124:127], v[184:187]\n"
"v_mfma_f32_16x16x32_bf16 v[192:195], v[0:3], a[128:131], v[192:195]\n"
"global_load_dwordx2 v[40:41], v24, s[20:21] sc1\n"
"v_mfma_f32_16x16x32_bf16 v[192:195], v[4:7], a[132:135], v[192:195]\n"
"v_mfma_f32_16x16x32_bf16 v[200:203], v[0:3], a[136:139], v[200:203]\n"
"v_mfma_f32_16x16x32_bf16 v[200:203], v[4:7], a[140:143], v[200:203]\n"
"s_waitcnt lgkmcnt(3)\n"
"v_mfma_f32_16x16x32_bf16 v[164:167], v[8:11], a[96:99], v[164:167]\n"
"ds_read_b64_tr_b16 v[0:1], v18 offset:41472\n"
"v_mfma_f32_16x16x32_bf16 v[164:167], v[12:15], a[100:103], v[164:167]\n"
"ds_read_b64_tr_b16 v[2:3], v18 offset:43520\n"
"v_mfma_f32_16x16x32_bf16 v[172:175], v[8:11], a[104:107], v[172:175]\n"
"ds_read_b64_tr_b16 v[4:5], v18 offset:45568\n"
"v_mfma_f32_16x16x32_bf16 v[172:175], v[12:15], a[108:111], v[172:175]\n"
"ds_read_b64_tr_b16 v[6:7], v18 offset:47616\n"
"v_mfma_f32_16x16x32_bf16 v[180:183], v[8:11], a[112:115], v[180:183]\n"
"ds_read_b128 a[168:171], v17 offset:44032\n"
"v_mfma_f32_16x16x32_bf16 v[180:183], v[12:15], a[116:119], v[180:183]\n"
"ds_read_b128 a[172:175], v17 offset:50176\n"
"v_mfma_f32_16x16x32_bf16 v[188:191], v[8:11], a[120:123], v[188:191]\n"
"ds_read_b128 a[176:179], v17 offset:45056\n"
"v_mfma_f32_16x16x32_bf16 v[188:191], v[12:15], a[124:127], v[188:191]\n"
"buffer_load_dwordx4 v46, s[40:43], s66 offen offset:2048 lds\n"
"v_mfma_f32_16x16x32_bf16 v[196:199], v[8:11], a[128:131], v[196:199]\n"
"v_mfma_f32_16x16x32_bf16 v[196:199], v[12:15], a[132:135], v[196:199]\n"
"buffer_load_dwordx4 v47, s[40:43], s66 offen offset:3072 lds\n"
"v_mfma_f32_16x16x32_bf16 v[204:207], v[8:11], a[136:139], v[204:207]\n"
"v_mfma_f32_16x16x32_bf16 v[204:207], v[12:15], a[140:143], v[204:207]\n"
"s_waitcnt lgkmcnt(3)\n"
"v_mfma_f32_16x16x32_bf16 v[208:211], v[0:3], a[96:99], v[208:211]\n"
"ds_read_b64_tr_b16 v[8:9], v19 offset:41472\n"
"v_mfma_f32_16x16x32_bf16 v[208:211], v[4:7], a[100:103], v[208:211]\n"
"ds_read_b64_tr_b16 v[10:11], v19 offset:43520\n"
"v_mfma_f32_16x16x32_bf16 v[216:219], v[0:3], a[104:107], v[216:219]\n"
"ds_read_b64_tr_b16 v[12:13], v19 offset:45568\n"
"v_mfma_f32_16x16x32_bf16 v[216:219], v[4:7], a[108:111], v[216:219]\n"
"ds_read_b64_tr_b16 v[14:15], v19 offset:47616\n"
"v_mfma_f32_16x16x32_bf16 v[224:227], v[0:3], a[112:115], v[224:227]\n"
"ds_read_b128 a[180:183], v17 offset:51200\n"
"v_mfma_f32_16x16x32_bf16 v[224:227], v[4:7], a[116:119], v[224:227]\n"
"ds_read_b128 a[184:187], v17 offset:46080\n"
"v_mfma_f32_16x16x32_bf16 v[232:235], v[0:3], a[120:123], v[232:235]\n"
"ds_read_b128 a[188:191], v17 offset:52224\n"
"v_mfma_f32_16x16x32_bf16 v[232:235], v[4:7], a[124:127], v[232:235]\n"
"v_mfma_f32_16x16x32_bf16 v[240:243], v[0:3], a[128:131], v[240:243]\n"
"global_load_dwordx2 v[40:41], v24, s[20:21] sc1\n"
"v_mfma_f32_16x16x32_bf16 v[240:243], v[4:7], a[132:135], v[240:243]\n"
"v_mfma_f32_16x16x32_bf16 v[248:251], v[0:3], a[136:139], v[248:251]\n"
"v_mfma_f32_16x16x32_bf16 v[248:251], v[4:7], a[140:143], v[248:251]\n"
"s_mov_b32 s87, 0x100000\n"
"P2_SA:\n"
"v_cmp_eq_u64 vcc, s[88:89], v[40:41]\n"
"s_sub_u32 s87, s87, 1\n"
"s_cbranch_scc1 P2_SAX\n"
"s_and_b32 s65, vcc_lo, s84\n"
"s_cmp_eq_u32 s65, s84\n"
"s_cbranch_scc1 P2_SAX\n"
"s_waitcnt vmcnt(36)\n"
"global_load_dwordx2 v[40:41], v24, s[20:21] sc1\n"
"s_sleep 4\n"
"s_branch P2_SA\n"
"P2_SAX:\n"
"s_waitcnt vmcnt(10) lgkmcnt(0)\n"
"s_barrier\n"
"v_mfma_f32_16x16x32_bf16 v[212:215], v[8:11], a[96:99], v[212:215]\n"
"ds_read_b64_tr_b16 v[0:1], v20 offset:0\n"
"v_mfma_f32_16x16x32_bf16 v[212:215], v[12:15], a[100:103], v[212:215]\n"
"ds_read_b64_tr_b16 v[2:3], v20 offset:2048\n"
"v_mfma_f32_16x16x32_bf16 v[220:223], v[8:11], a[104:107], v[220:223]\n"
"s_cmp_le_u32 s80, 0\n"
"ds_read_b64_tr_b16 v[4:5], v20 offset:4096\n"
"v_mfma_f32_16x16x32_bf16 v[220:223], v[12:15], a[108:111], v[220:223]\n"
"s_cselect_b32 s56, 4, 0\n"
"ds_read_b64_tr_b16 v[6:7], v20 offset:6144\n"
"v_mfma_f32_16x16x32_bf16 v[228:231], v[8:11], a[112:115], v[228:231]\n"
"s_add_u32 s56, s56, s82\n"
"v_mfma_f32_16x16x32_bf16 v[228:231], v[12:15], a[116:119], v[228:231]\n"
"s_mul_i32 s56, s56, 12288\n"
"v_mfma_f32_16x16x32_bf16 v[236:239], v[8:11], a[120:123], v[236:239]\n"
"s_add_u32 m0, s67, 0\n"
"v_mfma_f32_16x16x32_bf16 v[236:239], v[12:15], a[124:127], v[236:239]\n"
"buffer_load_dwordx4 v56, s[36:39], s56 offen offset:0 sc1 lds\n"
"v_mfma_f32_16x16x32_bf16 v[244:247], v[8:11], a[128:131], v[244:247]\n"
"buffer_load_dwordx4 v56, s[36:39], s56 offen offset:1024 sc1 lds\n"
"v_mfma_f32_16x16x32_bf16 v[244:247], v[12:15], a[132:135], v[244:247]\n"
"v_mfma_f32_16x16x32_bf16 v[252:255], v[8:11], a[136:139], v[252:255]\n"
"buffer_load_dwordx4 v56, s[36:39], s56 offen offset:2048 sc1 lds\n"
"v_mfma_f32_16x16x32_bf16 v[252:255], v[12:15], a[140:143], v[252:255]\n"
"s_waitcnt lgkmcnt(0)\n"
"v_mfma_f32_16x16x32_bf16 v[64:67], v[0:3], a[144:147], v[64:67]\n"
"ds_read_b64_tr_b16 v[8:9], v21 offset:0\n"
"v_mfma_f32_16x16x32_bf16 v[64:67], v[4:7], a[148:151], v[64:67]\n"
"s_cmp_le_u32 s80, 0\n"
"ds_read_b64_tr_b16 v[10:11], v21 offset:2048\n"
"v_mfma_f32_16x16x32_bf16 v[72:75], v[0:3], a[152:155], v[72:75]\n"
"s_cselect_b32 s56, 4, 0\n"
"ds_read_b64_tr_b16 v[12:13], v21 offset:4096\n"
"v_mfma_f32_16x16x32_bf16 v[72:75], v[4:7], a[156:159], v[72:75]\n"
"s_add_u32 s56, s56, s82\n"
"ds_read_b64_tr_b16 v[14:15], v21 offset:6144\n"
"v_mfma_f32_16x16x32_bf16 v[80:83], v[0:3], a[160:163], v[80:83]\n"
"s_mul_i32 s56, s56, 12288\n"
"v_mfma_f32_16x16x32_bf16 v[80:83], v[4:7], a[164:167], v[80:83]\n"
"s_add_u32 s56, s56, 3072\n"
"v_mfma_f32_16x16x32_bf16 v[88:91], v[0:3], a[168:171], v[88:91]\n"
"s_add_u32 m0, s67, 3072\n"
"v_mfma_f32_16x16x32_bf16 v[88:91], v[4:7], a[172:175], v[88:91]\n"
"buffer_load_dwordx4 v56, s[36:39], s56 offen offset:0 sc1 lds\n"
"v_mfma_f32_16x16x32_bf16 v[96:99], v[0:3], a[176:179], v[96:99]\n"
"v_mfma_f32_16x16x32_bf16 v[96:99], v[4:7], a[180:183], v[96:99]\n"
"buffer_load_dwordx4 v56, s[36:39], s56 offen offset:1024 sc1 lds\n"
"v_mfma_f32_16x16x32_bf16 v[104:107], v[0:3], a[184:187], v[104:107]\n"
"v_mfma_f32_16x16x32_bf16 v[104:107], v[4:7], a[188:191], v[104:107]\n"
"s_waitcnt lgkmcnt(0)\n"
"v_mfma_f32_16x16x32_bf16 v[68:71], v[8:11], a[144:147], v[68:71]\n"
"ds_read_b64_tr_b16 v[0:1], v20 offset:512\n"
"v_mfma_f32_16x16x32_bf16 v[68:71], v[12:15], a[148:151], v[68:71]\n"
"ds_read_b64_tr_b16 v[2:3], v20 offset:2560\n"
"v_mfma_f32_16x16x32_bf16 v[76:79], v[8:11], a[152:155], v[76:79]\n"
"ds_read_b64_tr_b16 v[4:5], v20 offset:4608\n"
"v_mfma_f32_16x16x32_bf16 v[76:79], v[12:15], a[156:159], v[76:79]\n"
"ds_read_b64_tr_b16 v[6:7], v20 offset:6656\n"
"v_mfma_f32_16x16x32_bf16 v[84:87], v[8:11], a[160:163], v[84:87]\n"
"v_mfma_f32_16x16x32_bf16 v[84:87], v[12:15], a[164:167], v[84:87]\n"
"v_mfma_f32_16x16x32_bf16 v[92:95], v[8:11], a[168:171], v[92:95]\n"
"v_mfma_f32_16x16x32_bf16 v[92:95], v[12:15], a[172:175], v[92:95]\n"
"buffer_load_dwordx4 v56, s[36:39], s56 offen offset:2048 sc1 lds\n"
"v_mfma_f32_16x16x32_bf16 v[100:103], v[8:11], a[176:179], v[100:103]\n"
"s_cmp_le_u32 s80, 0\n"
"s_cselect_b32 s66, 4, 0\n"
"s_add_u32 s66, s66, s82\n"
"s_lshl_b32 s66, s66, 17\n"
"s_or_b32 s66, s66, s81\n"
"s_xor_b32 s66, s66, 128\n"
"s_add_u32 m0, s68, 40960\n"
"s_nop 0\n"
"buffer_load_dwordx4 v44, s[40:43], s66 offen offset:0 lds\n"
"v_mfma_f32_16x16x32_bf16 v[100:103], v[12:15], a[180:183], v[100:103]\n"
"v_mfma_f32_16x16x32_bf16 v[108:111], v[8:11], a[184:187], v[108:111]\n"
"buffer_load_dwordx4 v45, s[40:43], s66 offen offset:1024 lds\n"
"v_mfma_f32_16x16x32_bf16 v[108:111], v[12:15], a[188:191], v[108:111]\n"
"s_waitcnt lgkmcnt(0)\n"
"v_mfma_f32_16x16x32_bf16 v[112:115], v[0:3], a[144:147], v[112:115]\n"
"ds_read_b64_tr_b16 v[8:9], v21 offset:512\n"
"v_mfma_f32_16x16x32_bf16 v[112:115], v[4:7], a[148:151], v[112:115]\n"
"ds_read_b64_tr_b16 v[10:11], v21 offset:2560\n"
"v_mfma_f32_16x16x32_bf16 v[120:123], v[0:3], a[152:155], v[120:123]\n"
"ds_read_b64_tr_b16 v[12:13], v21 offset:4608\n"
"v_mfma_f32_16x16x32_bf16 v[120:123], v[4:7], a[156:159], v[120:123]\n"
"ds_read_b64_tr_b16 v[14:15], v21 offset:6656\n"
"v_mfma_f32_16x16x32_bf16 v[128:131], v[0:3], a[160:163], v[128:131]\n"
"v_mfma_f32_16x16x32_bf16 v[128:131], v[4:7], a[164:167], v[128:131]\n"
"v_mfma_f32_16x16x32_bf16 v[136:139], v[0:3], a[168:171], v[136:139]\n"
"v_mfma_f32_16x16x32_bf16 v[136:139], v[4:7], a[172:175], v[136:139]\n"
"buffer_load_dwordx4 v46, s[40:43], s66 offen offset:2048 lds\n"
"v_mfma_f32_16x16x32_bf16 v[144:147], v[0:3], a[176:179], v[144:147]\n"
"v_mfma_f32_16x16x32_bf16 v[144:147], v[4:7], a[180:183], v[144:147]\n"
"buffer_load_dwordx4 v47, s[40:43], s66 offen offset:3072 lds\n"
"v_mfma_f32_16x16x32_bf16 v[152:155], v[0:3], a[184:187], v[152:155]\n"
"v_mfma_f32_16x16x32_bf16 v[152:155], v[4:7], a[188:191], v[152:155]\n"
"s_waitcnt vmcnt(4)\n"
"s_waitcnt vmcnt(16) lgkmcnt(0)\n"
"s_barrier\n"
"v_mfma_f32_16x16x32_bf16 v[116:119], v[8:11], a[144:147], v[116:119]\n"
"ds_read_b64_tr_b16 v[0:1], v20 offset:40960\n"
"v_mfma_f32_16x16x32_bf16 v[116:119], v[12:15], a[148:151], v[116:119]\n"
"ds_read_b64_tr_b16 v[2:3], v20 offset:43008\n"
"v_mfma_f32_16x16x32_bf16 v[124:127], v[8:11], a[152:155], v[124:127]\n"
"s_cmp_le_u32 s80, 1\n"
"ds_read_b64_tr_b16 v[4:5], v20 offset:45056\n"
"v_mfma_f32_16x16x32_bf16 v[124:127], v[12:15], a[156:159], v[124:127]\n"
"s_cselect_b32 s56, 5, 1\n"
"ds_read_b64_tr_b16 v[6:7], v20 offset:47104\n"
"v_mfma_f32_16x16x32_bf16 v[132:135], v[8:11], a[160:163], v[132:135]\n"
"s_add_u32 s56, s56, s82\n"
"ds_read_b128 a[0:3], v16 offset:0\n"
"v_mfma_f32_16x16x32_bf16 v[132:135], v[12:15], a[164:167], v[132:135]\n"
"s_mul_i32 s56, s56, 12288\n"
"ds_read_b128 a[4:7], v16 offset:6144\n"
"v_mfma_f32_16x16x32_bf16 v[140:143], v[8:11], a[168:171], v[140:143]\n"
"s_add_u32 m0, s67, 40960\n"
"ds_read_b128 a[8:11], v16 offset:1024\n"
"v_mfma_f32_16x16x32_bf16 v[140:143], v[12:15], a[172:175], v[140:143]\n"
"buffer_load_dwordx4 v56, s[36:39], s56 offen offset:0 sc1 lds\n"
"v_mfma_f32_16x16x32_bf16 v[148:151], v[8:11], a[176:179], v[148:151]\n"
"buffer_load_dwordx4 v56, s[36:39], s56 offen offset:1024 sc1 lds\n"
"v_mfma_f32_16x16x32_bf16 v[148:151], v[12:15], a[180:183], v[148:151]\n"
"v_mfma_f32_16x16x32_bf16 v[156:159], v[8:11], a[184:187], v[156:159]\n"
"buffer_load_dwordx4 v56, s[36:39], s56 offen offset:2048 sc1 lds\n"
"v_mfma_f32_16x16x32_bf16 v[156:159], v[12:15], a[188:191], v[156:159]\n"
"s_waitcnt lgkmcnt(3)\n"
"v_mfma_f32_16x16x32_bf16 v[160:163], v[0:3], a[144:147], v[160:163]\n"
"ds_read_b64_tr_b16 v[8:9], v21 offset:40960\n"
"v_mfma_f32_16x16x32_bf16 v[160:163], v[4:7], a[148:151], v[160:163]\n"
"s_cmp_le_u32 s80, 1\n"
"ds_read_b64_tr_b16 v[10:11], v21 offset:43008\n"
"v_mfma_f32_16x16x32_bf16 v[168:171], v[0:3], a[152:155], v[168:171]\n"
"s_cselect_b32 s56, 5, 1\n"
"ds_read_b64_tr_b16 v[12:13], v21 offset:45056\n"
"v_mfma_f32_16x16x32_bf16 v[168:171], v[4:7], a[156:159], v[168:171]\n"
"s_add_u32 s56, s56, s82\n"
"ds_read_b64_tr_b16 v[14:15], v21 offset:47104\n"
"v_mfma_f32_16x16x32_bf16 v[176:179], v[0:3], a[160:163], v[176:179]\n"
"s_mul_i32 s56, s56, 12288\n"
"ds_read_b128 a[12:15], v16 offset:7168\n"
"v_mfma_f32_16x16x32_bf16 v[176:179], v[4:7], a[164:167], v[176:179]\n"
"s_add_u32 s56, s56, 3072\n"
"ds_read_b128 a[16:19], v16 offset:2048\n"
"v_mfma_f32_16x16x32_bf16 v[184:187], v[0:3], a[168:171], v[184:187]\n"
"s_add_u32 m0, s67, 44032\n"
"ds_read_b128 a[20:23], v16 offset:8192\n"
"v_mfma_f32_16x16x32_bf16 v[184:187], v[4:7], a[172:175], v[184:187]\n"
"buffer_load_dwordx4 v56, s[36:39], s56 offen offset:0 sc1 lds\n"
"v_mfma_f32_16x16x32_bf16 v[192:195], v[0:3], a[176:179], v[192:195]\n"
"v_mfma_f32_16x16x32_bf16 v[192:195], v[4:7], a[180:183], v[192:195]\n"
"buffer_load_dwordx4 v56, s[36:39], s56 offen offset:1024 sc1 lds\n"
"v_mfma_f32_16x16x32_bf16 v[200:203], v[0:3], a[184:187], v[200:203]\n"
"v_mfma_f32_16x16x32_bf16 v[200:203], v[4:7], a[188:191], v[200:203]\n"
"s_waitcnt lgkmcnt(3)\n"
"v_mfma_f32_16x16x32_bf16 v[164:167], v[8:11], a[144:147], v[164:167]\n"
"ds_read_b64_tr_b16 v[0:1], v20 offset:41472\n"
"v_mfma_f32_16x16x32_bf16 v[164:167], v[12:15], a[148:151], v[164:167]\n"
"ds_read_b64_tr_b16 v[2:3], v20 offset:43520\n"
"v_mfma_f32_16x16x32_bf16 v[172:175], v[8:11], a[152:155], v[172:175]\n"
"ds_read_b64_tr_b16 v[4:5], v20 offset:45568\n"
"v_mfma_f32_16x16x32_bf16 v[172:175], v[12:15], a[156:159], v[172:175]\n"
"ds_read_b64_tr_b16 v[6:7], v20 offset:47616\n"
"v_mfma_f32_16x16x32_bf16 v[180:183], v[8:11], a[160:163], v[180:183]\n"
"ds_read_b128 a[24:27], v16 offset:3072\n"
"v_mfma_f32_16x16x32_bf16 v[180:183], v[12:15], a[164:167], v[180:183]\n"
"ds_read_b128 a[28:31], v16 offset:9216\n"
"v_mfma_f32_16x16x32_bf16 v[188:191], v[8:11], a[168:171], v[188:191]\n"
"ds_read_b128 a[32:35], v16 offset:4096\n"
"v_mfma_f32_16x16x32_bf16 v[188:191], v[12:15], a[172:175], v[188:191]\n"
"buffer_load_dwordx4 v56, s[36:39], s56 offen offset:2048 sc1 lds\n"
"v_mfma_f32_16x16x32_bf16 v[196:199], v[8:11], a[176:179], v[196:199]\n"
"s_cmp_le_u32 s80, 1\n"
"s_cselect_b32 s66, 5, 1\n"
"s_add_u32 s66, s66, s82\n"
"s_lshl_b32 s66, s66, 17\n"
"s_or_b32 s66, s66, s81\n"
"s_add_u32 m0, s68, 81920\n"
"s_nop 0\n"
"buffer_load_dwordx4 v44, s[40:43], s66 offen offset:0 lds\n"
"v_mfma_f32_16x16x32_bf16 v[196:199], v[12:15], a[180:183], v[196:199]\n"
"v_mfma_f32_16x16x32_bf16 v[204:207], v[8:11], a[184:187], v[204:207]\n"
"buffer_load_dwordx4 v45, s[40:43], s66 offen offset:1024 lds\n"
"v_mfma_f32_16x16x32_bf16 v[204:207], v[12:15], a[188:191], v[204:207]\n"
"s_waitcnt lgkmcnt(3)\n"
"v_mfma_f32_16x16x32_bf16 v[208:211], v[0:3], a[144:147], v[208:211]\n"
"ds_read_b64_tr_b16 v[8:9], v21 offset:41472\n"
"v_mfma_f32_16x16x32_bf16 v[208:211], v[4:7], a[148:151], v[208:211]\n"
"ds_read_b64_tr_b16 v[10:11], v21 offset:43520\n"
"v_mfma_f32_16x16x32_bf16 v[216:219], v[0:3], a[152:155], v[216:219]\n"
"ds_read_b64_tr_b16 v[12:13], v21 offset:45568\n"
"v_mfma_f32_16x16x32_bf16 v[216:219], v[4:7], a[156:159], v[216:219]\n"
"ds_read_b64_tr_b16 v[14:15], v21 offset:47616\n"
"v_mfma_f32_16x16x32_bf16 v[224:227], v[0:3], a[160:163], v[224:227]\n"
"ds_read_b128 a[36:39], v16 offset:10240\n"
"v_mfma_f32_16x16x32_bf16 v[224:227], v[4:7], a[164:167], v[224:227]\n"
"ds_read_b128 a[40:43], v16 offset:5120\n"
"v_mfma_f32_16x16x32_bf16 v[232:235], v[0:3], a[168:171], v[232:235]\n"
"ds_read_b128 a[44:47], v16 offset:11264\n"
"v_mfma_f32_16x16x32_bf16 v[232:235], v[4:7], a[172:175], v[232:235]\n"
"buffer_load_dwordx4 v46, s[40:43], s66 offen offset:2048 lds\n"
"v_mfma_f32_16x16x32_bf16 v[240:243], v[0:3], a[176:179], v[240:243]\n"
"v_mfma_f32_16x16x32_bf16 v[240:243], v[4:7], a[180:183], v[240:243]\n"
"buffer_load_dwordx4 v47, s[40:43], s66 offen offset:3072 lds\n"
"v_mfma_f32_16x16x32_bf16 v[248:251], v[0:3], a[184:187], v[248:251]\n"
"v_mfma_f32_16x16x32_bf16 v[248:251], v[4:7], a[188:191], v[248:251]\n"
"s_waitcnt vmcnt(21) lgkmcnt(0)\n"
"s_barrier\n"
"v_mfma_f32_16x16x32_bf16 v[212:215], v[8:11], a[144:147], v[212:215]\n"
"ds_read_b64_tr_b16 v[0:1], v18 offset:0\n"
"v_mfma_f32_16x16x32_bf16 v[212:215], v[12:15], a[148:151], v[212:215]\n"
"ds_read_b64_tr_b16 v[2:3], v18 offset:2048\n"
"v_mfma_f32_16x16x32_bf16 v[220:223], v[8:11], a[152:155], v[220:223]\n"
"s_cmp_le_u32 s80, 2\n"
"ds_read_b64_tr_b16 v[4:5], v18 offset:4096\n"
"v_mfma_f32_16x16x32_bf16 v[220:223], v[12:15], a[156:159], v[220:223]\n"
"s_cselect_b32 s56, 6, 2\n"
"ds_read_b64_tr_b16 v[6:7], v18 offset:6144\n"
"v_mfma_f32_16x16x32_bf16 v[228:231], v[8:11], a[160:163], v[228:231]\n"
"s_add_u32 s56, s56, s82\n"
"v_mfma_f32_16x16x32_bf16 v[228:231], v[12:15], a[164:167], v[228:231]\n"
"s_mul_i32 s56, s56, 12288\n"
"v_mfma_f32_16x16x32_bf16 v[236:239], v[8:11], a[168:171], v[236:239]\n"
"s_add_u32 m0, s67, 81920\n"
"v_mfma_f32_16x16x32_bf16 v[236:239], v[12:15], a[172:175], v[236:239]\n"
"buffer_load_dwordx4 v56, s[36:39], s56 offen offset:0 sc1 lds\n"
"v_mfma_f32_16x16x32_bf16 v[244:247], v[8:11], a[176:179], v[244:247]\n"
"buffer_load_dwordx4 v56, s[36:39], s56 offen offset:1024 sc1 lds\n"
"v_mfma_f32_16x16x32_bf16 v[244:247], v[12:15], a[180:183], v[244:247]\n"
"buffer_load_dwordx4 v56, s[36:39], s56 offen offset:2048 sc1 lds\n"
"v_mfma_f32_16x16x32_bf16 v[252:255], v[8:11], a[184:187], v[252:255]\n"
"s_cmp_le_u32 s80, 2\n"
"s_cselect_b32 s56, 6, 2\n"
"s_add_u32 s56, s56, s82\n"
"s_mul_i32 s56, s56, 12288\n"
"s_add_u32 s56, s56, 3072\n"
"s_add_u32 m0, s67, 84992\n"
"s_nop 0\n"
"buffer_load_dwordx4 v56, s[36:39], s56 offen offset:0 sc1 lds\n"
"v_mfma_f32_16x16x32_bf16 v[252:255], v[12:15], a[188:191], v[252:255]\n"
"s_waitcnt lgkmcnt(0)\n"
"v_mfma_f32_16x16x32_bf16 v[64:67], v[0:3], a[0:3], v[64:67]\n"
"ds_read_b64_tr_b16 v[8:9], v19 offset:0\n"
"v_mfma_f32_16x16x32_bf16 v[64:67], v[4:7], a[4:7], v[64:67]\n"
"ds_read_b64_tr_b16 v[10:11], v19 offset:2048\n"
"v_mfma_f32_16x16x32_bf16 v[72:75], v[0:3], a[8:11], v[72:75]\n"
"ds_read_b64_tr_b16 v[12:13], v19 offset:4096\n"
"v_mfma_f32_16x16x32_bf16 v[72:75], v[4:7], a[12:15], v[72:75]\n"
"ds_read_b64_tr_b16 v[14:15], v19 offset:6144\n"
"v_mfma_f32_16x16x32_bf16 v[80:83], v[0:3], a[16:19], v[80:83]\n"
"v_mfma_f32_16x16x32_bf16 v[80:83], v[4:7], a[20:23], v[80:83]\n"
"v_mfma_f32_16x16x32_bf16 v[88:91], v[0:3], a[24:27], v[88:91]\n"
"v_mfma_f32_16x16x32_bf16 v[88:91], v[4:7], a[28:31], v[88:91]\n"
"buffer_load_dwordx4 v56, s[36:39], s56 offen offset:1024 sc1 lds\n"
"v_mfma_f32_16x16x32_bf16 v[96:99], v[0:3], a[32:35], v[96:99]\n"
"v_mfma_f32_16x16x32_bf16 v[96:99], v[4:7], a[36:39], v[96:99]\n"
"buffer_load_dwordx4 v56, s[36:39], s56 offen offset:2048 sc1 lds\n"
"v_mfma_f32_16x16x32_bf16 v[104:107], v[0:3], a[40:43], v[104:107]\n"
"s_cmp_le_u32 s80, 1\n"
"s_cselect_b32 s66, 5, 1\n"
"s_add_u32 s66, s66, s82\n"
"s_lshl_b32 s66, s66, 17\n"
"s_or_b32 s66, s66, s81\n"
"s_xor_b32 s66, s66, 128\n"
"s_add_u32 m0, s68, 122880\n"
"s_nop 0\n"
"buffer_load_dwordx4 v44, s[40:43], s66 offen offset:0 lds\n"
"v_mfma_f32_16x16x32_bf16 v[104:107], v[4:7], a[44:47], v[104:107]\n"
"s_waitcnt lgkmcnt(0)\n"
"v_mfma_f32_16x16x32_bf16 v[68:71], v[8:11], a[0:3], v[68:71]\n"
"ds_read_b64_tr_b16 v[0:1], v18 offset:512\n"
"v_mfma_f32_16x16x32_bf16 v[68:71], v[12:15], a[4:7], v[68:71]\n"
"ds_read_b64_tr_b16 v[2:3], v18 offset:2560\n"
"v_mfma_f32_16x16x32_bf16 v[76:79], v[8:11], a[8:11], v[76:79]\n"
"ds_read_b64_tr_b16 v[4:5], v18 offset:4608\n"
"v_mfma_f32_16x16x32_bf16 v[76:79], v[12:15], a[12:15], v[76:79]\n"
"ds_read_b64_tr_b16 v[6:7], v18 offset:6656\n"
"v_mfma_f32_16x16x32_bf16 v[84:87], v[8:11], a[16:19], v[84:87]\n"
"v_mfma_f32_16x16x32_bf16 v[84:87], v[12:15], a[20:23], v[84:87]\n"
"v_mfma_f32_16x16x32_bf16 v[92:95], v[8:11], a[24:27], v[92:95]\n"
"v_mfma_f32_16x16x32_bf16 v[92:95], v[12:15], a[28:31], v[92:95]\n"
"buffer_load_dwordx4 v45, s[40:43], s66 offen offset:1024 lds\n"
"v_mfma_f32_16x16x32_bf16 v[100:103], v[8:11], a[32:35], v[100:103]\n"
"buffer_load_dwordx4 v46, s[40:43], s66 offen offset:2048 lds\n"
"v_mfma_f32_16x16x32_bf16 v[100:103], v[12:15], a[36:39], v[100:103]\n"
"v_mfma_f32_16x16x32_bf16 v[108:111], v[8:11], a[40:43], v[108:111]\n"
"buffer_load_dwordx4 v47, s[40:43], s66 offen offset:3072 lds\n"
"v_mfma_f32_16x16x32_bf16 v[108:111], v[12:15], a[44:47], v[108:111]\n"
"s_waitcnt lgkmcnt(0)\n"
"v_mfma_f32_16x16x32_bf16 v[112:115], v[0:3], a[0:3], v[112:115]\n"
"ds_read_b64_tr_b16 v[8:9], v19 offset:512\n"
"v_mfma_f32_16x16x32_bf16 v[112:115], v[4:7], a[4:7], v[112:115]\n"
"ds_read_b64_tr_b16 v[10:11], v19 offset:2560\n"
"v_mfma_f32_16x16x32_bf16 v[120:123], v[0:3], a[8:11], v[120:123]\n"
"s_cmp_le_u32 s80, 4\n"
"ds_read_b64_tr_b16 v[12:13], v19 offset:4608\n"
"v_mfma_f32_16x16x32_bf16 v[120:123], v[4:7], a[12:15], v[120:123]\n"
"s_cselect_b32 s56, 8, 4\n"
"ds_read_b64_tr_b16 v[14:15], v19 offset:6656\n"
"v_mfma_f32_16x16x32_bf16 v[128:131], v[0:3], a[16:19], v[128:131]\n"
"s_add_u32 s56, s56, s82\n"
"v_mfma_f32_16x16x32_bf16 v[128:131], v[4:7], a[20:23], v[128:131]\n"
"s_mul_i32 s56, s56, 12288\n"
"v_mfma_f32_16x16x32_bf16 v[136:139], v[0:3], a[24:27], v[136:139]\n"
"s_add_u32 m0, s67, 0\n"
"v_mfma_f32_16x16x32_bf16 v[136:139], v[4:7], a[28:31], v[136:139]\n"
"buffer_load_dwordx4 v56, s[36:39], s56 offen offset:0 sc1 lds\n"
"v_mfma_f32_16x16x32_bf16 v[144:147], v[0:3], a[32:35], v[144:147]\n"
"buffer_load_dwordx4 v56, s[36:39], s56 offen offset:1024 sc1 lds\n"
"v_mfma_f32_16x16x32_bf16 v[144:147], v[4:7], a[36:39], v[144:147]\n"
"buffer_load_dwordx4 v56, s[36:39], s56 offen offset:2048 sc1 lds\n"
"v_mfma_f32_16x16x32_bf16 v[152:155], v[0:3], a[40:43], v[152:155]\n"
"v_mfma_f32_16x16x32_bf16 v[152:155], v[4:7], a[44:47], v[152:155]\n"
"s_waitcnt vmcnt(17)\n"
"s_waitcnt vmcnt(23) lgkmcnt(0)\n"
"s_barrier\n"
"v_mfma_f32_16x16x32_bf16 v[116:119], v[8:11], a[0:3], v[116:119]\n"
"ds_read_b64_tr_b16 v[0:1], v18 offset:40960\n"
"v_mfma_f32_16x16x32_bf16 v[116:119], v[12:15], a[4:7], v[116:119]\n"
"ds_read_b64_tr_b16 v[2:3], v18 offset:43008\n"
"v_mfma_f32_16x16x32_bf16 v[124:127], v[8:11], a[8:11], v[124:127]\n"
"s_cmp_le_u32 s80, 3\n"
"ds_read_b64_tr_b16 v[4:5], v18 offset:45056\n"
"v_mfma_f32_16x16x32_bf16 v[124:127], v[12:15], a[12:15], v[124:127]\n"
"s_cselect_b32 s56, 7, 3\n"
"ds_read_b64_tr_b16 v[6:7], v18 offset:47104\n"
"v_mfma_f32_16x16x32_bf16 v[132:135], v[8:11], a[16:19], v[132:135]\n"
"s_add_u32 s56, s56, s82\n"
"ds_read_b128 a[48:51], v16 offset:40960\n"
"v_mfma_f32_16x16x32_bf16 v[132:135], v[12:15], a[20:23], v[132:135]\n"
"s_mul_i32 s56, s56, 12288\n"
"ds_read_b128 a[52:55], v16 offset:47104\n"
"v_mfma_f32_16x16x32_bf16 v[140:143], v[8:11], a[24:27], v[140:143]\n"
"s_add_u32 m0, s67, 122880\n"
"ds_read_b128 a[56:59], v16 offset:41984\n"
"v_mfma_f32_16x16x32_bf16 v[140:143], v[12:15], a[28:31], v[140:143]\n"
"buffer_load_dwordx4 v56, s[36:39], s56 offen offset:0 sc1 lds\n"
"v_mfma_f32_16x16x32_bf16 v[148:151], v[8:11], a[32:35], v[148:151]\n"
"buffer_load_dwordx4 v56, s[36:39], s56 offen offset:1024 sc1 lds\n"
"v_mfma_f32_16x16x32_bf16 v[148:151], v[12:15], a[36:39], v[148:151]\n"
"buffer_load_dwordx4 v56, s[36:39], s56 offen offset:2048 sc1 lds\n"
"v_mfma_f32_16x16x32_bf16 v[156:159], v[8:11], a[40:43], v[156:159]\n"
"s_cmp_le_u32 s80, 3\n"
"s_cselect_b32 s56, 7, 3\n"
"s_add_u32 s56, s56, s82\n"
"s_mul_i32 s56, s56, 12288\n"
"s_add_u32 s56, s56, 3072\n"
"s_add_u32 m0, s67, 125952\n"
"s_nop 0\n"
"buffer_load_dwordx4 v56, s[36:39], s56 offen offset:0 sc1 lds\n"
"v_mfma_f32_16x16x32_bf16 v[156:159], v[12:15], a[44:47], v[156:159]\n"
"s_waitcnt lgkmcnt(3)\n"
"v_mfma_f32_16x16x32_bf16 v[160:163], v[0:3], a[0:3], v[160:163]\n"
"ds_read_b64_tr_b16 v[8:9], v19 offset:40960\n"
"v_mfma_f32_16x16x32_bf16 v[160:163], v[4:7], a[4:7], v[160:163]\n"
"ds_read_b64_tr_b16 v[10:11], v19 offset:43008\n"
"v_mfma_f32_16x16x32_bf16 v[168:171], v[0:3], a[8:11], v[168:171]\n"
"ds_read_b64_tr_b16 v[12:13], v19 offset:45056\n"
"v_mfma_f32_16x16x32_bf16 v[168:171], v[4:7], a[12:15], v[168:171]\n"
"ds_read_b64_tr_b16 v[14:15], v19 offset:47104\n"
"v_mfma_f32_16x16x32_bf16 v[176:179], v[0:3], a[16:19], v[176:179]\n"
"ds_read_b128 a[60:63], v16 offset:48128\n"
"v_mfma_f32_16x16x32_bf16 v[176:179], v[4:7], a[20:23], v[176:179]\n"
"ds_read_b128 a[64:67], v16 offset:43008\n"
"v_mfma_f32_16x16x32_bf16 v[184:187], v[0:3], a[24:27], v[184:187]\n"
"ds_read_b128 a[68:71], v16 offset:49152\n"
"v_mfma_f32_16x16x32_bf16 v[184:187], v[4:7], a[28:31], v[184:187]\n"
"buffer_load_dwordx4 v56, s[36:39], s56 offen offset:1024 sc1 lds\n"
"v_mfma_f32_16x16x32_bf16 v[192:195], v[0:3], a[32:35], v[192:195]\n"
"v_mfma_f32_16x16x32_bf16 v[192:195], v[4:7], a[36:39], v[192:195]\n"
"buffer_load_dwordx4 v56, s[36:39], s56 offen offset:2048 sc1 lds\n"
"v_mfma_f32_16x16x32_bf16 v[200:203], v[0:3], a[40:43], v[200:203]\n"
"s_cmp_le_u32 s80, 2\n"
"s_cselect_b32 s66, 6, 2\n"
"s_add_u32 s66, s66, s82\n"
"s_lshl_b32 s66, s66, 17\n"
"s_or_b32 s66, s66, s81\n"
"s_add_u32 m0, s68, 0\n"
"s_nop 0\n"
"buffer_load_dwordx4 v44, s[40:43], s66 offen offset:0 lds\n"
"v_mfma_f32_16x16x32_bf16 v[200:203], v[4:7], a[44:47], v[200:203]\n"
"s_waitcnt lgkmcnt(3)\n"
"v_mfma_f32_16x16x32_bf16 v[164:167], v[8:11], a[0:3], v[164:167]\n"
"ds_read_b64_tr_b16 v[0:1], v18 offset:41472\n"
"v_mfma_f32_16x16x32_bf16 v[164:167], v[12:15], a[4:7], v[164:167]\n"
"ds_read_b64_tr_b16 v[2:3], v18 offset:43520\n"
"v_mfma_f32_16x16x32_bf16 v[172:175], v[8:11], a[8:11], v[172:175]\n"
"ds_read_b64_tr_b16 v[4:5], v18 offset:45568\n"
"v_mfma_f32_16x16x32_bf16 v[172:175], v[12:15], a[12:15], v[172:175]\n"
"ds_read_b64_tr_b16 v[6:7], v18 offset:47616\n"
"v_mfma_f32_16x16x32_bf16 v[180:183], v[8:11], a[16:19], v[180:183]\n"
"ds_read_b128 a[72:75], v16 offset:44032\n"
"v_mfma_f32_16x16x32_bf16 v[180:183], v[12:15], a[20:23], v[180:183]\n"
"ds_read_b128 a[76:79], v16 offset:50176\n"
"v_mfma_f32_16x16x32_bf16 v[188:191], v[8:11], a[24:27], v[188:191]\n"
"ds_read_b128 a[80:83], v16 offset:45056\n"
"v_mfma_f32_16x16x32_bf16 v[188:191], v[12:15], a[28:31], v[188:191]\n"
"buffer_load_dwordx4 v45, s[40:43], s66 offen offset:1024 lds\n"
"v_mfma_f32_16x16x32_bf16 v[196:199], v[8:11], a[32:35], v[196:199]\n"
"buffer_load_dwordx4 v46, s[40:43], s66 offen offset:2048 lds\n"
"v_mfma_f32_16x16x32_bf16 v[196:199], v[12:15], a[36:39], v[196:199]\n"
"v_mfma_f32_16x16x32_bf16 v[204:207], v[8:11], a[40:43], v[204:207]\n"
"buffer_load_dwordx4 v47, s[40:43], s66 offen offset:3072 lds\n"
"v_mfma_f32_16x16x32_bf16 v[204:207], v[12:15], a[44:47], v[204:207]\n"
"s_waitcnt lgkmcnt(3)\n"
"v_mfma_f32_16x16x32_bf16 v[208:211], v[0:3], a[0:3], v[208:211]\n"
"ds_read_b64_tr_b16 v[8:9], v19 offset:41472\n"
"v_mfma_f32_16x16x32_bf16 v[208:211], v[4:7], a[4:7], v[208:211]\n"
"s_cmp_le_u32 s80, 4\n"
"ds_read_b64_tr_b16 v[10:11], v19 offset:43520\n"
"v_mfma_f32_16x16x32_bf16 v[216:219], v[0:3], a[8:11], v[216:219]\n"
"s_cselect_b32 s56, 8, 4\n"
"ds_read_b64_tr_b16 v[12:13], v19 offset:45568\n"
"v_mfma_f32_16x16x32_bf16 v[216:219], v[4:7], a[12:15], v[216:219]\n"
"s_add_u32 s56, s56, s82\n"
"ds_read_b64_tr_b16 v[14:15], v19 offset:47616\n"
"v_mfma_f32_16x16x32_bf16 v[224:227], v[0:3], a[16:19], v[224:227]\n"
"s_mul_i32 s56, s56, 12288\n"
"ds_read_b128 a[84:87], v16 offset:51200\n"
"v_mfma_f32_16x16x32_bf16 v[224:227], v[4:7], a[20:23], v[224:227]\n"
"s_add_u32 s56, s56, 3072\n"
"ds_read_b128 a[88:91], v16 offset:46080\n"
"v_mfma_f32_16x16x32_bf16 v[232:235], v[0:3], a[24:27], v[232:235]\n"
"s_add_u32 m0, s67, 3072\n"
"ds_read_b128 a[92:95], v16 offset:52224\n"
"v_mfma_f32_16x16x32_bf16 v[232:235], v[4:7], a[28:31], v[232:235]\n"
"buffer_load_dwordx4 v56, s[36:39], s56 offen offset:0 sc1 lds\n"
"v_mfma_f32_16x16x32_bf16 v[240:243], v[0:3], a[32:35], v[240:243]\n"
"buffer_load_dwordx4 v56, s[36:39], s56 offen offset:1024 sc1 lds\n"
"v_mfma_f32_16x16x32_bf16 v[240:243], v[4:7], a[36:39], v[240:243]\n"
"buffer_load_dwordx4 v56, s[36:39], s56 offen offset:2048 sc1 lds\n"
"v_mfma_f32_16x16x32_bf16 v[248:251], v[0:3], a[40:43], v[248:251]\n"
"v_mfma_f32_16x16x32_bf16 v[248:251], v[4:7], a[44:47], v[248:251]\n"
"s_waitcnt vmcnt(26) lgkmcnt(0)\n"
"s_barrier\n"
"v_mfma_f32_16x16x32_bf16 v[212:215], v[8:11], a[0:3], v[212:215]\n"
"s_cmp_le_u32 s80, 2\n"
"ds_read_b64_tr_b16 v[0:1], v20 offset:0\n"
"v_mfma_f32_16x16x32_bf16 v[212:215], v[12:15], a[4:7], v[212:215]\n"
"s_cselect_b32 s66, 6, 2\n"
"ds_read_b64_tr_b16 v[2:3], v20 offset:2048\n"
"v_mfma_f32_16x16x32_bf16 v[220:223], v[8:11], a[8:11], v[220:223]\n"
"s_add_u32 s66, s66, s82\n"
"ds_read_b64_tr_b16 v[4:5], v20 offset:4096\n"
"v_mfma_f32_16x16x32_bf16 v[220:223], v[12:15], a[12:15], v[220:223]\n"
"s_lshl_b32 s66, s66, 17\n"
"ds_read_b64_tr_b16 v[6:7], v20 offset:6144\n"
"v_mfma_f32_16x16x32_bf16 v[228:231], v[8:11], a[16:19], v[228:231]\n"
"s_or_b32 s66, s66, s81\n"
"v_mfma_f32_16x16x32_bf16 v[228:231], v[12:15], a[20:23], v[228:231]\n"
"s_xor_b32 s66, s66, 128\n"
"v_mfma_f32_16x16x32_bf16 v[236:239], v[8:11], a[24:27], v[236:239]\n"
"s_add_u32 m0, s68, 40960\n"
"v_mfma_f32_16x16x32_bf16 v[236:239], v[12:15], a[28:31], v[236:239]\n"
"buffer_load_dwordx4 v44, s[40:43], s66 offen offset:0 lds\n"
"v_mfma_f32_16x16x32_bf16 v[244:247], v[8:11], a[32:35], v[244:247]\n"
"v_mfma_f32_16x16x32_bf16 v[244:247], v[12:15], a[36:39], v[244:247]\n"
"buffer_load_dwordx4 v45, s[40:43], s66 offen offset:1024 lds\n"
"v_mfma_f32_16x16x32_bf16 v[252:255], v[8:11], a[40:43], v[252:255]\n"
"v_mfma_f32_16x16x32_bf16 v[252:255], v[12:15], a[44:47], v[252:255]\n"
"s_waitcnt lgkmcnt(0)\n"
"v_mfma_f32_16x16x32_bf16 v[64:67], v[0:3], a[48:51], v[64:67]\n"
"ds_read_b64_tr_b16 v[8:9], v21 offset:0\n"
"v_mfma_f32_16x16x32_bf16 v[64:67], v[4:7], a[52:55], v[64:67]\n"
"ds_read_b64_tr_b16 v[10:11], v21 offset:2048\n"
"v_mfma_f32_16x16x32_bf16 v[72:75], v[0:3], a[56:59], v[72:75]\n"
"ds_read_b64_tr_b16 v[12:13], v21 offset:4096\n"
"v_mfma_f32_16x16x32_bf16 v[72:75], v[4:7], a[60:63], v[72:75]\n"
"ds_read_b64_tr_b16 v[14:15], v21 offset:6144\n"
"v_mfma_f32_16x16x32_bf16 v[80:83], v[0:3], a[64:67], v[80:83]\n"
"v_mfma_f32_16x16x32_bf16 v[80:83], v[4:7], a[68:71], v[80:83]\n"
"v_mfma_f32_16x16x32_bf16 v[88:91], v[0:3], a[72:75], v[88:91]\n"
"v_mfma_f32_16x16x32_bf16 v[88:91], v[4:7], a[76:79], v[88:91]\n"
"buffer_load_dwordx4 v46, s[40:43], s66 offen offset:2048 lds\n"
"v_mfma_f32_16x16x32_bf16 v[96:99], v[0:3], a[80:83], v[96:99]\n"
"v_mfma_f32_16x16x32_bf16 v[96:99], v[4:7], a[84:87], v[96:99]\n"
"buffer_load_dwordx4 v47, s[40:43], s66 offen offset:3072 lds\n"
"v_mfma_f32_16x16x32_bf16 v[104:107], v[0:3], a[88:91], v[104:107]\n"
"v_mfma_f32_16x16x32_bf16 v[104:107], v[4:7], a[92:95], v[104:107]\n"
"s_waitcnt lgkmcnt(0)\n"
"v_mfma_f32_16x16x32_bf16 v[68:71], v[8:11], a[48:51], v[68:71]\n"
"ds_read_b64_tr_b16 v[0:1], v20 offset:512\n"
"v_mfma_f32_16x16x32_bf16 v[68:71], v[12:15], a[52:55], v[68:71]\n"
"ds_read_b64_tr_b16 v[2:3], v20 offset:2560\n"
"v_mfma_f32_16x16x32_bf16 v[76:79], v[8:11], a[56:59], v[76:79]\n"
"ds_read_b64_tr_b16 v[4:5], v20 offset:4608\n"
"v_mfma_f32_16x16x32_bf16 v[76:79], v[12:15], a[60:63], v[76:79]\n"
"s_cmp_le_u32 s80, 5\n"
"ds_read_b64_tr_b16 v[6:7], v20 offset:6656\n"
"v_mfma_f32_16x16x32_bf16 v[84:87], v[8:11], a[64:67], v[84:87]\n"
"s_cselect_b32 s56, 9, 5\n"
"v_mfma_f32_16x16x32_bf16 v[84:87], v[12:15], a[68:71], v[84:87]\n"
"s_add_u32 s56, s56, s82\n"
"v_mfma_f32_16x16x32_bf16 v[92:95], v[8:11], a[72:75], v[92:95]\n"
"s_mul_i32 s56, s56, 12288\n"
"v_mfma_f32_16x16x32_bf16 v[92:95], v[12:15], a[76:79], v[92:95]\n"
"s_add_u32 m0, s67, 40960\n"
"v_mfma_f32_16x16x32_bf16 v[100:103], v[8:11], a[80:83], v[100:103]\n"
"buffer_load_dwordx4 v56, s[36:39], s56 offen offset:0 sc1 lds\n"
"v_mfma_f32_16x16x32_bf16 v[100:103], v[12:15], a[84:87], v[100:103]\n"
"v_mfma_f32_16x16x32_bf16 v[108:111], v[8:11], a[88:91], v[108:111]\n"
"buffer_load_dwordx4 v56, s[36:39], s56 offen offset:1024 sc1 lds\n"
"v_mfma_f32_16x16x32_bf16 v[108:111], v[12:15], a[92:95], v[108:111]\n"
"s_waitcnt lgkmcnt(0)\n"
"v_mfma_f32_16x16x32_bf16 v[112:115], v[0:3], a[48:51], v[112:115]\n"
"ds_read_b64_tr_b16 v[8:9], v21 offset:512\n"
"v_mfma_f32_16x16x32_bf16 v[112:115], v[4:7], a[52:55], v[112:115]\n"
"ds_read_b64_tr_b16 v[10:11], v21 offset:2560\n"
"v_mfma_f32_16x16x32_bf16 v[120:123], v[0:3], a[56:59], v[120:123]\n"
"ds_read_b64_tr_b16 v[12:13], v21 offset:4608\n"
"v_mfma_f32_16x16x32_bf16 v[120:123], v[4:7], a[60:63], v[120:123]\n"
"ds_read_b64_tr_b16 v[14:15], v21 offset:6656\n"
"v_mfma_f32_16x16x32_bf16 v[128:131], v[0:3], a[64:67], v[128:131]\n"
"v_mfma_f32_16x16x32_bf16 v[128:131], v[4:7], a[68:71], v[128:131]\n"
"v_mfma_f32_16x16x32_bf16 v[136:139], v[0:3], a[72:75], v[136:139]\n"
"v_mfma_f32_16x16x32_bf16 v[136:139], v[4:7], a[76:79], v[136:139]\n"
"v_mfma_f32_16x16x32_bf16 v[144:147], v[0:3], a[80:83], v[144:147]\n"
"buffer_load_dwordx4 v56, s[36:39], s56 offen offset:2048 sc1 lds\n"
"v_mfma_f32_16x16x32_bf16 v[144:147], v[4:7], a[84:87], v[144:147]\n"
"v_mfma_f32_16x16x32_bf16 v[152:155], v[0:3], a[88:91], v[152:155]\n"
"v_mfma_f32_16x16x32_bf16 v[152:155], v[4:7], a[92:95], v[152:155]\n"
"s_waitcnt vmcnt(27)\n"
"s_waitcnt vmcnt(23) lgkmcnt(0)\n"
"s_barrier\n"
"v_mfma_f32_16x16x32_bf16 v[116:119], v[8:11], a[48:51], v[116:119]\n"
"ds_read_b64_tr_b16 v[0:1], v20 offset:40960\n"
"v_mfma_f32_16x16x32_bf16 v[116:119], v[12:15], a[52:55], v[116:119]\n"
"s_cmp_le_u32 s80, 3\n"
"ds_read_b64_tr_b16 v[2:3], v20 offset:43008\n"
"v_mfma_f32_16x16x32_bf16 v[124:127], v[8:11], a[56:59], v[124:127]\n"
"s_cselect_b32 s66, 7, 3\n"
"ds_read_b64_tr_b16 v[4:5], v20 offset:45056\n"
"v_mfma_f32_16x16x32_bf16 v[124:127], v[12:15], a[60:63], v[124:127]\n"
"s_add_u32 s66, s66, s82\n"
"ds_read_b64_tr_b16 v[6:7], v20 offset:47104\n"
"v_mfma_f32_16x16x32_bf16 v[132:135], v[8:11], a[64:67], v[132:135]\n"
"s_lshl_b32 s66, s66, 17\n"
"ds_read_b128 a[96:99], v17 offset:0\n"
"v_mfma_f32_16x16x32_bf16 v[132:135], v[12:15], a[68:71], v[132:135]\n"
"s_or_b32 s66, s66, s81\n"
"ds_read_b128 a[100:103], v17 offset:6144\n"
"v_mfma_f32_16x16x32_bf16 v[140:143], v[8:11], a[72:75], v[140:143]\n"
"s_add_u32 m0, s68, 81920\n"
"ds_read_b128 a[104:107], v17 offset:1024\n"
"v_mfma_f32_16x16x32_bf16 v[140:143], v[12:15], a[76:79], v[140:143]\n"
"buffer_load_dwordx4 v44, s[40:43], s66 offen offset:0 lds\n"
"v_mfma_f32_16x16x32_bf16 v[148:151], v[8:11], a[80:83], v[148:151]\n"
"v_mfma_f32_16x16x32_bf16 v[148:151], v[12:15], a[84:87], v[148:151]\n"
"buffer_load_dwordx4 v45, s[40:43], s66 offen offset:1024 lds\n"
"v_mfma_f32_16x16x32_bf16 v[156:159], v[8:11], a[88:91], v[156:159]\n"
"v_mfma_f32_16x16x32_bf16 v[156:159], v[12:15], a[92:95], v[156:159]\n"
"s_waitcnt lgkmcnt(3)\n"
"v_mfma_f32_16x16x32_bf16 v[160:163], v[0:3], a[48:51], v[160:163]\n"
"ds_read_b64_tr_b16 v[8:9], v21 offset:40960\n"
"v_mfma_f32_16x16x32_bf16 v[160:163], v[4:7], a[52:55], v[160:163]\n"
"ds_read_b64_tr_b16 v[10:11], v21 offset:43008\n"
"v_mfma_f32_16x16x32_bf16 v[168:171], v[0:3], a[56:59], v[168:171]\n"
"ds_read_b64_tr_b16 v[12:13], v21 offset:45056\n"
"v_mfma_f32_16x16x32_bf16 v[168:171], v[4:7], a[60:63], v[168:171]\n"
"ds_read_b64_tr_b16 v[14:15], v21 offset:47104\n"
"v_mfma_f32_16x16x32_bf16 v[176:179], v[0:3], a[64:67], v[176:179]\n"
"ds_read_b128 a[108:111], v17 offset:7168\n"
"v_mfma_f32_16x16x32_bf16 v[176:179], v[4:7], a[68:71], v[176:179]\n"
"ds_read_b128 a[112:115], v17 offset:2048\n"
"v_mfma_f32_16x16x32_bf16 v[184:187], v[0:3], a[72:75], v[184:187]\n"
"ds_read_b128 a[116:119], v17 offset:8192\n"
"v_mfma_f32_16x16x32_bf16 v[184:187], v[4:7], a[76:79], v[184:187]\n"
"buffer_load_dwordx4 v46, s[40:43], s66 offen offset:2048 lds\n"
"v_mfma_f32_16x16x32_bf16 v[192:195], v[0:3], a[80:83], v[192:195]\n"
"v_mfma_f32_16x16x32_bf16 v[192:195], v[4:7], a[84:87], v[192:195]\n"
"buffer_load_dwordx4 v47, s[40:43], s66 offen offset:3072 lds\n"
"v_mfma_f32_16x16x32_bf16 v[200:203], v[0:3], a[88:91], v[200:203]\n"
"v_mfma_f32_16x16x32_bf16 v[200:203], v[4:7], a[92:95], v[200:203]\n"
"s_waitcnt lgkmcnt(3)\n"
"v_mfma_f32_16x16x32_bf16 v[164:167], v[8:11], a[48:51], v[164:167]\n"
"ds_read_b64_tr_b16 v[0:1], v20 offset:41472\n"
"v_mfma_f32_16x16x32_bf16 v[164:167], v[12:15], a[52:55], v[164:167]\n"
"ds_read_b64_tr_b16 v[2:3], v20 offset:43520\n"
"v_mfma_f32_16x16x32_bf16 v[172:175], v[8:11], a[56:59], v[172:175]\n"
"s_cmp_le_u32 s80, 5\n"
"ds_read_b64_tr_b16 v[4:5], v20 offset:45568\n"
"v_mfma_f32_16x16x32_bf16 v[172:175], v[12:15], a[60:63], v[172:175]\n"
"s_cselect_b32 s56, 9, 5\n"
"ds_read_b64_tr_b16 v[6:7], v20 offset:47616\n"
"v_mfma_f32_16x16x32_bf16 v[180:183], v[8:11], a[64:67], v[180:183]\n"
"s_add_u32 s56, s56, s82\n"
"ds_read_b128 a[120:123], v17 offset:3072\n"
"v_mfma_f32_16x16x32_bf16 v[180:183], v[12:15], a[68:71], v[180:183]\n"
"s_mul_i32 s56, s56, 12288\n"
"ds_read_b128 a[124:127], v17 offset:9216\n"
"v_mfma_f32_16x16x32_bf16 v[188:191], v[8:11], a[72:75], v[188:191]\n"
"s_add_u32 s56, s56, 3072\n"
"ds_read_b128 a[128:131], v17 offset:4096\n"
"v_mfma_f32_16x16x32_bf16 v[188:191], v[12:15], a[76:79], v[188:191]\n"
"s_add_u32 m0, s67, 44032\n"
"v_mfma_f32_16x16x32_bf16 v[196:199], v[8:11], a[80:83], v[196:199]\n"
"buffer_load_dwordx4 v56, s[36:39], s56 offen offset:0 sc1 lds\n"
"v_mfma_f32_16x16x32_bf16 v[196:199], v[12:15], a[84:87], v[196:199]\n"
"v_mfma_f32_16x16x32_bf16 v[204:207], v[8:11], a[88:91], v[204:207]\n"
"buffer_load_dwordx4 v56, s[36:39], s56 offen offset:1024 sc1 lds\n"
"v_mfma_f32_16x16x32_bf16 v[204:207], v[12:15], a[92:95], v[204:207]\n"
"s_waitcnt lgkmcnt(3)\n"
"v_mfma_f32_16x16x32_bf16 v[208:211], v[0:3], a[48:51], v[208:211]\n"
"ds_read_b64_tr_b16 v[8:9], v21 offset:41472\n"
"v_mfma_f32_16x16x32_bf16 v[208:211], v[4:7], a[52:55], v[208:211]\n"
"ds_read_b64_tr_b16 v[10:11], v21 offset:43520\n"
"v_mfma_f32_16x16x32_bf16 v[216:219], v[0:3], a[56:59], v[216:219]\n"
"ds_read_b64_tr_b16 v[12:13], v21 offset:45568\n"
"v_mfma_f32_16x16x32_bf16 v[216:219], v[4:7], a[60:63], v[216:219]\n"
"ds_read_b64_tr_b16 v[14:15], v21 offset:47616\n"
"v_mfma_f32_16x16x32_bf16 v[224:227], v[0:3], a[64:67], v[224:227]\n"
"ds_read_b128 a[132:135], v17 offset:10240\n"
"v_mfma_f32_16x16x32_bf16 v[224:227], v[4:7], a[68:71], v[224:227]\n"
"ds_read_b128 a[136:139], v17 offset:5120\n"
"v_mfma_f32_16x16x32_bf16 v[232:235], v[0:3], a[72:75], v[232:235]\n"
"ds_read_b128 a[140:143], v17 offset:11264\n"
"v_mfma_f32_16x16x32_bf16 v[232:235], v[4:7], a[76:79], v[232:235]\n"
"v_mfma_f32_16x16x32_bf16 v[240:243], v[0:3], a[80:83], v[240:243]\n"
"buffer_load_dwordx4 v56, s[36:39], s56 offen offset:2048 sc1 lds\n"
"v_mfma_f32_16x16x32_bf16 v[240:243], v[4:7], a[84:87], v[240:243]\n"
"v_mfma_f32_16x16x32_bf16 v[248:251], v[0:3], a[88:91], v[248:251]\n"
"v_mfma_f32_16x16x32_bf16 v[248:251], v[4:7], a[92:95], v[248:251]\n"
"s_waitcnt vmcnt(17) lgkmcnt(0)\n"
"s_barrier\n"
"v_mfma_f32_16x16x32_bf16 v[212:215], v[8:11], a[48:51], v[212:215]\n"
"s_cmp_le_u32 s80, 3\n"
"ds_read_b64_tr_b16 v[0:1], v18 offset:0\n"
"v_mfma_f32_16x16x32_bf16 v[212:215], v[12:15], a[52:55], v[212:215]\n"
"s_cselect_b32 s66, 7, 3\n"
"ds_read_b64_tr_b16 v[2:3], v18 offset:2048\n"
"v_mfma_f32_16x16x32_bf16 v[220:223], v[8:11], a[56:59], v[220:223]\n"
"s_add_u32 s66, s66, s82\n"
"ds_read_b64_tr_b16 v[4:5], v18 offset:4096\n"
"v_mfma_f32_16x16x32_bf16 v[220:223], v[12:15], a[60:63], v[220:223]\n"
"s_lshl_b32 s66, s66, 17\n"
"ds_read_b64_tr_b16 v[6:7], v18 offset:6144\n"
"v_mfma_f32_16x16x32_bf16 v[228:231], v[8:11], a[64:67], v[228:231]\n"
"s_or_b32 s66, s66, s81\n"
"v_mfma_f32_16x16x32_bf16 v[228:231], v[12:15], a[68:71], v[228:231]\n"
"s_xor_b32 s66, s66, 128\n"
"v_mfma_f32_16x16x32_bf16 v[236:239], v[8:11], a[72:75], v[236:239]\n"
"s_add_u32 m0, s68, 122880\n"
"v_mfma_f32_16x16x32_bf16 v[236:239], v[12:15], a[76:79], v[236:239]\n"
"buffer_load_dwordx4 v44, s[40:43], s66 offen offset:0 lds\n"
"v_mfma_f32_16x16x32_bf16 v[244:247], v[8:11], a[80:83], v[244:247]\n"
"v_mfma_f32_16x16x32_bf16 v[244:247], v[12:15], a[84:87], v[244:247]\n"
"buffer_load_dwordx4 v45, s[40:43], s66 offen offset:1024 lds\n"
"v_mfma_f32_16x16x32_bf16 v[252:255], v[8:11], a[88:91], v[252:255]\n"
"v_mfma_f32_16x16x32_bf16 v[252:255], v[12:15], a[92:95], v[252:255]\n"
"s_waitcnt lgkmcnt(0)\n"
"v_mfma_f32_16x16x32_bf16 v[64:67], v[0:3], a[96:99], v[64:67]\n"
"ds_read_b64_tr_b16 v[8:9], v19 offset:0\n"
"v_mfma_f32_16x16x32_bf16 v[64:67], v[4:7], a[100:103], v[64:67]\n"
"ds_read_b64_tr_b16 v[10:11], v19 offset:2048\n"
"v_mfma_f32_16x16x32_bf16 v[72:75], v[0:3], a[104:107], v[72:75]\n"
"ds_read_b64_tr_b16 v[12:13], v19 offset:4096\n"
"v_mfma_f32_16x16x32_bf16 v[72:75], v[4:7], a[108:111], v[72:75]\n"
"ds_read_b64_tr_b16 v[14:15], v19 offset:6144\n"
"v_mfma_f32_16x16x32_bf16 v[80:83], v[0:3], a[112:115], v[80:83]\n"
"v_mfma_f32_16x16x32_bf16 v[80:83], v[4:7], a[116:119], v[80:83]\n"
"v_mfma_f32_16x16x32_bf16 v[88:91], v[0:3], a[120:123], v[88:91]\n"
"v_mfma_f32_16x16x32_bf16 v[88:91], v[4:7], a[124:127], v[88:91]\n"
"buffer_load_dwordx4 v46, s[40:43], s66 offen offset:2048 lds\n"
"v_mfma_f32_16x16x32_bf16 v[96:99], v[0:3], a[128:131], v[96:99]\n"
"v_mfma_f32_16x16x32_bf16 v[96:99], v[4:7], a[132:135], v[96:99]\n"
"buffer_load_dwordx4 v47, s[40:43], s66 offen offset:3072 lds\n"
"v_mfma_f32_16x16x32_bf16 v[104:107], v[0:3], a[136:139], v[104:107]\n"
"v_mfma_f32_16x16x32_bf16 v[104:107], v[4:7], a[140:143], v[104:107]\n"
"s_waitcnt lgkmcnt(0)\n"
"v_mfma_f32_16x16x32_bf16 v[68:71], v[8:11], a[96:99], v[68:71]\n"
"ds_read_b64_tr_b16 v[0:1], v18 offset:512\n"
"v_mfma_f32_16x16x32_bf16 v[68:71], v[12:15], a[100:103], v[68:71]\n"
"ds_read_b64_tr_b16 v[2:3], v18 offset:2560\n"
"v_mfma_f32_16x16x32_bf16 v[76:79], v[8:11], a[104:107], v[76:79]\n"
"s_cmp_le_u32 s80, 6\n"
"ds_read_b64_tr_b16 v[4:5], v18 offset:4608\n"
"v_mfma_f32_16x16x32_bf16 v[76:79], v[12:15], a[108:111], v[76:79]\n"
"s_cselect_b32 s56, 10, 6\n"
"ds_read_b64_tr_b16 v[6:7], v18 offset:6656\n"
"v_mfma_f32_16x16x32_bf16 v[84:87], v[8:11], a[112:115], v[84:87]\n"
"s_add_u32 s56, s56, s82\n"
"v_mfma_f32_16x16x32_bf16 v[84:87], v[12:15], a[116:119], v[84:87]\n"
"s_mul_i32 s56, s56, 12288\n"
"v_mfma_f32_16x16x32_bf16 v[92:95], v[8:11], a[120:123], v[92:95]\n"
"s_add_u32 m0, s67, 81920\n"
"v_mfma_f32_16x16x32_bf16 v[92:95], v[12:15], a[124:127], v[92:95]\n"
"buffer_load_dwordx4 v56, s[36:39], s56 offen offset:0 sc1 lds\n"
"v_mfma_f32_16x16x32_bf16 v[100:103], v[8:11], a[128:131], v[100:103]\n"
"v_mfma_f32_16x16x32_bf16 v[100:103], v[12:15], a[132:135], v[100:103]\n"
"buffer_load_dwordx4 v56, s[36:39], s56 offen offset:1024 sc1 lds\n"
"v_mfma_f32_16x16x32_bf16 v[108:111], v[8:11], a[136:139], v[108:111]\n"
"v_mfma_f32_16x16x32_bf16 v[108:111], v[12:15], a[140:143], v[108:111]\n"
"s_waitcnt lgkmcnt(0)\n"
"v_mfma_f32_16x16x32_bf16 v[112:115], v[0:3], a[96:99], v[112:115]\n"
"ds_read_b64_tr_b16 v[8:9], v19 offset:512\n"
"v_mfma_f32_16x16x32_bf16 v[112:115], v[4:7], a[100:103], v[112:115]\n"
"ds_read_b64_tr_b16 v[10:11], v19 offset:2560\n"
"v_mfma_f32_16x16x32_bf16 v[120:123], v[0:3], a[104:107], v[120:123]\n"
"ds_read_b64_tr_b16 v[12:13], v19 offset:4608\n"
"v_mfma_f32_16x16x32_bf16 v[120:123], v[4:7], a[108:111], v[120:123]\n"
"ds_read_b64_tr_b16 v[14:15], v19 offset:6656\n"
"v_mfma_f32_16x16x32_bf16 v[128:131], v[0:3], a[112:115], v[128:131]\n"
"v_mfma_f32_16x16x32_bf16 v[128:131], v[4:7], a[116:119], v[128:131]\n"
"v_mfma_f32_16x16x32_bf16 v[136:139], v[0:3], a[120:123], v[136:139]\n"
"v_mfma_f32_16x16x32_bf16 v[136:139], v[4:7], a[124:127], v[136:139]\n"
"buffer_load_dwordx4 v56, s[36:39], s56 offen offset:2048 sc1 lds\n"
"v_mfma_f32_16x16x32_bf16 v[144:147], v[0:3], a[128:131], v[144:147]\n"
"v_mfma_f32_16x16x32_bf16 v[144:147], v[4:7], a[132:135], v[144:147]\n"
"s_lshl_b32 s93, s52, 7\n"
"v_lshl_add_u32 v36, v57, 9, s93\n"
"v_min_u32 v36, P2_GENX-P2_C-4, v36\n"
"v_add_u32 v36, P2_C-K1_PC, v36\n"
"global_load_dword v38, v36, s[72:73]\n"
"v_mfma_f32_16x16x32_bf16 v[152:155], v[0:3], a[136:139], v[152:155]\n"
"v_mfma_f32_16x16x32_bf16 v[152:155], v[4:7], a[140:143], v[152:155]\n"
"s_waitcnt vmcnt(29)\n"
"s_waitcnt vmcnt(18) lgkmcnt(0)\n"
"s_barrier\n"
"v_mfma_f32_16x16x32_bf16 v[116:119], v[8:11], a[96:99], v[116:119]\n"
"ds_read_b64_tr_b16 v[0:1], v18 offset:40960\n"
"v_mfma_f32_16x16x32_bf16 v[116:119], v[12:15], a[100:103], v[116:119]\n"
"s_cmp_le_u32 s80, 4\n"
"ds_read_b64_tr_b16 v[2:3], v18 offset:43008\n"
"v_mfma_f32_16x16x32_bf16 v[124:127], v[8:11], a[104:107], v[124:127]\n"
"s_cselect_b32 s66, 8, 4\n"
"ds_read_b64_tr_b16 v[4:5], v18 offset:45056\n"
"v_mfma_f32_16x16x32_bf16 v[124:127], v[12:15], a[108:111], v[124:127]\n"
"s_add_u32 s66, s66, s82\n"
"ds_read_b64_tr_b16 v[6:7], v18 offset:47104\n"
"v_mfma_f32_16x16x32_bf16 v[132:135], v[8:11], a[112:115], v[132:135]\n"
"s_lshl_b32 s66, s66, 17\n"
"ds_read_b128 a[144:147], v17 offset:40960\n"
"v_mfma_f32_16x16x32_bf16 v[132:135], v[12:15], a[116:119], v[132:135]\n"
"s_or_b32 s66, s66, s81\n"
"ds_read_b128 a[148:151], v17 offset:47104\n"
"v_mfma_f32_16x16x32_bf16 v[140:143], v[8:11], a[120:123], v[140:143]\n"
"s_add_u32 m0, s68, 0\n"
"ds_read_b128 a[152:155], v17 offset:41984\n"
"v_mfma_f32_16x16x32_bf16 v[140:143], v[12:15], a[124:127], v[140:143]\n"
"buffer_load_dwordx4 v44, s[40:43], s66 offen offset:0 lds\n"
"v_mfma_f32_16x16x32_bf16 v[148:151], v[8:11], a[128:131], v[148:151]\n"
"v_mfma_f32_16x16x32_bf16 v[148:151], v[12:15], a[132:135], v[148:151]\n"
"buffer_load_dwordx4 v45, s[40:43], s66 offen offset:1024 lds\n"
"v_mfma_f32_16x16x32_bf16 v[156:159], v[8:11], a[136:139], v[156:159]\n"
"v_mfma_f32_16x16x32_bf16 v[156:159], v[12:15], a[140:143], v[156:159]\n"
"s_waitcnt lgkmcnt(3)\n"
"v_mfma_f32_16x16x32_bf16 v[160:163], v[0:3], a[96:99], v[160:163]\n"
"ds_read_b64_tr_b16 v[8:9], v19 offset:40960\n"
"v_mfma_f32_16x16x32_bf16 v[160:163], v[4:7], a[100:103], v[160:163]\n"
"ds_read_b64_tr_b16 v[10:11], v19 offset:43008\n"
"v_mfma_f32_16x16x32_bf16 v[168:171], v[0:3], a[104:107], v[168:171]\n"
"ds_read_b64_tr_b16 v[12:13], v19 offset:45056\n"
"v_mfma_f32_16x16x32_bf16 v[168:171], v[4:7], a[108:111], v[168:171]\n"
"ds_read_b64_tr_b16 v[14:15], v19 offset:47104\n"
"v_mfma_f32_16x16x32_bf16 v[176:179], v[0:3], a[112:115], v[176:179]\n"
"ds_read_b128 a[156:159], v17 offset:48128\n"
"v_mfma_f32_16x16x32_bf16 v[176:179], v[4:7], a[116:119], v[176:179]\n"
"ds_read_b128 a[160:163], v17 offset:43008\n"
"v_mfma_f32_16x16x32_bf16 v[184:187], v[0:3], a[120:123], v[184:187]\n"
"ds_read_b128 a[164:167], v17 offset:49152\n"
"v_mfma_f32_16x16x32_bf16 v[184:187], v[4:7], a[124:127], v[184:187]\n"
"buffer_load_dwordx4 v46, s[40:43], s66 offen offset:2048 lds\n"
"v_mfma_f32_16x16x32_bf16 v[192:195], v[0:3], a[128:131], v[192:195]\n"
"v_mfma_f32_16x16x32_bf16 v[192:195], v[4:7], a[132:135], v[192:195]\n"
"buffer_load_dwordx4 v47, s[40:43], s66 offen offset:3072 lds\n"
"v_mfma_f32_16x16x32_bf16 v[200:203], v[0:3], a[136:139], v[200:203]\n"
"v_mfma_f32_16x16x32_bf16 v[200:203], v[4:7], a[140:143], v[200:203]\n"
"s_waitcnt lgkmcnt(3)\n"
"v_mfma_f32_16x16x32_bf16 v[164:167], v[8:11], a[96:99], v[164:167]\n"
"ds_read_b64_tr_b16 v[0:1], v18 offset:41472\n"
"v_mfma_f32_16x16x32_bf16 v[164:167], v[12:15], a[100:103], v[164:167]\n"
"ds_read_b64_tr_b16 v[2:3], v18 offset:43520\n"
"v_mfma_f32_16x16x32_bf16 v[172:175], v[8:11], a[104:107], v[172:175]\n"
"s_cmp_le_u32 s80, 6\n"
"ds_read_b64_tr_b16 v[4:5], v18 offset:45568\n"
"v_mfma_f32_16x16x32_bf16 v[172:175], v[12:15], a[108:111], v[172:175]\n"
"s_cselect_b32 s56, 10, 6\n"
"ds_read_b64_tr_b16 v[6:7], v18 offset:47616\n"
"v_mfma_f32_16x16x32_bf16 v[180:183], v[8:11], a[112:115], v[180:183]\n"
"s_add_u32 s56, s56, s82\n"
"ds_read_b128 a[168:171], v17 offset:44032\n"
"v_mfma_f32_16x16x32_bf16 v[180:183], v[12:15], a[116:119], v[180:183]\n"
"s_mul_i32 s56, s56, 12288\n"
"ds_read_b128 a[172:175], v17 offset:50176\n"
"v_mfma_f32_16x16x32_bf16 v[188:191], v[8:11], a[120:123], v[188:191]\n"
"s_add_u32 s56, s56, 3072\n"
"ds_read_b128 a[176:179], v17 offset:45056\n"
"v_mfma_f32_16x16x32_bf16 v[188:191], v[12:15], a[124:127], v[188:191]\n"
"s_add_u32 m0, s67, 84992\n"
"v_mfma_f32_16x16x32_bf16 v[196:199], v[8:11], a[128:131], v[196:199]\n"
"buffer_load_dwordx4 v56, s[36:39], s56 offen offset:0 sc1 lds\n"
"v_mfma_f32_16x16x32_bf16 v[196:199], v[12:15], a[132:135], v[196:199]\n"
"v_mfma_f32_16x16x32_bf16 v[204:207], v[8:11], a[136:139], v[204:207]\n"
"buffer_load_dwordx4 v56, s[36:39], s56 offen offset:1024 sc1 lds\n"
"v_mfma_f32_16x16x32_bf16 v[204:207], v[12:15], a[140:143], v[204:207]\n"
"s_waitcnt lgkmcnt(3)\n"
"v_mfma_f32_16x16x32_bf16 v[208:211], v[0:3], a[96:99], v[208:211]\n"
"ds_read_b64_tr_b16 v[8:9], v19 offset:41472\n"
"v_mfma_f32_16x16x32_bf16 v[208:211], v[4:7], a[100:103], v[208:211]\n"
"ds_read_b64_tr_b16 v[10:11], v19 offset:43520\n"
"v_mfma_f32_16x16x32_bf16 v[216:219], v[0:3], a[104:107], v[216:219]\n"
"ds_read_b64_tr_b16 v[12:13], v19 offset:45568\n"
"v_mfma_f32_16x16x32_bf16 v[216:219], v[4:7], a[108:111], v[216:219]\n"
"ds_read_b64_tr_b16 v[14:15], v19 offset:47616\n"
"v_mfma_f32_16x16x32_bf16 v[224:227], v[0:3], a[112:115], v[224:227]\n"
"ds_read_b128 a[180:183], v17 offset:51200\n"
"v_mfma_f32_16x16x32_bf16 v[224:227], v[4:7], a[116:119], v[224:227]\n"
"ds_read_b128 a[184:187], v17 offset:46080\n"
"v_mfma_f32_16x16x32_bf16 v[232:235], v[0:3], a[120:123], v[232:235]\n"
"ds_read_b128 a[188:191], v17 offset:52224\n"
"v_mfma_f32_16x16x32_bf16 v[232:235], v[4:7], a[124:127], v[232:235]\n"
"v_mfma_f32_16x16x32_bf16 v[240:243], v[0:3], a[128:131], v[240:243]\n"
"buffer_load_dwordx4 v56, s[36:39], s56 offen offset:2048 sc1 lds\n"
"v_mfma_f32_16x16x32_bf16 v[240:243], v[4:7], a[132:135], v[240:243]\n"
"v_mfma_f32_16x16x32_bf16 v[248:251], v[0:3], a[136:139], v[248:251]\n"
"v_mfma_f32_16x16x32_bf16 v[248:251], v[4:7], a[140:143], v[248:251]\n"
"s_waitcnt vmcnt(18) lgkmcnt(0)\n"
"s_barrier\n"
"v_mfma_f32_16x16x32_bf16 v[212:215], v[8:11], a[96:99], v[212:215]\n"
"ds_read_b64_tr_b16 v[0:1], v20 offset:0\n"
"v_mfma_f32_16x16x32_bf16 v[212:215], v[12:15], a[100:103], v[212:215]\n"
"s_cmp_le_u32 s80, 5\n"
"ds_read_b64_tr_b16 v[2:3], v20 offset:2048\n"
"v_mfma_f32_16x16x32_bf16 v[220:223], v[8:11], a[104:107], v[220:223]\n"
"s_cselect_b32 s66, 9, 5\n"
"ds_read_b64_tr_b16 v[4:5], v20 offset:4096\n"
"v_mfma_f32_16x16x32_bf16 v[220:223], v[12:15], a[108:111], v[220:223]\n"
"s_add_u32 s66, s66, s82\n"
"ds_read_b64_tr_b16 v[6:7], v20 offset:6144\n"
"v_mfma_f32_16x16x32_bf16 v[228:231], v[8:11], a[112:115], v[228:231]\n"
"s_lshl_b32 s66, s66, 17\n"
"v_mfma_f32_16x16x32_bf16 v[228:231], v[12:15], a[116:119], v[228:231]\n"
"s_or_b32 s66, s66, s81\n"
"v_mfma_f32_16x16x32_bf16 v[236:239], v[8:11], a[120:123], v[236:239]\n"
"s_add_u32 m0, s68, 40960\n"
"v_mfma_f32_16x16x32_bf16 v[236:239], v[12:15], a[124:127], v[236:239]\n"
"buffer_load_dwordx4 v44, s[40:43], s66 offen offset:0 lds\n"
"v_mfma_f32_16x16x32_bf16 v[244:247], v[8:11], a[128:131], v[244:247]\n"
"v_mfma_f32_16x16x32_bf16 v[244:247], v[12:15], a[132:135], v[244:247]\n"
"buffer_load_dwordx4 v45, s[40:43], s66 offen offset:1024 lds\n"
"v_mfma_f32_16x16x32_bf16 v[252:255], v[8:11], a[136:139], v[252:255]\n"
"v_mfma_f32_16x16x32_bf16 v[252:255], v[12:15], a[140:143], v[252:255]\n"
"s_waitcnt lgkmcnt(0)\n"
"v_mfma_f32_16x16x32_bf16 v[64:67], v[0:3], a[144:147], v[64:67]\n"
"ds_read_b64_tr_b16 v[8:9], v21 offset:0\n"
"v_mfma_f32_16x16x32_bf16 v[64:67], v[4:7], a[148:151], v[64:67]\n"
"ds_read_b64_tr_b16 v[10:11], v21 offset:2048\n"
"v_mfma_f32_16x16x32_bf16 v[72:75], v[0:3], a[152:155], v[72:75]\n"
"ds_read_b64_tr_b16 v[12:13], v21 offset:4096\n"
"v_mfma_f32_16x16x32_bf16 v[72:75], v[4:7], a[156:159], v[72:75]\n"
"ds_read_b64_tr_b16 v[14:15], v21 offset:6144\n"
"v_mfma_f32_16x16x32_bf16 v[80:83], v[0:3], a[160:163], v[80:83]\n"
"v_mfma_f32_16x16x32_bf16 v[80:83], v[4:7], a[164:167], v[80:83]\n"
"v_mfma_f32_16x16x32_bf16 v[88:91], v[0:3], a[168:171], v[88:91]\n"
"v_mfma_f32_16x16x32_bf16 v[88:91], v[4:7], a[172:175], v[88:91]\n"
"buffer_load_dwordx4 v46, s[40:43], s66 offen offset:2048 lds\n"
"v_mfma_f32_16x16x32_bf16 v[96:99], v[0:3], a[176:179], v[96:99]\n"
"v_mfma_f32_16x16x32_bf16 v[96:99], v[4:7], a[180:183], v[96:99]\n"
"buffer_load_dwordx4 v47, s[40:43], s66 offen offset:3072 lds\n"
"v_mfma_f32_16x16x32_bf16 v[104:107], v[0:3], a[184:187], v[104:107]\n"
"v_mfma_f32_16x16x32_bf16 v[104:107], v[4:7], a[188:191], v[104:107]\n"
"s_waitcnt lgkmcnt(0)\n"
"v_mfma_f32_16x16x32_bf16 v[68:71], v[8:11], a[144:147], v[68:71]\n"
"ds_read_b64_tr_b16 v[0:1], v20 offset:512\n"
"v_mfma_f32_16x16x32_bf16 v[68:71], v[12:15], a[148:151], v[68:71]\n"
"ds_read_b64_tr_b16 v[2:3], v20 offset:2560\n"
"v_mfma_f32_16x16x32_bf16 v[76:79], v[8:11], a[152:155], v[76:79]\n"
"ds_read_b64_tr_b16 v[4:5], v20 offset:4608\n"
"v_mfma_f32_16x16x32_bf16 v[76:79], v[12:15], a[156:159], v[76:79]\n"
"s_cmp_le_u32 s80, 7\n"
"ds_read_b64_tr_b16 v[6:7], v20 offset:6656\n"
"v_mfma_f32_16x16x32_bf16 v[84:87], v[8:11], a[160:163], v[84:87]\n"
"s_cselect_b32 s56, 11, 7\n"
"v_mfma_f32_16x16x32_bf16 v[84:87], v[12:15], a[164:167], v[84:87]\n"
"s_add_u32 s56, s56, s82\n"
"v_mfma_f32_16x16x32_bf16 v[92:95], v[8:11], a[168:171], v[92:95]\n"
"s_mul_i32 s56, s56, 12288\n"
"v_mfma_f32_16x16x32_bf16 v[92:95], v[12:15], a[172:175], v[92:95]\n"
"s_add_u32 m0, s67, 122880\n"
"v_mfma_f32_16x16x32_bf16 v[100:103], v[8:11], a[176:179], v[100:103]\n"
"buffer_load_dwordx4 v56, s[36:39], s56 offen offset:0 sc1 lds\n"
"v_mfma_f32_16x16x32_bf16 v[100:103], v[12:15], a[180:183], v[100:103]\n"
"v_mfma_f32_16x16x32_bf16 v[108:111], v[8:11], a[184:187], v[108:111]\n"
"buffer_load_dwordx4 v56, s[36:39], s56 offen offset:1024 sc1 lds\n"
"v_mfma_f32_16x16x32_bf16 v[108:111], v[12:15], a[188:191], v[108:111]\n"
"s_waitcnt lgkmcnt(0)\n"
"v_mfma_f32_16x16x32_bf16 v[112:115], v[0:3], a[144:147], v[112:115]\n"
"ds_read_b64_tr_b16 v[8:9], v21 offset:512\n"
"v_mfma_f32_16x16x32_bf16 v[112:115], v[4:7], a[148:151], v[112:115]\n"
"ds_read_b64_tr_b16 v[10:11], v21 offset:2560\n"
"v_mfma_f32_16x16x32_bf16 v[120:123], v[0:3], a[152:155], v[120:123]\n"
"ds_read_b64_tr_b16 v[12:13], v21 offset:4608\n"
"v_mfma_f32_16x16x32_bf16 v[120:123], v[4:7], a[156:159], v[120:123]\n"
"ds_read_b64_tr_b16 v[14:15], v21 offset:6656\n"
"v_mfma_f32_16x16x32_bf16 v[128:131], v[0:3], a[160:163], v[128:131]\n"
"v_mfma_f32_16x16x32_bf16 v[128:131], v[4:7], a[164:167], v[128:131]\n"
"v_mfma_f32_16x16x32_bf16 v[136:139], v[0:3], a[168:171], v[136:139]\n"
"v_mfma_f32_16x16x32_bf16 v[136:139], v[4:7], a[172:175], v[136:139]\n"
"v_mfma_f32_16x16x32_bf16 v[144:147], v[0:3], a[176:179], v[144:147]\n"
"buffer_load_dwordx4 v56, s[36:39], s56 offen offset:2048 sc1 lds\n"
"v_mfma_f32_16x16x32_bf16 v[144:147], v[4:7], a[180:183], v[144:147]\n"
"v_mfma_f32_16x16x32_bf16 v[152:155], v[0:3], a[184:187], v[152:155]\n"
"v_mfma_f32_16x16x32_bf16 v[152:155], v[4:7], a[188:191], v[152:155]\n"
"s_waitcnt vmcnt(36)\n"
"s_waitcnt vmcnt(18) lgkmcnt(0)\n"
"s_barrier\n"
"v_mfma_f32_16x16x32_bf16 v[116:119], v[8:11], a[144:147], v[116:119]\n"
"ds_read_b64_tr_b16 v[0:1], v20 offset:40960\n"
"v_mfma_f32_16x16x32_bf16 v[116:119], v[12:15], a[148:151], v[116:119]\n"
"s_cmp_le_u32 s80, 6\n"
"ds_read_b64_tr_b16 v[2:3], v20 offset:43008\n"
"v_mfma_f32_16x16x32_bf16 v[124:127], v[8:11], a[152:155], v[124:127]\n"
"s_cselect_b32 s66, 10, 6\n"
"ds_read_b64_tr_b16 v[4:5], v20 offset:45056\n"
"v_mfma_f32_16x16x32_bf16 v[124:127], v[12:15], a[156:159], v[124:127]\n"
"s_add_u32 s66, s66, s82\n"
"ds_read_b64_tr_b16 v[6:7], v20 offset:47104\n"
"v_mfma_f32_16x16x32_bf16 v[132:135], v[8:11], a[160:163], v[132:135]\n"
"s_lshl_b32 s66, s66, 17\n"
"ds_read_b128 a[0:3], v16 offset:0\n"
"v_mfma_f32_16x16x32_bf16 v[132:135], v[12:15], a[164:167], v[132:135]\n"
"s_or_b32 s66, s66, s81\n"
"ds_read_b128 a[4:7], v16 offset:6144\n"
"v_mfma_f32_16x16x32_bf16 v[140:143], v[8:11], a[168:171], v[140:143]\n"
"s_add_u32 m0, s68, 81920\n"
"ds_read_b128 a[8:11], v16 offset:1024\n"
"v_mfma_f32_16x16x32_bf16 v[140:143], v[12:15], a[172:175], v[140:143]\n"
"buffer_load_dwordx4 v44, s[40:43], s66 offen offset:0 lds\n"
"v_mfma_f32_16x16x32_bf16 v[148:151], v[8:11], a[176:179], v[148:151]\n"
"v_mfma_f32_16x16x32_bf16 v[148:151], v[12:15], a[180:183], v[148:151]\n"
"buffer_load_dwordx4 v45, s[40:43], s66 offen offset:1024 lds\n"
"v_mfma_f32_16x16x32_bf16 v[156:159], v[8:11], a[184:187], v[156:159]\n"
"v_mfma_f32_16x16x32_bf16 v[156:159], v[12:15], a[188:191], v[156:159]\n"
"s_waitcnt lgkmcnt(3)\n"
"v_mfma_f32_16x16x32_bf16 v[160:163], v[0:3], a[144:147], v[160:163]\n"
"ds_read_b64_tr_b16 v[8:9], v21 offset:40960\n"
"v_mfma_f32_16x16x32_bf16 v[160:163], v[4:7], a[148:151], v[160:163]\n"
"ds_read_b64_tr_b16 v[10:11], v21 offset:43008\n"
"v_mfma_f32_16x16x32_bf16 v[168:171], v[0:3], a[152:155], v[168:171]\n"
"ds_read_b64_tr_b16 v[12:13], v21 offset:45056\n"
"v_mfma_f32_16x16x32_bf16 v[168:171], v[4:7], a[156:159], v[168:171]\n"
"ds_read_b64_tr_b16 v[14:15], v21 offset:47104\n"
"v_mfma_f32_16x16x32_bf16 v[176:179], v[0:3], a[160:163], v[176:179]\n"
"ds_read_b128 a[12:15], v16 offset:7168\n"
"v_mfma_f32_16x16x32_bf16 v[176:179], v[4:7], a[164:167], v[176:179]\n"
"ds_read_b128 a[16:19], v16 offset:2048\n"
"v_mfma_f32_16x16x32_bf16 v[184:187], v[0:3], a[168:171], v[184:187]\n"
"ds_read_b128 a[20:23], v16 offset:8192\n"
"v_mfma_f32_16x16x32_bf16 v[184:187], v[4:7], a[172:175], v[184:187]\n"
"buffer_load_dwordx4 v46, s[40:43], s66 offen offset:2048 lds\n"
"v_mfma_f32_16x16x32_bf16 v[192:195], v[0:3], a[176:179], v[192:195]\n"
"v_mfma_f32_16x16x32_bf16 v[192:195], v[4:7], a[180:183], v[192:195]\n"
"buffer_load_dwordx4 v47, s[40:43], s66 offen offset:3072 lds\n"
"v_mfma_f32_16x16x32_bf16 v[200:203], v[0:3], a[184:187], v[200:203]\n"
"v_mfma_f32_16x16x32_bf16 v[200:203], v[4:7], a[188:191], v[200:203]\n"
"s_waitcnt lgkmcnt(3)\n"
"v_mfma_f32_16x16x32_bf16 v[164:167], v[8:11], a[144:147], v[164:167]\n"
"ds_read_b64_tr_b16 v[0:1], v20 offset:41472\n"
"v_mfma_f32_16x16x32_bf16 v[164:167], v[12:15], a[148:151], v[164:167]\n"
"ds_read_b64_tr_b16 v[2:3], v20 offset:43520\n"
"v_mfma_f32_16x16x32_bf16 v[172:175], v[8:11], a[152:155], v[172:175]\n"
"s_cmp_le_u32 s80, 7\n"
"ds_read_b64_tr_b16 v[4:5], v20 offset:45568\n"
"v_mfma_f32_16x16x32_bf16 v[172:175], v[12:15], a[156:159], v[172:175]\n"
"s_cselect_b32 s56, 11, 7\n"
"ds_read_b64_tr_b16 v[6:7], v20 offset:47616\n"
"v_mfma_f32_16x16x32_bf16 v[180:183], v[8:11], a[160:163], v[180:183]\n"
"s_add_u32 s56, s56, s82\n"
"ds_read_b128 a[24:27], v16 offset:3072\n"
"v_mfma_f32_16x16x32_bf16 v[180:183], v[12:15], a[164:167], v[180:183]\n"
"s_mul_i32 s56, s56, 12288\n"
"ds_read_b128 a[28:31], v16 offset:9216\n"
"v_mfma_f32_16x16x32_bf16 v[188:191], v[8:11], a[168:171], v[188:191]\n"
"s_add_u32 s56, s56, 3072\n"
"ds_read_b128 a[32:35], v16 offset:4096\n"
"v_mfma_f32_16x16x32_bf16 v[188:191], v[12:15], a[172:175], v[188:191]\n"
"s_add_u32 m0, s67, 125952\n"
"v_mfma_f32_16x16x32_bf16 v[196:199], v[8:11], a[176:179], v[196:199]\n"
"buffer_load_dwordx4 v56, s[36:39], s56 offen offset:0 sc1 lds\n"
"v_mfma_f32_16x16x32_bf16 v[196:199], v[12:15], a[180:183], v[196:199]\n"
"v_mfma_f32_16x16x32_bf16 v[204:207], v[8:11], a[184:187], v[204:207]\n"
"buffer_load_dwordx4 v56, s[36:39], s56 offen offset:1024 sc1 lds\n"
"v_mfma_f32_16x16x32_bf16 v[204:207], v[12:15], a[188:191], v[204:207]\n"
"s_waitcnt lgkmcnt(3)\n"
"v_mfma_f32_16x16x32_bf16 v[208:211], v[0:3], a[144:147], v[208:211]\n"
"ds_read_b64_tr_b16 v[8:9], v21 offset:41472\n"
"v_mfma_f32_16x16x32_bf16 v[208:211], v[4:7], a[148:151], v[208:211]\n"
"ds_read_b64_tr_b16 v[10:11], v21 offset:43520\n"
"v_mfma_f32_16x16x32_bf16 v[216:219], v[0:3], a[152:155], v[216:219]\n"
"ds_read_b64_tr_b16 v[12:13], v21 offset:45568\n"
"v_mfma_f32_16x16x32_bf16 v[216:219], v[4:7], a[156:159], v[216:219]\n"
"ds_read_b64_tr_b16 v[14:15], v21 offset:47616\n"
"v_mfma_f32_16x16x32_bf16 v[224:227], v[0:3], a[160:163], v[224:227]\n"
"ds_read_b128 a[36:39], v16 offset:10240\n"
"v_mfma_f32_16x16x32_bf16 v[224:227], v[4:7], a[164:167], v[224:227]\n"
"ds_read_b128 a[40:43], v16 offset:5120\n"
"v_mfma_f32_16x16x32_bf16 v[232:235], v[0:3], a[168:171], v[232:235]\n"
"ds_read_b128 a[44:47], v16 offset:11264\n"
"v_mfma_f32_16x16x32_bf16 v[232:235], v[4:7], a[172:175], v[232:235]\n"
"v_mfma_f32_16x16x32_bf16 v[240:243], v[0:3], a[176:179], v[240:243]\n"
"buffer_load_dwordx4 v56, s[36:39], s56 offen offset:2048 sc1 lds\n"
"v_mfma_f32_16x16x32_bf16 v[240:243], v[4:7], a[180:183], v[240:243]\n"
"v_mfma_f32_16x16x32_bf16 v[248:251], v[0:3], a[184:187], v[248:251]\n"
"v_mfma_f32_16x16x32_bf16 v[248:251], v[4:7], a[188:191], v[248:251]\n"
"s_waitcnt vmcnt(29)\n"
"s_waitcnt vmcnt(17) lgkmcnt(0)\n"
"s_barrier\n"
"v_mfma_f32_16x16x32_bf16 v[212:215], v[8:11], a[144:147], v[212:215]\n"
"ds_read_b64_tr_b16 v[0:1], v18 offset:0\n"
"v_mfma_f32_16x16x32_bf16 v[212:215], v[12:15], a[148:151], v[212:215]\n"
"ds_read_b64_tr_b16 v[2:3], v18 offset:2048\n"
"v_mfma_f32_16x16x32_bf16 v[220:223], v[8:11], a[152:155], v[220:223]\n"
"s_cmp_le_u32 s80, 8\n"
"ds_read_b64_tr_b16 v[4:5], v18 offset:4096\n"
"v_mfma_f32_16x16x32_bf16 v[220:223], v[12:15], a[156:159], v[220:223]\n"
"s_cselect_b32 s56, 12, 8\n"
"ds_read_b64_tr_b16 v[6:7], v18 offset:6144\n"
"v_mfma_f32_16x16x32_bf16 v[228:231], v[8:11], a[160:163], v[228:231]\n"
"s_add_u32 s56, s56, s82\n"
"ds_read_b128 a[48:51], v16 offset:40960\n"
"v_mfma_f32_16x16x32_bf16 v[228:231], v[12:15], a[164:167], v[228:231]\n"
"s_mul_i32 s56, s56, 12288\n"
"ds_read_b128 a[52:55], v16 offset:47104\n"
"v_mfma_f32_16x16x32_bf16 v[236:239], v[8:11], a[168:171], v[236:239]\n"
"s_add_u32 m0, s67, 0\n"
"ds_read_b128 a[56:59], v16 offset:41984\n"
"v_mfma_f32_16x16x32_bf16 v[236:239], v[12:15], a[172:175], v[236:239]\n"
"buffer_load_dwordx4 v56, s[36:39], s56 offen offset:0 sc1 lds\n"
"v_mfma_f32_16x16x32_bf16 v[244:247], v[8:11], a[176:179], v[244:247]\n"
"buffer_load_dwordx4 v56, s[36:39], s56 offen offset:1024 sc1 lds\n"
"v_mfma_f32_16x16x32_bf16 v[244:247], v[12:15], a[180:183], v[244:247]\n"
"v_mfma_f32_16x16x32_bf16 v[252:255], v[8:11], a[184:187], v[252:255]\n"
"buffer_load_dwordx4 v56, s[36:39], s56 offen offset:2048 sc1 lds\n"
"v_mfma_f32_16x16x32_bf16 v[252:255], v[12:15], a[188:191], v[252:255]\n"
"s_waitcnt lgkmcnt(3)\n"
"v_mfma_f32_16x16x32_bf16 v[64:67], v[0:3], a[0:3], v[64:67]\n"
"ds_read_b64_tr_b16 v[8:9], v19 offset:0\n"
"v_mfma_f32_16x16x32_bf16 v[64:67], v[4:7], a[4:7], v[64:67]\n"
"s_cmp_le_u32 s80, 8\n"
"ds_read_b64_tr_b16 v[10:11], v19 offset:2048\n"
"v_mfma_f32_16x16x32_bf16 v[72:75], v[0:3], a[8:11], v[72:75]\n"
"s_cselect_b32 s56, 12, 8\n"
"ds_read_b64_tr_b16 v[12:13], v19 offset:4096\n"
"v_mfma_f32_16x16x32_bf16 v[72:75], v[4:7], a[12:15], v[72:75]\n"
"s_add_u32 s56, s56, s82\n"
"ds_read_b64_tr_b16 v[14:15], v19 offset:6144\n"
"v_mfma_f32_16x16x32_bf16 v[80:83], v[0:3], a[16:19], v[80:83]\n"
"s_mul_i32 s56, s56, 12288\n"
"ds_read_b128 a[60:63], v16 offset:48128\n"
"v_mfma_f32_16x16x32_bf16 v[80:83], v[4:7], a[20:23], v[80:83]\n"
"s_add_u32 s56, s56, 3072\n"
"ds_read_b128 a[64:67], v16 offset:43008\n"
"v_mfma_f32_16x16x32_bf16 v[88:91], v[0:3], a[24:27], v[88:91]\n"
"s_add_u32 m0, s67, 3072\n"
"ds_read_b128 a[68:71], v16 offset:49152\n"
"v_mfma_f32_16x16x32_bf16 v[88:91], v[4:7], a[28:31], v[88:91]\n"
"buffer_load_dwordx4 v56, s[36:39], s56 offen offset:0 sc1 lds\n"
"v_mfma_f32_16x16x32_bf16 v[96:99], v[0:3], a[32:35], v[96:99]\n"
"v_mfma_f32_16x16x32_bf16 v[96:99], v[4:7], a[36:39], v[96:99]\n"
"buffer_load_dwordx4 v56, s[36:39], s56 offen offset:1024 sc1 lds\n"
"v_mfma_f32_16x16x32_bf16 v[104:107], v[0:3], a[40:43], v[104:107]\n"
"v_mfma_f32_16x16x32_bf16 v[104:107], v[4:7], a[44:47], v[104:107]\n"
"s_waitcnt lgkmcnt(3)\n"
"v_mfma_f32_16x16x32_bf16 v[68:71], v[8:11], a[0:3], v[68:71]\n"
"ds_read_b64_tr_b16 v[0:1], v18 offset:512\n"
"v_mfma_f32_16x16x32_bf16 v[68:71], v[12:15], a[4:7], v[68:71]\n"
"ds_read_b64_tr_b16 v[2:3], v18 offset:2560\n"
"v_mfma_f32_16x16x32_bf16 v[76:79], v[8:11], a[8:11], v[76:79]\n"
"ds_read_b64_tr_b16 v[4:5], v18 offset:4608\n"
"v_mfma_f32_16x16x32_bf16 v[76:79], v[12:15], a[12:15], v[76:79]\n"
"ds_read_b64_tr_b16 v[6:7], v18 offset:6656\n"
"v_mfma_f32_16x16x32_bf16 v[84:87], v[8:11], a[16:19], v[84:87]\n"
"ds_read_b128 a[72:75], v16 offset:44032\n"
"v_mfma_f32_16x16x32_bf16 v[84:87], v[12:15], a[20:23], v[84:87]\n"
"ds_read_b128 a[76:79], v16 offset:50176\n"
"v_mfma_f32_16x16x32_bf16 v[92:95], v[8:11], a[24:27], v[92:95]\n"
"ds_read_b128 a[80:83], v16 offset:45056\n"
"v_mfma_f32_16x16x32_bf16 v[92:95], v[12:15], a[28:31], v[92:95]\n"
"buffer_load_dwordx4 v56, s[36:39], s56 offen offset:2048 sc1 lds\n"
"v_mfma_f32_16x16x32_bf16 v[100:103], v[8:11], a[32:35], v[100:103]\n"
"s_cmp_le_u32 s80, 7\n"
"s_cselect_b32 s66, 11, 7\n"
"s_add_u32 s66, s66, s82\n"
"s_lshl_b32 s66, s66, 17\n"
"s_or_b32 s66, s66, s81\n"
"s_add_u32 m0, s68, 122880\n"
"s_nop 0\n"
"buffer_load_dwordx4 v44, s[40:43], s66 offen offset:0 lds\n"
"v_mfma_f32_16x16x32_bf16 v[100:103], v[12:15], a[36:39], v[100:103]\n"
"v_mfma_f32_16x16x32_bf16 v[108:111], v[8:11], a[40:43], v[108:111]\n"
"buffer_load_dwordx4 v45, s[40:43], s66 offen offset:1024 lds\n"
"v_mfma_f32_16x16x32_bf16 v[108:111], v[12:15], a[44:47], v[108:111]\n"
"s_waitcnt lgkmcnt(3)\n"
"v_mfma_f32_16x16x32_bf16 v[112:115], v[0:3], a[0:3], v[112:115]\n"
"ds_read_b64_tr_b16 v[8:9], v19 offset:512\n"
"v_mfma_f32_16x16x32_bf16 v[112:115], v[4:7], a[4:7], v[112:115]\n"
"ds_read_b64_tr_b16 v[10:11], v19 offset:2560\n"
"v_mfma_f32_16x16x32_bf16 v[120:123], v[0:3], a[8:11], v[120:123]\n"
"ds_read_b64_tr_b16 v[12:13], v19 offset:4608\n"
"v_mfma_f32_16x16x32_bf16 v[120:123], v[4:7], a[12:15], v[120:123]\n"
"ds_read_b64_tr_b16 v[14:15], v19 offset:6656\n"
"v_mfma_f32_16x16x32_bf16 v[128:131], v[0:3], a[16:19], v[128:131]\n"
"ds_read_b128 a[84:87], v16 offset:51200\n"
"v_mfma_f32_16x16x32_bf16 v[128:131], v[4:7], a[20:23], v[128:131]\n"
"ds_read_b128 a[88:91], v16 offset:46080\n"
"v_mfma_f32_16x16x32_bf16 v[136:139], v[0:3], a[24:27], v[136:139]\n"
"ds_read_b128 a[92:95], v16 offset:52224\n"
"v_mfma_f32_16x16x32_bf16 v[136:139], v[4:7], a[28:31], v[136:139]\n"
"buffer_load_dwordx4 v46, s[40:43], s66 offen offset:2048 lds\n"
"v_mfma_f32_16x16x32_bf16 v[144:147], v[0:3], a[32:35], v[144:147]\n"
"v_mfma_f32_16x16x32_bf16 v[144:147], v[4:7], a[36:39], v[144:147]\n"
"buffer_load_dwordx4 v47, s[40:43], s66 offen offset:3072 lds\n"
"v_mfma_f32_16x16x32_bf16 v[152:155], v[0:3], a[40:43], v[152:155]\n"
"v_mfma_f32_16x16x32_bf16 v[152:155], v[4:7], a[44:47], v[152:155]\n"
"s_waitcnt vmcnt(24)\n"
"s_waitcnt vmcnt(20) lgkmcnt(0)\n"
"s_barrier\n"
"v_mfma_f32_16x16x32_bf16 v[116:119], v[8:11], a[0:3], v[116:119]\n"
"ds_read_b64_tr_b16 v[0:1], v18 offset:40960\n"
"v_mfma_f32_16x16x32_bf16 v[116:119], v[12:15], a[4:7], v[116:119]\n"
"ds_read_b64_tr_b16 v[2:3], v18 offset:43008\n"
"v_mfma_f32_16x16x32_bf16 v[124:127], v[8:11], a[8:11], v[124:127]\n"
"s_cmp_le_u32 s80, 9\n"
"ds_read_b64_tr_b16 v[4:5], v18 offset:45056\n"
"v_mfma_f32_16x16x32_bf16 v[124:127], v[12:15], a[12:15], v[124:127]\n"
"s_cselect_b32 s56, 13, 9\n"
"ds_read_b64_tr_b16 v[6:7], v18 offset:47104\n"
"v_mfma_f32_16x16x32_bf16 v[132:135], v[8:11], a[16:19], v[132:135]\n"
"s_add_u32 s56, s56, s82\n"
"ds_read_b128 a[96:99], v17 offset:0\n"
"v_mfma_f32_16x16x32_bf16 v[132:135], v[12:15], a[20:23], v[132:135]\n"
"s_mul_i32 s56, s56, 12288\n"
"ds_read_b128 a[100:103], v17 offset:6144\n"
"v_mfma_f32_16x16x32_bf16 v[140:143], v[8:11], a[24:27], v[140:143]\n"
"s_add_u32 m0, s67, 40960\n"
"ds_read_b128 a[104:107], v17 offset:1024\n"
"v_mfma_f32_16x16x32_bf16 v[140:143], v[12:15], a[28:31], v[140:143]\n"
"buffer_load_dwordx4 v56, s[36:39], s56 offen offset:0 sc1 lds\n"
"v_mfma_f32_16x16x32_bf16 v[148:151], v[8:11], a[32:35], v[148:151]\n"
"buffer_load_dwordx4 v56, s[36:39], s56 offen offset:1024 sc1 lds\n"
"v_mfma_f32_16x16x32_bf16 v[148:151], v[12:15], a[36:39], v[148:151]\n"
"v_mfma_f32_16x16x32_bf16 v[156:159], v[8:11], a[40:43], v[156:159]\n"
"buffer_load_dwordx4 v56, s[36:39], s56 offen offset:2048 sc1 lds\n"
"v_mfma_f32_16x16x32_bf16 v[156:159], v[12:15], a[44:47], v[156:159]\n"
"s_waitcnt lgkmcnt(3)\n"
"v_mfma_f32_16x16x32_bf16 v[64:67], v[0:3], a[48:51], v[64:67]\n"
"ds_read_b64_tr_b16 v[8:9], v19 offset:40960\n"
"v_mfma_f32_16x16x32_bf16 v[64:67], v[4:7], a[52:55], v[64:67]\n"
"s_cmp_le_u32 s80, 9\n"
"ds_read_b64_tr_b16 v[10:11], v19 offset:43008\n"
"v_mfma_f32_16x16x32_bf16 v[72:75], v[0:3], a[56:59], v[72:75]\n"
"s_cselect_b32 s56, 13, 9\n"
"ds_read_b64_tr_b16 v[12:13], v19 offset:45056\n"
"v_mfma_f32_16x16x32_bf16 v[72:75], v[4:7], a[60:63], v[72:75]\n"
"s_add_u32 s56, s56, s82\n"
"ds_read_b64_tr_b16 v[14:15], v19 offset:47104\n"
"v_mfma_f32_16x16x32_bf16 v[80:83], v[0:3], a[64:67], v[80:83]\n"
"s_mul_i32 s56, s56, 12288\n"
"ds_read_b128 a[108:111], v17 offset:7168\n"
"v_mfma_f32_16x16x32_bf16 v[80:83], v[4:7], a[68:71], v[80:83]\n"
"s_add_u32 s56, s56, 3072\n"
"ds_read_b128 a[112:115], v17 offset:2048\n"
"v_mfma_f32_16x16x32_bf16 v[88:91], v[0:3], a[72:75], v[88:91]\n"
"s_add_u32 m0, s67, 44032\n"
"ds_read_b128 a[116:119], v17 offset:8192\n"
"v_mfma_f32_16x16x32_bf16 v[88:91], v[4:7], a[76:79], v[88:91]\n"
"buffer_load_dwordx4 v56, s[36:39], s56 offen offset:0 sc1 lds\n"
"v_mfma_f32_16x16x32_bf16 v[96:99], v[0:3], a[80:83], v[96:99]\n"
"v_mfma_f32_16x16x32_bf16 v[96:99], v[4:7], a[84:87], v[96:99]\n"
"buffer_load_dwordx4 v56, s[36:39], s56 offen offset:1024 sc1 lds\n"
"v_mfma_f32_16x16x32_bf16 v[104:107], v[0:3], a[88:91], v[104:107]\n"
"v_mfma_f32_16x16x32_bf16 v[104:107], v[4:7], a[92:95], v[104:107]\n"
"s_waitcnt lgkmcnt(3)\n"
"v_mfma_f32_16x16x32_bf16 v[68:71], v[8:11], a[48:51], v[68:71]\n"
"ds_read_b64_tr_b16 v[0:1], v18 offset:41472\n"
"v_mfma_f32_16x16x32_bf16 v[68:71], v[12:15], a[52:55], v[68:71]\n"
"ds_read_b64_tr_b16 v[2:3], v18 offset:43520\n"
"v_mfma_f32_16x16x32_bf16 v[76:79], v[8:11], a[56:59], v[76:79]\n"
"ds_read_b64_tr_b16 v[4:5], v18 offset:45568\n"
"v_mfma_f32_16x16x32_bf16 v[76:79], v[12:15], a[60:63], v[76:79]\n"
"ds_read_b64_tr_b16 v[6:7], v18 offset:47616\n"
"v_mfma_f32_16x16x32_bf16 v[84:87], v[8:11], a[64:67], v[84:87]\n"
"ds_read_b128 a[120:123], v17 offset:3072\n"
"v_mfma_f32_16x16x32_bf16 v[84:87], v[12:15], a[68:71], v[84:87]\n"
"ds_read_b128 a[124:127], v17 offset:9216\n"
"v_mfma_f32_16x16x32_bf16 v[92:95], v[8:11], a[72:75], v[92:95]\n"
"ds_read_b128 a[128:131], v17 offset:4096\n"
"v_mfma_f32_16x16x32_bf16 v[92:95], v[12:15], a[76:79], v[92:95]\n"
"buffer_load_dwordx4 v56, s[36:39], s56 offen offset:2048 sc1 lds\n"
"v_mfma_f32_16x16x32_bf16 v[100:103], v[8:11], a[80:83], v[100:103]\n"
"s_cmp_le_u32 s80, 8\n"
"s_cselect_b32 s66, 12, 8\n"
"s_add_u32 s66, s66, s82\n"
"s_lshl_b32 s66, s66, 17\n"
"s_or_b32 s66, s66, s81\n"
"s_add_u32 m0, s68, 0\n"
"s_nop 0\n"
"buffer_load_dwordx4 v44, s[40:43], s66 offen offset:0 lds\n"
"v_mfma_f32_16x16x32_bf16 v[100:103], v[12:15], a[84:87], v[100:103]\n"
"v_mfma_f32_16x16x32_bf16 v[108:111], v[8:11], a[88:91], v[108:111]\n"
"buffer_load_dwordx4 v45, s[40:43], s66 offen offset:1024 lds\n"
"v_mfma_f32_16x16x32_bf16 v[108:111], v[12:15], a[92:95], v[108:111]\n"
"s_waitcnt lgkmcnt(3)\n"
"v_mfma_f32_16x16x32_bf16 v[112:115], v[0:3], a[48:51], v[112:115]\n"
"ds_read_b64_tr_b16 v[8:9], v19 offset:41472\n"
"v_mfma_f32_16x16x32_bf16 v[112:115], v[4:7], a[52:55], v[112:115]\n"
"ds_read_b64_tr_b16 v[10:11], v19 offset:43520\n"
"v_mfma_f32_16x16x32_bf16 v[120:123], v[0:3], a[56:59], v[120:123]\n"
"ds_read_b64_tr_b16 v[12:13], v19 offset:45568\n"
"v_mfma_f32_16x16x32_bf16 v[120:123], v[4:7], a[60:63], v[120:123]\n"
"ds_read_b64_tr_b16 v[14:15], v19 offset:47616\n"
"v_mfma_f32_16x16x32_bf16 v[128:131], v[0:3], a[64:67], v[128:131]\n"
"ds_read_b128 a[132:135], v17 offset:10240\n"
"v_mfma_f32_16x16x32_bf16 v[128:131], v[4:7], a[68:71], v[128:131]\n"
"ds_read_b128 a[136:139], v17 offset:5120\n"
"v_mfma_f32_16x16x32_bf16 v[136:139], v[0:3], a[72:75], v[136:139]\n"
"ds_read_b128 a[140:143], v17 offset:11264\n"
"v_mfma_f32_16x16x32_bf16 v[136:139], v[4:7], a[76:79], v[136:139]\n"
"buffer_load_dwordx4 v46, s[40:43], s66 offen offset:2048 lds\n"
"v_mfma_f32_16x16x32_bf16 v[144:147], v[0:3], a[80:83], v[144:147]\n"
"v_mfma_f32_16x16x32_bf16 v[144:147], v[4:7], a[84:87], v[144:147]\n"
"buffer_load_dwordx4 v47, s[40:43], s66 offen offset:3072 lds\n"
"v_mfma_f32_16x16x32_bf16 v[152:155], v[0:3], a[88:91], v[152:155]\n"
"v_mfma_f32_16x16x32_bf16 v[152:155], v[4:7], a[92:95], v[152:155]\n"
"s_waitcnt vmcnt(20)\n"
"s_waitcnt vmcnt(23) lgkmcnt(0)\n"
"s_barrier\n"
"v_mfma_f32_16x16x32_bf16 v[116:119], v[8:11], a[48:51], v[116:119]\n"
"ds_read_b64_tr_b16 v[0:1], v20 offset:0\n"
"v_mfma_f32_16x16x32_bf16 v[116:119], v[12:15], a[52:55], v[116:119]\n"
"ds_read_b64_tr_b16 v[2:3], v20 offset:2048\n"
"v_mfma_f32_16x16x32_bf16 v[124:127], v[8:11], a[56:59], v[124:127]\n"
"s_cmp_le_u32 s80, 10\n"
"ds_read_b64_tr_b16 v[4:5], v20 offset:4096\n"
"v_mfma_f32_16x16x32_bf16 v[124:127], v[12:15], a[60:63], v[124:127]\n"
"s_cselect_b32 s56, 14, 10\n"
"ds_read_b64_tr_b16 v[6:7], v20 offset:6144\n"
"v_mfma_f32_16x16x32_bf16 v[132:135], v[8:11], a[64:67], v[132:135]\n"
"s_add_u32 s56, s56, s82\n"
"ds_read_b128 a[144:147], v17 offset:40960\n"
"v_mfma_f32_16x16x32_bf16 v[132:135], v[12:15], a[68:71], v[132:135]\n"
"s_mul_i32 s56, s56, 12288\n"
"ds_read_b128 a[148:151], v17 offset:47104\n"
"v_mfma_f32_16x16x32_bf16 v[140:143], v[8:11], a[72:75], v[140:143]\n"
"s_add_u32 m0, s67, 81920\n"
"ds_read_b128 a[152:155], v17 offset:41984\n"
"v_mfma_f32_16x16x32_bf16 v[140:143], v[12:15], a[76:79], v[140:143]\n"
"buffer_load_dwordx4 v56, s[36:39], s56 offen offset:0 sc1 lds\n"
"v_mfma_f32_16x16x32_bf16 v[148:151], v[8:11], a[80:83], v[148:151]\n"
"buffer_load_dwordx4 v56, s[36:39], s56 offen offset:1024 sc1 lds\n"
"v_mfma_f32_16x16x32_bf16 v[148:151], v[12:15], a[84:87], v[148:151]\n"
"v_mfma_f32_16x16x32_bf16 v[156:159], v[8:11], a[88:91], v[156:159]\n"
"buffer_load_dwordx4 v56, s[36:39], s56 offen offset:2048 sc1 lds\n"
"v_mfma_f32_16x16x32_bf16 v[156:159], v[12:15], a[92:95], v[156:159]\n"
"s_waitcnt lgkmcnt(3)\n"
"v_mfma_f32_16x16x32_bf16 v[64:67], v[0:3], a[96:99], v[64:67]\n"
"ds_read_b64_tr_b16 v[8:9], v21 offset:0\n"
"v_mfma_f32_16x16x32_bf16 v[64:67], v[4:7], a[100:103], v[64:67]\n"
"s_cmp_le_u32 s80, 10\n"
"ds_read_b64_tr_b16 v[10:11], v21 offset:2048\n"
"v_mfma_f32_16x16x32_bf16 v[72:75], v[0:3], a[104:107], v[72:75]\n"
"s_cselect_b32 s56, 14, 10\n"
"ds_read_b64_tr_b16 v[12:13], v21 offset:4096\n"
"v_mfma_f32_16x16x32_bf16 v[72:75], v[4:7], a[108:111], v[72:75]\n"
"s_add_u32 s56, s56, s82\n"
"ds_read_b64_tr_b16 v[14:15], v21 offset:6144\n"
"v_mfma_f32_16x16x32_bf16 v[80:83], v[0:3], a[112:115], v[80:83]\n"
"s_mul_i32 s56, s56, 12288\n"
"ds_read_b128 a[156:159], v17 offset:48128\n"
"v_mfma_f32_16x16x32_bf16 v[80:83], v[4:7], a[116:119], v[80:83]\n"
"s_add_u32 s56, s56, 3072\n"
"ds_read_b128 a[160:163], v17 offset:43008\n"
"v_mfma_f32_16x16x32_bf16 v[88:91], v[0:3], a[120:123], v[88:91]\n"
"s_add_u32 m0, s67, 84992\n"
"ds_read_b128 a[164:167], v17 offset:49152\n"
"v_mfma_f32_16x16x32_bf16 v[88:91], v[4:7], a[124:127], v[88:91]\n"
"buffer_load_dwordx4 v56, s[36:39], s56 offen offset:0 sc1 lds\n"
"v_mfma_f32_16x16x32_bf16 v[96:99], v[0:3], a[128:131], v[96:99]\n"
"v_mfma_f32_16x16x32_bf16 v[96:99], v[4:7], a[132:135], v[96:99]\n"
"buffer_load_dwordx4 v56, s[36:39], s56 offen offset:1024 sc1 lds\n"
"v_mfma_f32_16x16x32_bf16 v[104:107], v[0:3], a[136:139], v[104:107]\n"
"v_mfma_f32_16x16x32_bf16 v[104:107], v[4:7], a[140:143], v[104:107]\n"
"s_waitcnt lgkmcnt(3)\n"
"v_mfma_f32_16x16x32_bf16 v[68:71], v[8:11], a[96:99], v[68:71]\n"
"ds_read_b64_tr_b16 v[0:1], v20 offset:512\n"
"v_mfma_f32_16x16x32_bf16 v[68:71], v[12:15], a[100:103], v[68:71]\n"
"ds_read_b64_tr_b16 v[2:3], v20 offset:2560\n"
"v_mfma_f32_16x16x32_bf16 v[76:79], v[8:11], a[104:107], v[76:79]\n"
"ds_read_b64_tr_b16 v[4:5], v20 offset:4608\n"
"v_mfma_f32_16x16x32_bf16 v[76:79], v[12:15], a[108:111], v[76:79]\n"
"ds_read_b64_tr_b16 v[6:7], v20 offset:6656\n"
"v_mfma_f32_16x16x32_bf16 v[84:87], v[8:11], a[112:115], v[84:87]\n"
"ds_read_b128 a[168:171], v17 offset:44032\n"
"v_mfma_f32_16x16x32_bf16 v[84:87], v[12:15], a[116:119], v[84:87]\n"
"ds_read_b128 a[172:175], v17 offset:50176\n"
"v_mfma_f32_16x16x32_bf16 v[92:95], v[8:11], a[120:123], v[92:95]\n"
"ds_read_b128 a[176:179], v17 offset:45056\n"
"v_mfma_f32_16x16x32_bf16 v[92:95], v[12:15], a[124:127], v[92:95]\n"
"buffer_load_dwordx4 v56, s[36:39], s56 offen offset:2048 sc1 lds\n"
"v_mfma_f32_16x16x32_bf16 v[100:103], v[8:11], a[128:131], v[100:103]\n"
"s_cmp_le_u32 s80, 9\n"
"s_cselect_b32 s66, 13, 9\n"
"s_add_u32 s66, s66, s82\n"
"s_lshl_b32 s66, s66, 17\n"
"s_or_b32 s66, s66, s81\n"
"s_add_u32 m0, s68, 40960\n"
"s_nop 0\n"
"buffer_load_dwordx4 v44, s[40:43], s66 offen offset:0 lds\n"
"v_mfma_f32_16x16x32_bf16 v[100:103], v[12:15], a[132:135], v[100:103]\n"
"v_mfma_f32_16x16x32_bf16 v[108:111], v[8:11], a[136:139], v[108:111]\n"
"buffer_load_dwordx4 v45, s[40:43], s66 offen offset:1024 lds\n"
"v_mfma_f32_16x16x32_bf16 v[108:111], v[12:15], a[140:143], v[108:111]\n"
"s_waitcnt lgkmcnt(3)\n"
"v_mfma_f32_16x16x32_bf16 v[112:115], v[0:3], a[96:99], v[112:115]\n"
"ds_read_b64_tr_b16 v[8:9], v21 offset:512\n"
"v_mfma_f32_16x16x32_bf16 v[112:115], v[4:7], a[100:103], v[112:115]\n"
"ds_read_b64_tr_b16 v[10:11], v21 offset:2560\n"
"v_mfma_f32_16x16x32_bf16 v[120:123], v[0:3], a[104:107], v[120:123]\n"
"ds_read_b64_tr_b16 v[12:13], v21 offset:4608\n"
"v_mfma_f32_16x16x32_bf16 v[120:123], v[4:7], a[108:111], v[120:123]\n"
"ds_read_b64_tr_b16 v[14:15], v21 offset:6656\n"
"v_mfma_f32_16x16x32_bf16 v[128:131], v[0:3], a[112:115], v[128:131]\n"
"ds_read_b128 a[180:183], v17 offset:51200\n"
"v_mfma_f32_16x16x32_bf16 v[128:131], v[4:7], a[116:119], v[128:131]\n"
"ds_read_b128 a[184:187], v17 offset:46080\n"
"v_mfma_f32_16x16x32_bf16 v[136:139], v[0:3], a[120:123], v[136:139]\n"
"ds_read_b128 a[188:191], v17 offset:52224\n"
"v_mfma_f32_16x16x32_bf16 v[136:139], v[4:7], a[124:127], v[136:139]\n"
"buffer_load_dwordx4 v46, s[40:43], s66 offen offset:2048 lds\n"
"v_mfma_f32_16x16x32_bf16 v[144:147], v[0:3], a[128:131], v[144:147]\n"
"v_mfma_f32_16x16x32_bf16 v[144:147], v[4:7], a[132:135], v[144:147]\n"
"buffer_load_dwordx4 v47, s[40:43], s66 offen offset:3072 lds\n"
"v_mfma_f32_16x16x32_bf16 v[152:155], v[0:3], a[136:139], v[152:155]\n"
"v_mfma_f32_16x16x32_bf16 v[152:155], v[4:7], a[140:143], v[152:155]\n"
"s_waitcnt vmcnt(24)\n"
"s_waitcnt vmcnt(20) lgkmcnt(0)\n"
"s_barrier\n"
"v_mfma_f32_16x16x32_bf16 v[116:119], v[8:11], a[96:99], v[116:119]\n"
"ds_read_b64_tr_b16 v[0:1], v20 offset:40960\n"
"v_mfma_f32_16x16x32_bf16 v[116:119], v[12:15], a[100:103], v[116:119]\n"
"ds_read_b64_tr_b16 v[2:3], v20 offset:43008\n"
"v_mfma_f32_16x16x32_bf16 v[124:127], v[8:11], a[104:107], v[124:127]\n"
"s_cmp_le_u32 s80, 11\n"
"ds_read_b64_tr_b16 v[4:5], v20 offset:45056\n"
"v_mfma_f32_16x16x32_bf16 v[124:127], v[12:15], a[108:111], v[124:127]\n"
"s_cselect_b32 s56, 15, 11\n"
"ds_read_b64_tr_b16 v[6:7], v20 offset:47104\n"
"v_mfma_f32_16x16x32_bf16 v[132:135], v[8:11], a[112:115], v[132:135]\n"
"s_add_u32 s56, s56, s82\n"
"ds_read_b128 a[0:3], v16 offset:0\n"
"v_mfma_f32_16x16x32_bf16 v[132:135], v[12:15], a[116:119], v[132:135]\n"
"s_mul_i32 s56, s56, 12288\n"
"ds_read_b128 a[4:7], v16 offset:6144\n"
"v_mfma_f32_16x16x32_bf16 v[140:143], v[8:11], a[120:123], v[140:143]\n"
"s_add_u32 m0, s67, 122880\n"
"ds_read_b128 a[8:11], v16 offset:1024\n"
"v_mfma_f32_16x16x32_bf16 v[140:143], v[12:15], a[124:127], v[140:143]\n"
"buffer_load_dwordx4 v56, s[36:39], s56 offen offset:0 sc1 lds\n"
"v_mfma_f32_16x16x32_bf16 v[148:151], v[8:11], a[128:131], v[148:151]\n"
"buffer_load_dwordx4 v56, s[36:39], s56 offen offset:1024 sc1 lds\n"
"v_mfma_f32_16x16x32_bf16 v[148:151], v[12:15], a[132:135], v[148:151]\n"
"v_mfma_f32_16x16x32_bf16 v[156:159], v[8:11], a[136:139], v[156:159]\n"
"buffer_load_dwordx4 v56, s[36:39], s56 offen offset:2048 sc1 lds\n"
"v_mfma_f32_16x16x32_bf16 v[156:159], v[12:15], a[140:143], v[156:159]\n"
"s_waitcnt lgkmcnt(3)\n"
"v_mfma_f32_16x16x32_bf16 v[64:67], v[0:3], a[144:147], v[64:67]\n"
"ds_read_b64_tr_b16 v[8:9], v21 offset:40960\n"
"v_mfma_f32_16x16x32_bf16 v[64:67], v[4:7], a[148:151], v[64:67]\n"
"s_cmp_le_u32 s80, 11\n"
"ds_read_b64_tr_b16 v[10:11], v21 offset:43008\n"
"v_mfma_f32_16x16x32_bf16 v[72:75], v[0:3], a[152:155], v[72:75]\n"
"s_cselect_b32 s56, 15, 11\n"
"ds_read_b64_tr_b16 v[12:13], v21 offset:45056\n"
"v_mfma_f32_16x16x32_bf16 v[72:75], v[4:7], a[156:159], v[72:75]\n"
"s_add_u32 s56, s56, s82\n"
"ds_read_b64_tr_b16 v[14:15], v21 offset:47104\n"
"v_mfma_f32_16x16x32_bf16 v[80:83], v[0:3], a[160:163], v[80:83]\n"
"s_mul_i32 s56, s56, 12288\n"
"ds_read_b128 a[12:15], v16 offset:7168\n"
"v_mfma_f32_16x16x32_bf16 v[80:83], v[4:7], a[164:167], v[80:83]\n"
"s_add_u32 s56, s56, 3072\n"
"ds_read_b128 a[16:19], v16 offset:2048\n"
"v_mfma_f32_16x16x32_bf16 v[88:91], v[0:3], a[168:171], v[88:91]\n"
"s_add_u32 m0, s67, 125952\n"
"ds_read_b128 a[20:23], v16 offset:8192\n"
"v_mfma_f32_16x16x32_bf16 v[88:91], v[4:7], a[172:175], v[88:91]\n"
"buffer_load_dwordx4 v56, s[36:39], s56 offen offset:0 sc1 lds\n"
"v_mfma_f32_16x16x32_bf16 v[96:99], v[0:3], a[176:179], v[96:99]\n"
"v_mfma_f32_16x16x32_bf16 v[96:99], v[4:7], a[180:183], v[96:99]\n"
"buffer_load_dwordx4 v56, s[36:39], s56 offen offset:1024 sc1 lds\n"
"v_mfma_f32_16x16x32_bf16 v[104:107], v[0:3], a[184:187], v[104:107]\n"
"v_mfma_f32_16x16x32_bf16 v[104:107], v[4:7], a[188:191], v[104:107]\n"
"s_waitcnt lgkmcnt(3)\n"
"v_mfma_f32_16x16x32_bf16 v[68:71], v[8:11], a[144:147], v[68:71]\n"
"ds_read_b64_tr_b16 v[0:1], v20 offset:41472\n"
"v_mfma_f32_16x16x32_bf16 v[68:71], v[12:15], a[148:151], v[68:71]\n"
"ds_read_b64_tr_b16 v[2:3], v20 offset:43520\n"
"v_mfma_f32_16x16x32_bf16 v[76:79], v[8:11], a[152:155], v[76:79]\n"
"ds_read_b64_tr_b16 v[4:5], v20 offset:45568\n"
"v_mfma_f32_16x16x32_bf16 v[76:79], v[12:15], a[156:159], v[76:79]\n"
"ds_read_b64_tr_b16 v[6:7], v20 offset:47616\n"
"v_mfma_f32_16x16x32_bf16 v[84:87], v[8:11], a[160:163], v[84:87]\n"
"ds_read_b128 a[24:27], v16 offset:3072\n"
"v_mfma_f32_16x16x32_bf16 v[84:87], v[12:15], a[164:167], v[84:87]\n"
"ds_read_b128 a[28:31], v16 offset:9216\n"
"v_mfma_f32_16x16x32_bf16 v[92:95], v[8:11], a[168:171], v[92:95]\n"
"ds_read_b128 a[32:35], v16 offset:4096\n"
"v_mfma_f32_16x16x32_bf16 v[92:95], v[12:15], a[172:175], v[92:95]\n"
"buffer_load_dwordx4 v56, s[36:39], s56 offen offset:2048 sc1 lds\n"
"v_mfma_f32_16x16x32_bf16 v[100:103], v[8:11], a[176:179], v[100:103]\n"
"s_cmp_le_u32 s80, 10\n"
"s_cselect_b32 s66, 14, 10\n"
"s_add_u32 s66, s66, s82\n"
"s_lshl_b32 s66, s66, 17\n"
"s_or_b32 s66, s66, s81\n"
"s_add_u32 m0, s68, 81920\n"
"s_nop 0\n"
"buffer_load_dwordx4 v44, s[40:43], s66 offen offset:0 lds\n"
"v_mfma_f32_16x16x32_bf16 v[100:103], v[12:15], a[180:183], v[100:103]\n"
"v_mfma_f32_16x16x32_bf16 v[108:111], v[8:11], a[184:187], v[108:111]\n"
"buffer_load_dwordx4 v45, s[40:43], s66 offen offset:1024 lds\n"
"v_mfma_f32_16x16x32_bf16 v[108:111], v[12:15], a[188:191], v[108:111]\n"
"s_waitcnt lgkmcnt(3)\n"
"v_mfma_f32_16x16x32_bf16 v[112:115], v[0:3], a[144:147], v[112:115]\n"
"ds_read_b64_tr_b16 v[8:9], v21 offset:41472\n"
"v_mfma_f32_16x16x32_bf16 v[112:115], v[4:7], a[148:151], v[112:115]\n"
"ds_read_b64_tr_b16 v[10:11], v21 offset:43520\n"
"v_mfma_f32_16x16x32_bf16 v[120:123], v[0:3], a[152:155], v[120:123]\n"
"ds_read_b64_tr_b16 v[12:13], v21 offset:45568\n"
"v_mfma_f32_16x16x32_bf16 v[120:123], v[4:7], a[156:159], v[120:123]\n"
"ds_read_b64_tr_b16 v[14:15], v21 offset:47616\n"
"v_mfma_f32_16x16x32_bf16 v[128:131], v[0:3], a[160:163], v[128:131]\n"
"ds_read_b128 a[36:39], v16 offset:10240\n"
"v_mfma_f32_16x16x32_bf16 v[128:131], v[4:7], a[164:167], v[128:131]\n"
"ds_read_b128 a[40:43], v16 offset:5120\n"
"v_mfma_f32_16x16x32_bf16 v[136:139], v[0:3], a[168:171], v[136:139]\n"
"ds_read_b128 a[44:47], v16 offset:11264\n"
"v_mfma_f32_16x16x32_bf16 v[136:139], v[4:7], a[172:175], v[136:139]\n"
"buffer_load_dwordx4 v46, s[40:43], s66 offen offset:2048 lds\n"
"v_mfma_f32_16x16x32_bf16 v[144:147], v[0:3], a[176:179], v[144:147]\n"
"v_mfma_f32_16x16x32_bf16 v[144:147], v[4:7], a[180:183], v[144:147]\n"
"buffer_load_dwordx4 v47, s[40:43], s66 offen offset:3072 lds\n"
"v_mfma_f32_16x16x32_bf16 v[152:155], v[0:3], a[184:187], v[152:155]\n"
"v_mfma_f32_16x16x32_bf16 v[152:155], v[4:7], a[188:191], v[152:155]\n"
"s_waitcnt vmcnt(24)\n"
"s_waitcnt vmcnt(20) lgkmcnt(0)\n"
"s_barrier\n"
"P2_C:\n"
"v_mfma_f32_16x16x32_bf16 v[116:119], v[8:11], a[144:147], v[116:119]\n"
"ds_read_b64_tr_b16 v[0:1], v18 offset:0\n"
"v_mfma_f32_16x16x32_bf16 v[116:119], v[12:15], a[148:151], v[116:119]\n"
"ds_read_b64_tr_b16 v[2:3], v18 offset:2048\n"
"v_mfma_f32_16x16x32_bf16 v[124:127], v[8:11], a[152:155], v[124:127]\n"
"s_cmp_le_u32 s80, 4\n"
"ds_read_b64_tr_b16 v[4:5], v18 offset:4096\n"
"v_mfma_f32_16x16x32_bf16 v[124:127], v[12:15], a[156:159], v[124:127]\n"
"s_cselect_b32 s56, 8, 4\n"
"ds_read_b64_tr_b16 v[6:7], v18 offset:6144\n"
"v_mfma_f32_16x16x32_bf16 v[132:135], v[8:11], a[160:163], v[132:135]\n"
"s_add_u32 s56, s56, s82\n"
"ds_read_b128 a[48:51], v16 offset:40960\n"
"v_mfma_f32_16x16x32_bf16 v[132:135], v[12:15], a[164:167], v[132:135]\n"
"s_mul_i32 s56, s56, 12288\n"
"ds_read_b128 a[52:55], v16 offset:47104\n"
"v_mfma_f32_16x16x32_bf16 v[140:143], v[8:11], a[168:171], v[140:143]\n"
"s_add_u32 m0, s67, 0\n"
"ds_read_b128 a[56:59], v16 offset:41984\n"
"v_mfma_f32_16x16x32_bf16 v[140:143], v[12:15], a[172:175], v[140:143]\n"
"buffer_load_dwordx4 v56, s[36:39], s56 offen offset:0 sc1 lds\n"
"v_mfma_f32_16x16x32_bf16 v[148:151], v[8:11], a[176:179], v[148:151]\n"
"buffer_load_dwordx4 v56, s[36:39], s56 offen offset:1024 sc1 lds\n"
"v_mfma_f32_16x16x32_bf16 v[148:151], v[12:15], a[180:183], v[148:151]\n"
"v_mfma_f32_16x16x32_bf16 v[156:159], v[8:11], a[184:187], v[156:159]\n"
"buffer_load_dwordx4 v56, s[36:39], s56 offen offset:2048 sc1 lds\n"
"v_mfma_f32_16x16x32_bf16 v[156:159], v[12:15], a[188:191], v[156:159]\n"
"s_waitcnt lgkmcnt(3)\n"
"v_mfma_f32_16x16x32_bf16 v[64:67], v[0:3], a[0:3], v[64:67]\n"
"ds_read_b64_tr_b16 v[8:9], v19 offset:0\n"
"v_mfma_f32_16x16x32_bf16 v[64:67], v[4:7], a[4:7], v[64:67]\n"
"s_cmp_le_u32 s80, 4\n"
"ds_read_b64_tr_b16 v[10:11], v19 offset:2048\n"
"v_mfma_f32_16x16x32_bf16 v[72:75], v[0:3], a[8:11], v[72:75]\n"
"s_cselect_b32 s56, 8, 4\n"
"ds_read_b64_tr_b16 v[12:13], v19 offset:4096\n"
"v_mfma_f32_16x16x32_bf16 v[72:75], v[4:7], a[12:15], v[72:75]\n"
"s_add_u32 s56, s56, s82\n"
"ds_read_b64_tr_b16 v[14:15], v19 offset:6144\n"
"v_mfma_f32_16x16x32_bf16 v[80:83], v[0:3], a[16:19], v[80:83]\n"
"s_mul_i32 s56, s56, 12288\n"
"ds_read_b128 a[60:63], v16 offset:48128\n"
"v_mfma_f32_16x16x32_bf16 v[80:83], v[4:7], a[20:23], v[80:83]\n"
"s_add_u32 s56, s56, 3072\n"
"ds_read_b128 a[64:67], v16 offset:43008\n"
"v_mfma_f32_16x16x32_bf16 v[88:91], v[0:3], a[24:27], v[88:91]\n"
"s_add_u32 m0, s67, 3072\n"
"ds_read_b128 a[68:71], v16 offset:49152\n"
"v_mfma_f32_16x16x32_bf16 v[88:91], v[4:7], a[28:31], v[88:91]\n"
"buffer_load_dwordx4 v56, s[36:39], s56 offen offset:0 sc1 lds\n"
"v_mfma_f32_16x16x32_bf16 v[96:99], v[0:3], a[32:35], v[96:99]\n"
"v_mfma_f32_16x16x32_bf16 v[96:99], v[4:7], a[36:39], v[96:99]\n"
"buffer_load_dwordx4 v56, s[36:39], s56 offen offset:1024 sc1 lds\n"
"v_mfma_f32_16x16x32_bf16 v[104:107], v[0:3], a[40:43], v[104:107]\n"
"v_mfma_f32_16x16x32_bf16 v[104:107], v[4:7], a[44:47], v[104:107]\n"
"s_waitcnt lgkmcnt(3)\n"
"v_mfma_f32_16x16x32_bf16 v[68:71], v[8:11], a[0:3], v[68:71]\n"
"ds_read_b64_tr_b16 v[0:1], v18 offset:512\n"
"v_mfma_f32_16x16x32_bf16 v[68:71], v[12:15], a[4:7], v[68:71]\n"
"ds_read_b64_tr_b16 v[2:3], v18 offset:2560\n"
"v_mfma_f32_16x16x32_bf16 v[76:79], v[8:11], a[8:11], v[76:79]\n"
"ds_read_b64_tr_b16 v[4:5], v18 offset:4608\n"
"v_mfma_f32_16x16x32_bf16 v[76:79], v[12:15], a[12:15], v[76:79]\n"
"ds_read_b64_tr_b16 v[6:7], v18 offset:6656\n"
"v_mfma_f32_16x16x32_bf16 v[84:87], v[8:11], a[16:19], v[84:87]\n"
"ds_read_b128 a[72:75], v16 offset:44032\n"
"v_mfma_f32_16x16x32_bf16 v[84:87], v[12:15], a[20:23], v[84:87]\n"
"ds_read_b128 a[76:79], v16 offset:50176\n"
"v_mfma_f32_16x16x32_bf16 v[92:95], v[8:11], a[24:27], v[92:95]\n"
"ds_read_b128 a[80:83], v16 offset:45056\n"
"v_mfma_f32_16x16x32_bf16 v[92:95], v[12:15], a[28:31], v[92:95]\n"
"buffer_load_dwordx4 v56, s[36:39], s56 offen offset:2048 sc1 lds\n"
"v_mfma_f32_16x16x32_bf16 v[100:103], v[8:11], a[32:35], v[100:103]\n"
"s_cmp_le_u32 s80, 11\n"
"s_cselect_b32 s66, 15, 11\n"
"s_add_u32 s66, s66, s82\n"
"s_lshl_b32 s66, s66, 17\n"
"s_or_b32 s66, s66, s81\n"
"s_add_u32 m0, s68, 122880\n"
"s_nop 0\n"
"buffer_load_dwordx4 v44, s[40:43], s66 offen offset:0 lds\n"
"v_mfma_f32_16x16x32_bf16 v[100:103], v[12:15], a[36:39], v[100:103]\n"
"v_mfma_f32_16x16x32_bf16 v[108:111], v[8:11], a[40:43], v[108:111]\n"
"buffer_load_dwordx4 v45, s[40:43], s66 offen offset:1024 lds\n"
"v_mfma_f32_16x16x32_bf16 v[108:111], v[12:15], a[44:47], v[108:111]\n"
"s_waitcnt lgkmcnt(3)\n"
"v_mfma_f32_16x16x32_bf16 v[112:115], v[0:3], a[0:3], v[112:115]\n"
"ds_read_b64_tr_b16 v[8:9], v19 offset:512\n"
"v_mfma_f32_16x16x32_bf16 v[112:115], v[4:7], a[4:7], v[112:115]\n"
"ds_read_b64_tr_b16 v[10:11], v19 offset:2560\n"
"v_mfma_f32_16x16x32_bf16 v[120:123], v[0:3], a[8:11], v[120:123]\n"
"ds_read_b64_tr_b16 v[12:13], v19 offset:4608\n"
"v_mfma_f32_16x16x32_bf16 v[120:123], v[4:7], a[12:15], v[120:123]\n"
"ds_read_b64_tr_b16 v[14:15], v19 offset:6656\n"
"v_mfma_f32_16x16x32_bf16 v[128:131], v[0:3], a[16:19], v[128:131]\n"
"ds_read_b128 a[84:87], v16 offset:51200\n"
"v_mfma_f32_16x16x32_bf16 v[128:131], v[4:7], a[20:23], v[128:131]\n"
"ds_read_b128 a[88:91], v16 offset:46080\n"
"v_mfma_f32_16x16x32_bf16 v[136:139], v[0:3], a[24:27], v[136:139]\n"
"ds_read_b128 a[92:95], v16 offset:52224\n"
"v_mfma_f32_16x16x32_bf16 v[136:139], v[4:7], a[28:31], v[136:139]\n"
"buffer_load_dwordx4 v46, s[40:43], s66 offen offset:2048 lds\n"
"v_mfma_f32_16x16x32_bf16 v[144:147], v[0:3], a[32:35], v[144:147]\n"
"v_mfma_f32_16x16x32_bf16 v[144:147], v[4:7], a[36:39], v[144:147]\n"
"buffer_load_dwordx4 v47, s[40:43], s66 offen offset:3072 lds\n"
"v_mfma_f32_16x16x32_bf16 v[152:155], v[0:3], a[40:43], v[152:155]\n"
"v_mfma_f32_16x16x32_bf16 v[152:155], v[4:7], a[44:47], v[152:155]\n"
"s_waitcnt vmcnt(24)\n"
"s_waitcnt vmcnt(20) lgkmcnt(0)\n"
"s_barrier\n"
"v_mfma_f32_16x16x32_bf16 v[116:119], v[8:11], a[0:3], v[116:119]\n"
"ds_read_b64_tr_b16 v[0:1], v18 offset:40960\n"
"v_mfma_f32_16x16x32_bf16 v[116:119], v[12:15], a[4:7], v[116:119]\n"
"ds_read_b64_tr_b16 v[2:3], v18 offset:43008\n"
"v_mfma_f32_16x16x32_bf16 v[124:127], v[8:11], a[8:11], v[124:127]\n"
"s_cmp_le_u32 s80, 5\n"
"ds_read_b64_tr_b16 v[4:5], v18 offset:45056\n"
"v_mfma_f32_16x16x32_bf16 v[124:127], v[12:15], a[12:15], v[124:127]\n"
"s_cselect_b32 s56, 9, 5\n"
"ds_read_b64_tr_b16 v[6:7], v18 offset:47104\n"
"v_mfma_f32_16x16x32_bf16 v[132:135], v[8:11], a[16:19], v[132:135]\n"
"s_add_u32 s56, s56, s82\n"
"ds_read_b128 a[96:99], v17 offset:0\n"
"v_mfma_f32_16x16x32_bf16 v[132:135], v[12:15], a[20:23], v[132:135]\n"
"s_mul_i32 s56, s56, 12288\n"
"ds_read_b128 a[100:103], v17 offset:6144\n"
"v_mfma_f32_16x16x32_bf16 v[140:143], v[8:11], a[24:27], v[140:143]\n"
"s_add_u32 m0, s67, 40960\n"
"ds_read_b128 a[104:107], v17 offset:1024\n"
"v_mfma_f32_16x16x32_bf16 v[140:143], v[12:15], a[28:31], v[140:143]\n"
"buffer_load_dwordx4 v56, s[36:39], s56 offen offset:0 sc1 lds\n"
"v_mfma_f32_16x16x32_bf16 v[148:151], v[8:11], a[32:35], v[148:151]\n"
"buffer_load_dwordx4 v56, s[36:39], s56 offen offset:1024 sc1 lds\n"
"v_mfma_f32_16x16x32_bf16 v[148:151], v[12:15], a[36:39], v[148:151]\n"
"v_mfma_f32_16x16x32_bf16 v[156:159], v[8:11], a[40:43], v[156:159]\n"
"buffer_load_dwordx4 v56, s[36:39], s56 offen offset:2048 sc1 lds\n"
"v_mfma_f32_16x16x32_bf16 v[156:159], v[12:15], a[44:47], v[156:159]\n"
"s_waitcnt lgkmcnt(3)\n"
"v_mfma_f32_16x16x32_bf16 v[64:67], v[0:3], a[48:51], v[64:67]\n"
"ds_read_b64_tr_b16 v[8:9], v19 offset:40960\n"
"v_mfma_f32_16x16x32_bf16 v[64:67], v[4:7], a[52:55], v[64:67]\n"
"s_cmp_le_u32 s80, 5\n"
"ds_read_b64_tr_b16 v[10:11], v19 offset:43008\n"
"v_mfma_f32_16x16x32_bf16 v[72:75], v[0:3], a[56:59], v[72:75]\n"
"s_cselect_b32 s56, 9, 5\n"
"ds_read_b64_tr_b16 v[12:13], v19 offset:45056\n"
"v_mfma_f32_16x16x32_bf16 v[72:75], v[4:7], a[60:63], v[72:75]\n"
"s_add_u32 s56, s56, s82\n"
"ds_read_b64_tr_b16 v[14:15], v19 offset:47104\n"
"v_mfma_f32_16x16x32_bf16 v[80:83], v[0:3], a[64:67], v[80:83]\n"
"s_mul_i32 s56, s56, 12288\n"
"ds_read_b128 a[108:111], v17 offset:7168\n"
"v_mfma_f32_16x16x32_bf16 v[80:83], v[4:7], a[68:71], v[80:83]\n"
"s_add_u32 s56, s56, 3072\n"
"ds_read_b128 a[112:115], v17 offset:2048\n"
"v_mfma_f32_16x16x32_bf16 v[88:91], v[0:3], a[72:75], v[88:91]\n"
"s_add_u32 m0, s67, 44032\n"
"ds_read_b128 a[116:119], v17 offset:8192\n"
"v_mfma_f32_16x16x32_bf16 v[88:91], v[4:7], a[76:79], v[88:91]\n"
"buffer_load_dwordx4 v56, s[36:39], s56 offen offset:0 sc1 lds\n"
"v_mfma_f32_16x16x32_bf16 v[96:99], v[0:3], a[80:83], v[96:99]\n"
"v_mfma_f32_16x16x32_bf16 v[96:99], v[4:7], a[84:87], v[96:99]\n"
"buffer_load_dwordx4 v56, s[36:39], s56 offen offset:1024 sc1 lds\n"
"v_mfma_f32_16x16x32_bf16 v[104:107], v[0:3], a[88:91], v[104:107]\n"
"v_mfma_f32_16x16x32_bf16 v[104:107], v[4:7], a[92:95], v[104:107]\n"
"s_waitcnt lgkmcnt(3)\n"
"v_mfma_f32_16x16x32_bf16 v[68:71], v[8:11], a[48:51], v[68:71]\n"
"ds_read_b64_tr_b16 v[0:1], v18 offset:41472\n"
"v_mfma_f32_16x16x32_bf16 v[68:71], v[12:15], a[52:55], v[68:71]\n"
"ds_read_b64_tr_b16 v[2:3], v18 offset:43520\n"
"v_mfma_f32_16x16x32_bf16 v[76:79], v[8:11], a[56:59], v[76:79]\n"
"ds_read_b64_tr_b16 v[4:5], v18 offset:45568\n"
"v_mfma_f32_16x16x32_bf16 v[76:79], v[12:15], a[60:63], v[76:79]\n"
"ds_read_b64_tr_b16 v[6:7], v18 offset:47616\n"
"v_mfma_f32_16x16x32_bf16 v[84:87], v[8:11], a[64:67], v[84:87]\n"
"ds_read_b128 a[120:123], v17 offset:3072\n"
"v_mfma_f32_16x16x32_bf16 v[84:87], v[12:15], a[68:71], v[84:87]\n"
"ds_read_b128 a[124:127], v17 offset:9216\n"
"v_mfma_f32_16x16x32_bf16 v[92:95], v[8:11], a[72:75], v[92:95]\n"
"ds_read_b128 a[128:131], v17 offset:4096\n"
"v_mfma_f32_16x16x32_bf16 v[92:95], v[12:15], a[76:79], v[92:95]\n"
"buffer_load_dwordx4 v56, s[36:39], s56 offen offset:2048 sc1 lds\n"
"v_mfma_f32_16x16x32_bf16 v[100:103], v[8:11], a[80:83], v[100:103]\n"
"s_cmp_le_u32 s80, 8\n"
"s_cselect_b32 s66, 12, 8\n"
"s_add_u32 s66, s66, s82\n"
"s_lshl_b32 s66, s66, 17\n"
"s_or_b32 s66, s66, s81\n"
"s_xor_b32 s66, s66, 128\n"
"s_add_u32 m0, s68, 0\n"
"s_nop 0\n"
"buffer_load_dwordx4 v44, s[40:43], s66 offen offset:0 lds\n"
"v_mfma_f32_16x16x32_bf16 v[100:103], v[12:15], a[84:87], v[100:103]\n"
"v_mfma_f32_16x16x32_bf16 v[108:111], v[8:11], a[88:91], v[108:111]\n"
"buffer_load_dwordx4 v45, s[40:43], s66 offen offset:1024 lds\n"
"v_mfma_f32_16x16x32_bf16 v[108:111], v[12:15], a[92:95], v[108:111]\n"
"s_waitcnt lgkmcnt(3)\n"
"v_mfma_f32_16x16x32_bf16 v[112:115], v[0:3], a[48:51], v[112:115]\n"
"ds_read_b64_tr_b16 v[8:9], v19 offset:41472\n"
"v_mfma_f32_16x16x32_bf16 v[112:115], v[4:7], a[52:55], v[112:115]\n"
"ds_read_b64_tr_b16 v[10:11], v19 offset:43520\n"
"v_mfma_f32_16x16x32_bf16 v[120:123], v[0:3], a[56:59], v[120:123]\n"
"ds_read_b64_tr_b16 v[12:13], v19 offset:45568\n"
"v_mfma_f32_16x16x32_bf16 v[120:123], v[4:7], a[60:63], v[120:123]\n"
"ds_read_b64_tr_b16 v[14:15], v19 offset:47616\n"
"v_mfma_f32_16x16x32_bf16 v[128:131], v[0:3], a[64:67], v[128:131]\n"
"ds_read_b128 a[132:135], v17 offset:10240\n"
"v_mfma_f32_16x16x32_bf16 v[128:131], v[4:7], a[68:71], v[128:131]\n"
"ds_read_b128 a[136:139], v17 offset:5120\n"
"v_mfma_f32_16x16x32_bf16 v[136:139], v[0:3], a[72:75], v[136:139]\n"
"ds_read_b128 a[140:143], v17 offset:11264\n"
"v_mfma_f32_16x16x32_bf16 v[136:139], v[4:7], a[76:79], v[136:139]\n"
"buffer_load_dwordx4 v46, s[40:43], s66 offen offset:2048 lds\n"
"v_mfma_f32_16x16x32_bf16 v[144:147], v[0:3], a[80:83], v[144:147]\n"
"v_mfma_f32_16x16x32_bf16 v[144:147], v[4:7], a[84:87], v[144:147]\n"
"buffer_load_dwordx4 v47, s[40:43], s66 offen offset:3072 lds\n"
"v_mfma_f32_16x16x32_bf16 v[152:155], v[0:3], a[88:91], v[152:155]\n"
"v_mfma_f32_16x16x32_bf16 v[152:155], v[4:7], a[92:95], v[152:155]\n"
"s_waitcnt vmcnt(24)\n"
"s_waitcnt vmcnt(20) lgkmcnt(0)\n"
"s_barrier\n"
"v_mfma_f32_16x16x32_bf16 v[116:119], v[8:11], a[48:51], v[116:119]\n"
"ds_read_b64_tr_b16 v[0:1], v20 offset:0\n"
"v_mfma_f32_16x16x32_bf16 v[116:119], v[12:15], a[52:55], v[116:119]\n"
"ds_read_b64_tr_b16 v[2:3], v20 offset:2048\n"
"v_mfma_f32_16x16x32_bf16 v[124:127], v[8:11], a[56:59], v[124:127]\n"
"s_cmp_le_u32 s80, 6\n"
"ds_read_b64_tr_b16 v[4:5], v20 offset:4096\n"
"v_mfma_f32_16x16x32_bf16 v[124:127], v[12:15], a[60:63], v[124:127]\n"
"s_cselect_b32 s56, 10, 6\n"
"ds_read_b64_tr_b16 v[6:7], v20 offset:6144\n"
"v_mfma_f32_16x16x32_bf16 v[132:135], v[8:11], a[64:67], v[132:135]\n"
"s_add_u32 s56, s56, s82\n"
"ds_read_b128 a[144:147], v17 offset:40960\n"
"v_mfma_f32_16x16x32_bf16 v[132:135], v[12:15], a[68:71], v[132:135]\n"
"s_mul_i32 s56, s56, 12288\n"
"ds_read_b128 a[148:151], v17 offset:47104\n"
"v_mfma_f32_16x16x32_bf16 v[140:143], v[8:11], a[72:75], v[140:143]\n"
"s_add_u32 m0, s67, 81920\n"
"ds_read_b128 a[152:155], v17 offset:41984\n"
"v_mfma_f32_16x16x32_bf16 v[140:143], v[12:15], a[76:79], v[140:143]\n"
"buffer_load_dwordx4 v56, s[36:39], s56 offen offset:0 sc1 lds\n"
"v_mfma_f32_16x16x32_bf16 v[148:151], v[8:11], a[80:83], v[148:151]\n"
"buffer_load_dwordx4 v56, s[36:39], s56 offen offset:1024 sc1 lds\n"
"v_mfma_f32_16x16x32_bf16 v[148:151], v[12:15], a[84:87], v[148:151]\n"
"v_mfma_f32_16x16x32_bf16 v[156:159], v[8:11], a[88:91], v[156:159]\n"
"buffer_load_dwordx4 v56, s[36:39], s56 offen offset:2048 sc1 lds\n"
"v_mfma_f32_16x16x32_bf16 v[156:159], v[12:15], a[92:95], v[156:159]\n"
"s_waitcnt lgkmcnt(3)\n"
"v_mfma_f32_16x16x32_bf16 v[64:67], v[0:3], a[96:99], v[64:67]\n"
"ds_read_b64_tr_b16 v[8:9], v21 offset:0\n"
"v_mfma_f32_16x16x32_bf16 v[64:67], v[4:7], a[100:103], v[64:67]\n"
"s_cmp_le_u32 s80, 6\n"
"ds_read_b64_tr_b16 v[10:11], v21 offset:2048\n"
"v_mfma_f32_16x16x32_bf16 v[72:75], v[0:3], a[104:107], v[72:75]\n"
"s_cselect_b32 s56, 10, 6\n"
"ds_read_b64_tr_b16 v[12:13], v21 offset:4096\n"
"v_mfma_f32_16x16x32_bf16 v[72:75], v[4:7], a[108:111], v[72:75]\n"
"s_add_u32 s56, s56, s82\n"
"ds_read_b64_tr_b16 v[14:15], v21 offset:6144\n"
"v_mfma_f32_16x16x32_bf16 v[80:83], v[0:3], a[112:115], v[80:83]\n"
"s_mul_i32 s56, s56, 12288\n"
"ds_read_b128 a[156:159], v17 offset:48128\n"
"v_mfma_f32_16x16x32_bf16 v[80:83], v[4:7], a[116:119], v[80:83]\n"
"s_add_u32 s56, s56, 3072\n"
"ds_read_b128 a[160:163], v17 offset:43008\n"
"v_mfma_f32_16x16x32_bf16 v[88:91], v[0:3], a[120:123], v[88:91]\n"
"s_add_u32 m0, s67, 84992\n"
"ds_read_b128 a[164:167], v17 offset:49152\n"
"v_mfma_f32_16x16x32_bf16 v[88:91], v[4:7], a[124:127], v[88:91]\n"
"buffer_load_dwordx4 v56, s[36:39], s56 offen offset:0 sc1 lds\n"
"v_mfma_f32_16x16x32_bf16 v[96:99], v[0:3], a[128:131], v[96:99]\n"
"v_mfma_f32_16x16x32_bf16 v[96:99], v[4:7], a[132:135], v[96:99]\n"
"buffer_load_dwordx4 v56, s[36:39], s56 offen offset:1024 sc1 lds\n"
"v_mfma_f32_16x16x32_bf16 v[104:107], v[0:3], a[136:139], v[104:107]\n"
"v_mfma_f32_16x16x32_bf16 v[104:107], v[4:7], a[140:143], v[104:107]\n"
"s_waitcnt lgkmcnt(3)\n"
"v_mfma_f32_16x16x32_bf16 v[68:71], v[8:11], a[96:99], v[68:71]\n"
"ds_read_b64_tr_b16 v[0:1], v20 offset:512\n"
"v_mfma_f32_16x16x32_bf16 v[68:71], v[12:15], a[100:103], v[68:71]\n"
"ds_read_b64_tr_b16 v[2:3], v20 offset:2560\n"
"v_mfma_f32_16x16x32_bf16 v[76:79], v[8:11], a[104:107], v[76:79]\n"
"ds_read_b64_tr_b16 v[4:5], v20 offset:4608\n"
"v_mfma_f32_16x16x32_bf16 v[76:79], v[12:15], a[108:111], v[76:79]\n"
"ds_read_b64_tr_b16 v[6:7], v20 offset:6656\n"
"v_mfma_f32_16x16x32_bf16 v[84:87], v[8:11], a[112:115], v[84:87]\n"
"ds_read_b128 a[168:171], v17 offset:44032\n"
"v_mfma_f32_16x16x32_bf16 v[84:87], v[12:15], a[116:119], v[84:87]\n"
"ds_read_b128 a[172:175], v17 offset:50176\n"
"v_mfma_f32_16x16x32_bf16 v[92:95], v[8:11], a[120:123], v[92:95]\n"
"ds_read_b128 a[176:179], v17 offset:45056\n"
"v_mfma_f32_16x16x32_bf16 v[92:95], v[12:15], a[124:127], v[92:95]\n"
"buffer_load_dwordx4 v56, s[36:39], s56 offen offset:2048 sc1 lds\n"
"v_mfma_f32_16x16x32_bf16 v[100:103], v[8:11], a[128:131], v[100:103]\n"
"s_cmp_le_u32 s80, 9\n"
"s_cselect_b32 s66, 13, 9\n"
"s_add_u32 s66, s66, s82\n"
"s_lshl_b32 s66, s66, 17\n"
"s_or_b32 s66, s66, s81\n"
"s_xor_b32 s66, s66, 128\n"
"s_add_u32 m0, s68, 40960\n"
"s_nop 0\n"
"buffer_load_dwordx4 v44, s[40:43], s66 offen offset:0 lds\n"
"v_mfma_f32_16x16x32_bf16 v[100:103], v[12:15], a[132:135], v[100:103]\n"
"v_mfma_f32_16x16x32_bf16 v[108:111], v[8:11], a[136:139], v[108:111]\n"
"buffer_load_dwordx4 v45, s[40:43], s66 offen offset:1024 lds\n"
"v_mfma_f32_16x16x32_bf16 v[108:111], v[12:15], a[140:143], v[108:111]\n"
"s_waitcnt lgkmcnt(3)\n"
"v_mfma_f32_16x16x32_bf16 v[112:115], v[0:3], a[96:99], v[112:115]\n"
"ds_read_b64_tr_b16 v[8:9], v21 offset:512\n"
"v_mfma_f32_16x16x32_bf16 v[112:115], v[4:7], a[100:103], v[112:115]\n"
"ds_read_b64_tr_b16 v[10:11], v21 offset:2560\n"
"v_mfma_f32_16x16x32_bf16 v[120:123], v[0:3], a[104:107], v[120:123]\n"
"ds_read_b64_tr_b16 v[12:13], v21 offset:4608\n"
"v_mfma_f32_16x16x32_bf16 v[120:123], v[4:7], a[108:111], v[120:123]\n"
"ds_read_b64_tr_b16 v[14:15], v21 offset:6656\n"
"v_mfma_f32_16x16x32_bf16 v[128:131], v[0:3], a[112:115], v[128:131]\n"
"ds_read_b128 a[180:183], v17 offset:51200\n"
"v_mfma_f32_16x16x32_bf16 v[128:131], v[4:7], a[116:119], v[128:131]\n"
"ds_read_b128 a[184:187], v17 offset:46080\n"
"v_mfma_f32_16x16x32_bf16 v[136:139], v[0:3], a[120:123], v[136:139]\n"
"ds_read_b128 a[188:191], v17 offset:52224\n"
"v_mfma_f32_16x16x32_bf16 v[136:139], v[4:7], a[124:127], v[136:139]\n"
"buffer_load_dwordx4 v46, s[40:43], s66 offen offset:2048 lds\n"
"v_mfma_f32_16x16x32_bf16 v[144:147], v[0:3], a[128:131], v[144:147]\n"
"v_mfma_f32_16x16x32_bf16 v[144:147], v[4:7], a[132:135], v[144:147]\n"
"buffer_load_dwordx4 v47, s[40:43], s66 offen offset:3072 lds\n"
"v_mfma_f32_16x16x32_bf16 v[152:155], v[0:3], a[136:139], v[152:155]\n"
"v_mfma_f32_16x16x32_bf16 v[152:155], v[4:7], a[140:143], v[152:155]\n"
"s_waitcnt vmcnt(20) lgkmcnt(0)\n"
"s_barrier\n"
"v_mfma_f32_16x16x32_bf16 v[116:119], v[8:11], a[96:99], v[116:119]\n"
"ds_read_b64_tr_b16 v[0:1], v20 offset:40960\n"
"v_mfma_f32_16x16x32_bf16 v[116:119], v[12:15], a[100:103], v[116:119]\n"
"ds_read_b64_tr_b16 v[2:3], v20 offset:43008\n"
"v_not_b32 v40, s88\n"
"v_not_b32 v41, s89\n"
"v_mfma_f32_16x16x32_bf16 v[124:127], v[8:11], a[104:107], v[124:127]\n"
"s_cmp_le_u32 s80, 7\n"
"ds_read_b64_tr_b16 v[4:5], v20 offset:45056\n"
"v_mfma_f32_16x16x32_bf16 v[124:127], v[12:15], a[108:111], v[124:127]\n"
"s_cselect_b32 s56, 11, 7\n"
"ds_read_b64_tr_b16 v[6:7], v20 offset:47104\n"
"v_mfma_f32_16x16x32_bf16 v[132:135], v[8:11], a[112:115], v[132:135]\n"
"s_add_u32 s56, s56, s82\n"
"v_mfma_f32_16x16x32_bf16 v[132:135], v[12:15], a[116:119], v[132:135]\n"
"s_mul_i32 s56, s56, 12288\n"
"v_mfma_f32_16x16x32_bf16 v[140:143], v[8:11], a[120:123], v[140:143]\n"
"s_add_u32 m0, s67, 122880\n"
"v_mfma_f32_16x16x32_bf16 v[140:143], v[12:15], a[124:127], v[140:143]\n"
"buffer_load_dwordx4 v56, s[36:39], s56 offen offset:0 sc1 lds\n"
"v_mfma_f32_16x16x32_bf16 v[148:151], v[8:11], a[128:131], v[148:151]\n"
"buffer_load_dwordx4 v56, s[36:39], s56 offen offset:1024 sc1 lds\n"
"v_mfma_f32_16x16x32_bf16 v[148:151], v[12:15], a[132:135], v[148:151]\n"
"v_mfma_f32_16x16x32_bf16 v[156:159], v[8:11], a[136:139], v[156:159]\n"
"buffer_load_dwordx4 v56, s[36:39], s56 offen offset:2048 sc1 lds\n"
"v_mfma_f32_16x16x32_bf16 v[156:159], v[12:15], a[140:143], v[156:159]\n"
"s_waitcnt lgkmcnt(0)\n"
"v_mfma_f32_16x16x32_bf16 v[64:67], v[0:3], a[144:147], v[64:67]\n"
"ds_read_b64_tr_b16 v[8:9], v21 offset:40960\n"
"v_mfma_f32_16x16x32_bf16 v[64:67], v[4:7], a[148:151], v[64:67]\n"
"s_cmp_le_u32 s80, 7\n"
"ds_read_b64_tr_b16 v[10:11], v21 offset:43008\n"
"v_mfma_f32_16x16x32_bf16 v[72:75], v[0:3], a[152:155], v[72:75]\n"
"s_cselect_b32 s56, 11, 7\n"
"ds_read_b64_tr_b16 v[12:13], v21 offset:45056\n"
"v_mfma_f32_16x16x32_bf16 v[72:75], v[4:7], a[156:159], v[72:75]\n"
"s_add_u32 s56, s56, s82\n"
"ds_read_b64_tr_b16 v[14:15], v21 offset:47104\n"
"v_mfma_f32_16x16x32_bf16 v[80:83], v[0:3], a[160:163], v[80:83]\n"
"s_mul_i32 s56, s56, 12288\n"
"v_mfma_f32_16x16x32_bf16 v[80:83], v[4:7], a[164:167], v[80:83]\n"
"s_add_u32 s56, s56, 3072\n"
"v_mfma_f32_16x16x32_bf16 v[88:91], v[0:3], a[168:171], v[88:91]\n"
"s_add_u32 m0, s67, 125952\n"
"v_mfma_f32_16x16x32_bf16 v[88:91], v[4:7], a[172:175], v[88:91]\n"
"buffer_load_dwordx4 v56, s[36:39], s56 offen offset:0 sc1 lds\n"
"v_mfma_f32_16x16x32_bf16 v[96:99], v[0:3], a[176:179], v[96:99]\n"
"v_mfma_f32_16x16x32_bf16 v[96:99], v[4:7], a[180:183], v[96:99]\n"
"buffer_load_dwordx4 v56, s[36:39], s56 offen offset:1024 sc1 lds\n"
"v_mfma_f32_16x16x32_bf16 v[104:107], v[0:3], a[184:187], v[104:107]\n"
"v_mfma_f32_16x16x32_bf16 v[104:107], v[4:7], a[188:191], v[104:107]\n"
"s_waitcnt lgkmcnt(0)\n"
"v_mfma_f32_16x16x32_bf16 v[68:71], v[8:11], a[144:147], v[68:71]\n"
"ds_read_b64_tr_b16 v[0:1], v20 offset:41472\n"
"v_mfma_f32_16x16x32_bf16 v[68:71], v[12:15], a[148:151], v[68:71]\n"
"ds_read_b64_tr_b16 v[2:3], v20 offset:43520\n"
"v_mfma_f32_16x16x32_bf16 v[76:79], v[8:11], a[152:155], v[76:79]\n"
"ds_read_b64_tr_b16 v[4:5], v20 offset:45568\n"
"v_mfma_f32_16x16x32_bf16 v[76:79], v[12:15], a[156:159], v[76:79]\n"
"ds_read_b64_tr_b16 v[6:7], v20 offset:47616\n"
"v_mfma_f32_16x16x32_bf16 v[84:87], v[8:11], a[160:163], v[84:87]\n"
"v_mfma_f32_16x16x32_bf16 v[84:87], v[12:15], a[164:167], v[84:87]\n"
"v_mfma_f32_16x16x32_bf16 v[92:95], v[8:11], a[168:171], v[92:95]\n"
"v_mfma_f32_16x16x32_bf16 v[92:95], v[12:15], a[172:175], v[92:95]\n"
"buffer_load_dwordx4 v56, s[36:39], s56 offen offset:2048 sc1 lds\n"
"v_mfma_f32_16x16x32_bf16 v[100:103], v[8:11], a[176:179], v[100:103]\n"
"s_cmp_le_u32 s80, 10\n"
"s_cselect_b32 s66, 14, 10\n"
"s_add_u32 s66, s66, s82\n"
"s_lshl_b32 s66, s66, 17\n"
"s_or_b32 s66, s66, s81\n"
"s_xor_b32 s66, s66, 128\n"
"s_add_u32 m0, s68, 81920\n"
"s_nop 0\n"
"buffer_load_dwordx4 v44, s[40:43], s66 offen offset:0 lds\n"
"v_mfma_f32_16x16x32_bf16 v[100:103], v[12:15], a[180:183], v[100:103]\n"
"v_mfma_f32_16x16x32_bf16 v[108:111], v[8:11], a[184:187], v[108:111]\n"
"buffer_load_dwordx4 v45, s[40:43], s66 offen offset:1024 lds\n"
"v_mfma_f32_16x16x32_bf16 v[108:111], v[12:15], a[188:191], v[108:111]\n"
"s_waitcnt lgkmcnt(0)\n"
"v_mfma_f32_16x16x32_bf16 v[112:115], v[0:3], a[144:147], v[112:115]\n"
"ds_read_b64_tr_b16 v[8:9], v21 offset:41472\n"
"v_mfma_f32_16x16x32_bf16 v[112:115], v[4:7], a[148:151], v[112:115]\n"
"ds_read_b64_tr_b16 v[10:11], v21 offset:43520\n"
"v_mfma_f32_16x16x32_bf16 v[120:123], v[0:3], a[152:155], v[120:123]\n"
"ds_read_b64_tr_b16 v[12:13], v21 offset:45568\n"
"v_mfma_f32_16x16x32_bf16 v[120:123], v[4:7], a[156:159], v[120:123]\n"
"ds_read_b64_tr_b16 v[14:15], v21 offset:47616\n"
"v_mfma_f32_16x16x32_bf16 v[128:131], v[0:3], a[160:163], v[128:131]\n"
"v_mfma_f32_16x16x32_bf16 v[128:131], v[4:7], a[164:167], v[128:131]\n"
"v_mfma_f32_16x16x32_bf16 v[136:139], v[0:3], a[168:171], v[136:139]\n"
"v_mfma_f32_16x16x32_bf16 v[136:139], v[4:7], a[172:175], v[136:139]\n"
"buffer_load_dwordx4 v46, s[40:43], s66 offen offset:2048 lds\n"
"v_mfma_f32_16x16x32_bf16 v[144:147], v[0:3], a[176:179], v[144:147]\n"
"v_mfma_f32_16x16x32_bf16 v[144:147], v[4:7], a[180:183], v[144:147]\n"
"buffer_load_dwordx4 v47, s[40:43], s66 offen offset:3072 lds\n"
"v_mfma_f32_16x16x32_bf16 v[152:155], v[0:3], a[184:187], v[152:155]\n"
"v_mfma_f32_16x16x32_bf16 v[152:155], v[4:7], a[188:191], v[152:155]\n"
"s_waitcnt vmcnt(20) lgkmcnt(0)\n"
"s_barrier\n"
"v_mfma_f32_16x16x32_bf16 v[116:119], v[8:11], a[144:147], v[116:119]\n"
"s_cmp_le_u32 s80, 11\n"
"ds_read_b64_tr_b16 v[0:1], v18 offset:0\n"
"v_mfma_f32_16x16x32_bf16 v[116:119], v[12:15], a[148:151], v[116:119]\n"
"s_cselect_b32 s66, 15, 11\n"
"ds_read_b64_tr_b16 v[2:3], v18 offset:2048\n"
"v_mfma_f32_16x16x32_bf16 v[124:127], v[8:11], a[152:155], v[124:127]\n"
"s_add_u32 s66, s66, s82\n"
"ds_read_b64_tr_b16 v[4:5], v18 offset:4096\n"
"v_cvt_pk_bf16_f32 v64, v64, v65\n"
"v_mfma_f32_16x16x32_bf16 v[124:127], v[12:15], a[156:159], v[124:127]\n"
"s_lshl_b32 s66, s66, 17\n"
"ds_read_b64_tr_b16 v[6:7], v18 offset:6144\n"
"v_cvt_pk_bf16_f32 v65, v66, v67\n"
"v_mfma_f32_16x16x32_bf16 v[132:135], v[8:11], a[160:163], v[132:135]\n"
"s_or_b32 s66, s66, s81\n"
"v_cvt_pk_bf16_f32 v66, v68, v69\n"
"v_cvt_pk_bf16_f32 v67, v70, v71\n"
"v_mfma_f32_16x16x32_bf16 v[132:135], v[12:15], a[164:167], v[132:135]\n"
"s_add_u32 s65, s85, 0\n"
"buffer_store_dwordx4 v[64:67], v56, s[96:99], s65 offen offset:0 sc1\n"
"s_xor_b32 s66, s66, 128\n"
"v_mfma_f32_16x16x32_bf16 v[140:143], v[8:11], a[168:171], v[140:143]\n"
"s_add_u32 m0, s68, 122880\n"
"v_mfma_f32_16x16x32_bf16 v[140:143], v[12:15], a[172:175], v[140:143]\n"
"buffer_load_dwordx4 v44, s[40:43], s66 offen offset:0 lds\n"
"v_mfma_f32_16x16x32_bf16 v[148:151], v[8:11], a[176:179], v[148:151]\n"
"v_mfma_f32_16x16x32_bf16 v[148:151], v[12:15], a[180:183], v[148:151]\n"
"v_mfma_f32_16x16x32_bf16 v[156:159], v[8:11], a[184:187], v[156:159]\n"
"v_mfma_f32_16x16x32_bf16 v[156:159], v[12:15], a[188:191], v[156:159]\n"
"s_waitcnt lgkmcnt(0)\n"
"v_mfma_f32_16x16x32_bf16 v[160:163], v[0:3], a[0:3], v[160:163]\n"
"ds_read_b64_tr_b16 v[8:9], v19 offset:0\n"
"v_cvt_pk_bf16_f32 v72, v72, v73\n"
"v_mfma_f32_16x16x32_bf16 v[160:163], v[4:7], a[4:7], v[160:163]\n"
"ds_read_b64_tr_b16 v[10:11], v19 offset:2048\n"
"v_cvt_pk_bf16_f32 v73, v74, v75\n"
"v_mfma_f32_16x16x32_bf16 v[168:171], v[0:3], a[8:11], v[168:171]\n"
"ds_read_b64_tr_b16 v[12:13], v19 offset:4096\n"
"v_cvt_pk_bf16_f32 v74, v76, v77\n"
"v_mfma_f32_16x16x32_bf16 v[168:171], v[4:7], a[12:15], v[168:171]\n"
"ds_read_b64_tr_b16 v[14:15], v19 offset:6144\n"
"v_cvt_pk_bf16_f32 v75, v78, v79\n"
"v_mfma_f32_16x16x32_bf16 v[176:179], v[0:3], a[16:19], v[176:179]\n"
"buffer_store_dwordx4 v[72:75], v56, s[96:99], s65 offen offset:1024 sc1\n"
"v_mfma_f32_16x16x32_bf16 v[176:179], v[4:7], a[20:23], v[176:179]\n"
"v_mfma_f32_16x16x32_bf16 v[184:187], v[0:3], a[24:27], v[184:187]\n"
"v_mfma_f32_16x16x32_bf16 v[184:187], v[4:7], a[28:31], v[184:187]\n"
"buffer_load_dwordx4 v45, s[40:43], s66 offen offset:1024 lds\n"
"v_mfma_f32_16x16x32_bf16 v[192:195], v[0:3], a[32:35], v[192:195]\n"
"v_mfma_f32_16x16x32_bf16 v[192:195], v[4:7], a[36:39], v[192:195]\n"
"v_mfma_f32_16x16x32_bf16 v[200:203], v[0:3], a[40:43], v[200:203]\n"
"v_mfma_f32_16x16x32_bf16 v[200:203], v[4:7], a[44:47], v[200:203]\n"
"v_cvt_pk_bf16_f32 v80, v80, v81\n"
"v_cvt_pk_bf16_f32 v81, v82, v83\n"
"s_waitcnt lgkmcnt(0)\n"
"v_mfma_f32_16x16x32_bf16 v[164:167], v[8:11], a[0:3], v[164:167]\n"
"ds_read_b64_tr_b16 v[0:1], v18 offset:512\n"
"v_cvt_pk_bf16_f32 v82, v84, v85\n"
"v_mfma_f32_16x16x32_bf16 v[164:167], v[12:15], a[4:7], v[164:167]\n"
"ds_read_b64_tr_b16 v[2:3], v18 offset:2560\n"
"v_cvt_pk_bf16_f32 v83, v86, v87\n"
"v_mfma_f32_16x16x32_bf16 v[172:175], v[8:11], a[8:11], v[172:175]\n"
"ds_read_b64_tr_b16 v[4:5], v18 offset:4608\n"
"v_mfma_f32_16x16x32_bf16 v[172:175], v[12:15], a[12:15], v[172:175]\n"
"ds_read_b64_tr_b16 v[6:7], v18 offset:6656\n"
"v_mfma_f32_16x16x32_bf16 v[180:183], v[8:11], a[16:19], v[180:183]\n"
"buffer_store_dwordx4 v[80:83], v56, s[96:99], s65 offen offset:2048 sc1\n"
"v_mfma_f32_16x16x32_bf16 v[180:183], v[12:15], a[20:23], v[180:183]\n"
"v_mfma_f32_16x16x32_bf16 v[188:191], v[8:11], a[24:27], v[188:191]\n"
"v_mfma_f32_16x16x32_bf16 v[188:191], v[12:15], a[28:31], v[188:191]\n"
"buffer_load_dwordx4 v46, s[40:43], s66 offen offset:2048 lds\n"
"v_mfma_f32_16x16x32_bf16 v[196:199], v[8:11], a[32:35], v[196:199]\n"
"v_mfma_f32_16x16x32_bf16 v[196:199], v[12:15], a[36:39], v[196:199]\n"
"v_mfma_f32_16x16x32_bf16 v[204:207], v[8:11], a[40:43], v[204:207]\n"
"v_mfma_f32_16x16x32_bf16 v[204:207], v[12:15], a[44:47], v[204:207]\n"
"v_cvt_pk_bf16_f32 v88, v88, v89\n"
"v_cvt_pk_bf16_f32 v89, v90, v91\n"
"s_waitcnt lgkmcnt(0)\n"
"v_mfma_f32_16x16x32_bf16 v[208:211], v[0:3], a[0:3], v[208:211]\n"
"ds_read_b64_tr_b16 v[8:9], v19 offset:512\n"
"v_cvt_pk_bf16_f32 v90, v92, v93\n"
"v_mfma_f32_16x16x32_bf16 v[208:211], v[4:7], a[4:7], v[208:211]\n"
"ds_read_b64_tr_b16 v[10:11], v19 offset:2560\n"
"v_cvt_pk_bf16_f32 v91, v94, v95\n"
"v_mfma_f32_16x16x32_bf16 v[216:219], v[0:3], a[8:11], v[216:219]\n"
"ds_read_b64_tr_b16 v[12:13], v19 offset:4608\n"
"v_mfma_f32_16x16x32_bf16 v[216:219], v[4:7], a[12:15], v[216:219]\n"
"ds_read_b64_tr_b16 v[14:15], v19 offset:6656\n"
"v_mfma_f32_16x16x32_bf16 v[224:227], v[0:3], a[16:19], v[224:227]\n"
"buffer_store_dwordx4 v[88:91], v56, s[96:99], s65 offen offset:3072 sc1\n"
"v_mfma_f32_16x16x32_bf16 v[224:227], v[4:7], a[20:23], v[224:227]\n"
"v_mfma_f32_16x16x32_bf16 v[232:235], v[0:3], a[24:27], v[232:235]\n"
"v_mfma_f32_16x16x32_bf16 v[232:235], v[4:7], a[28:31], v[232:235]\n"
"buffer_load_dwordx4 v47, s[40:43], s66 offen offset:3072 lds\n"
"v_mfma_f32_16x16x32_bf16 v[240:243], v[0:3], a[32:35], v[240:243]\n"
"v_mfma_f32_16x16x32_bf16 v[240:243], v[4:7], a[36:39], v[240:243]\n"
"v_mfma_f32_16x16x32_bf16 v[248:251], v[0:3], a[40:43], v[248:251]\n"
"v_mfma_f32_16x16x32_bf16 v[248:251], v[4:7], a[44:47], v[248:251]\n"
"v_cvt_pk_bf16_f32 v96, v96, v97\n"
"v_cvt_pk_bf16_f32 v97, v98, v99\n"
"s_waitcnt vmcnt(18) lgkmcnt(0)\n"
"s_barrier\n"
"v_mfma_f32_16x16x32_bf16 v[212:215], v[8:11], a[0:3], v[212:215]\n"
"s_cmp_le_u32 s80, 4\n"
"ds_read_b64_tr_b16 v[0:1], v18 offset:40960\n"
"v_cvt_pk_bf16_f32 v98, v100, v101\n"
"v_mfma_f32_16x16x32_bf16 v[212:215], v[12:15], a[4:7], v[212:215]\n"
"s_cselect_b32 s66, 8, 4\n"
"ds_read_b64_tr_b16 v[2:3], v18 offset:43008\n"
"v_cvt_pk_bf16_f32 v99, v102, v103\n"
"v_mfma_f32_16x16x32_bf16 v[220:223], v[8:11], a[8:11], v[220:223]\n"
"s_add_u32 s66, s66, s82\n"
"ds_read_b64_tr_b16 v[4:5], v18 offset:45056\n"
"v_mfma_f32_16x16x32_bf16 v[220:223], v[12:15], a[12:15], v[220:223]\n"
"s_lshl_b32 s66, s66, 17\n"
"ds_read_b64_tr_b16 v[6:7], v18 offset:47104\n"
"v_mfma_f32_16x16x32_bf16 v[228:231], v[8:11], a[16:19], v[228:231]\n"
"s_add_u32 s65, s85, 4096\n"
"buffer_store_dwordx4 v[96:99], v56, s[96:99], s65 offen offset:0 sc1\n"
"s_or_b32 s66, s66, s81\n"
"v_mfma_f32_16x16x32_bf16 v[228:231], v[12:15], a[20:23], v[228:231]\n"
"s_xor_b32 s66, s66, 128\n"
"v_mfma_f32_16x16x32_bf16 v[236:239], v[8:11], a[24:27], v[236:239]\n"
"s_add_u32 m0, s68, 0\n"
"v_mfma_f32_16x16x32_bf16 v[236:239], v[12:15], a[28:31], v[236:239]\n"
"buffer_load_dwordx4 v44, s[40:43], s66 offen offset:0 lds\n"
"v_mfma_f32_16x16x32_bf16 v[244:247], v[8:11], a[32:35], v[244:247]\n"
"v_mfma_f32_16x16x32_bf16 v[244:247], v[12:15], a[36:39], v[244:247]\n"
"v_mfma_f32_16x16x32_bf16 v[252:255], v[8:11], a[40:43], v[252:255]\n"
"v_mfma_f32_16x16x32_bf16 v[252:255], v[12:15], a[44:47], v[252:255]\n"
"v_cvt_pk_bf16_f32 v104, v104, v105\n"
"v_cvt_pk_bf16_f32 v105, v106, v107\n"
"s_waitcnt lgkmcnt(0)\n"
"v_mfma_f32_16x16x32_bf16 v[160:163], v[0:3], a[48:51], v[160:163]\n"
"ds_read_b64_tr_b16 v[8:9], v19 offset:40960\n"
"v_cvt_pk_bf16_f32 v106, v108, v109\n"
"v_mfma_f32_16x16x32_bf16 v[160:163], v[4:7], a[52:55], v[160:163]\n"
"ds_read_b64_tr_b16 v[10:11], v19 offset:43008\n"
"v_cvt_pk_bf16_f32 v107, v110, v111\n"
"v_mfma_f32_16x16x32_bf16 v[168:171], v[0:3], a[56:59], v[168:171]\n"
"ds_read_b64_tr_b16 v[12:13], v19 offset:45056\n"
"v_mfma_f32_16x16x32_bf16 v[168:171], v[4:7], a[60:63], v[168:171]\n"
"ds_read_b64_tr_b16 v[14:15], v19 offset:47104\n"
"v_mfma_f32_16x16x32_bf16 v[176:179], v[0:3], a[64:67], v[176:179]\n"
"buffer_store_dwordx4 v[104:107], v56, s[96:99], s65 offen offset:1024 sc1\n"
"v_mfma_f32_16x16x32_bf16 v[176:179], v[4:7], a[68:71], v[176:179]\n"
"v_mfma_f32_16x16x32_bf16 v[184:187], v[0:3], a[72:75], v[184:187]\n"
"v_mfma_f32_16x16x32_bf16 v[184:187], v[4:7], a[76:79], v[184:187]\n"
"buffer_load_dwordx4 v45, s[40:43], s66 offen offset:1024 lds\n"
"v_mfma_f32_16x16x32_bf16 v[192:195], v[0:3], a[80:83], v[192:195]\n"
"v_mfma_f32_16x16x32_bf16 v[192:195], v[4:7], a[84:87], v[192:195]\n"
"v_mfma_f32_16x16x32_bf16 v[200:203], v[0:3], a[88:91], v[200:203]\n"
"v_mfma_f32_16x16x32_bf16 v[200:203], v[4:7], a[92:95], v[200:203]\n"
"v_cvt_pk_bf16_f32 v112, v112, v113\n"
"v_cvt_pk_bf16_f32 v113, v114, v115\n"
"s_waitcnt lgkmcnt(0)\n"
"v_mfma_f32_16x16x32_bf16 v[164:167], v[8:11], a[48:51], v[164:167]\n"
"ds_read_b64_tr_b16 v[0:1], v18 offset:41472\n"
"v_cvt_pk_bf16_f32 v114, v116, v117\n"
"v_mfma_f32_16x16x32_bf16 v[164:167], v[12:15], a[52:55], v[164:167]\n"
"ds_read_b64_tr_b16 v[2:3], v18 offset:43520\n"
"v_cvt_pk_bf16_f32 v115, v118, v119\n"
"v_mfma_f32_16x16x32_bf16 v[172:175], v[8:11], a[56:59], v[172:175]\n"
"ds_read_b64_tr_b16 v[4:5], v18 offset:45568\n"
"v_mfma_f32_16x16x32_bf16 v[172:175], v[12:15], a[60:63], v[172:175]\n"
"ds_read_b64_tr_b16 v[6:7], v18 offset:47616\n"
"v_mfma_f32_16x16x32_bf16 v[180:183], v[8:11], a[64:67], v[180:183]\n"
"s_add_u32 s65, s86, 0\n"
"buffer_store_dwordx4 v[112:115], v56, s[96:99], s65 offen offset:0 sc1\n"
"v_mfma_f32_16x16x32_bf16 v[180:183], v[12:15], a[68:71], v[180:183]\n"
"v_mfma_f32_16x16x32_bf16 v[188:191], v[8:11], a[72:75], v[188:191]\n"
"v_mfma_f32_16x16x32_bf16 v[188:191], v[12:15], a[76:79], v[188:191]\n"
"buffer_load_dwordx4 v46, s[40:43], s66 offen offset:2048 lds\n"
"v_mfma_f32_16x16x32_bf16 v[196:199], v[8:11], a[80:83], v[196:199]\n"
"v_mfma_f32_16x16x32_bf16 v[196:199], v[12:15], a[84:87], v[196:199]\n"
"v_mfma_f32_16x16x32_bf16 v[204:207], v[8:11], a[88:91], v[204:207]\n"
"v_mfma_f32_16x16x32_bf16 v[204:207], v[12:15], a[92:95], v[204:207]\n"
"v_cvt_pk_bf16_f32 v120, v120, v121\n"
"v_cvt_pk_bf16_f32 v121, v122, v123\n"
"s_waitcnt lgkmcnt(0)\n"
"v_mfma_f32_16x16x32_bf16 v[208:211], v[0:3], a[48:51], v[208:211]\n"
"ds_read_b64_tr_b16 v[8:9], v19 offset:41472\n"
"v_cvt_pk_bf16_f32 v122, v124, v125\n"
"v_mfma_f32_16x16x32_bf16 v[208:211], v[4:7], a[52:55], v[208:211]\n"
"ds_read_b64_tr_b16 v[10:11], v19 offset:43520\n"
"v_cvt_pk_bf16_f32 v123, v126, v127\n"
"v_mfma_f32_16x16x32_bf16 v[216:219], v[0:3], a[56:59], v[216:219]\n"
"ds_read_b64_tr_b16 v[12:13], v19 offset:45568\n"
"v_mfma_f32_16x16x32_bf16 v[216:219], v[4:7], a[60:63], v[216:219]\n"
"ds_read_b64_tr_b16 v[14:15], v19 offset:47616\n"
"v_mfma_f32_16x16x32_bf16 v[224:227], v[0:3], a[64:67], v[224:227]\n"
"buffer_store_dwordx4 v[120:123], v56, s[96:99], s65 offen offset:1024 sc1\n"
"v_mfma_f32_16x16x32_bf16 v[224:227], v[4:7], a[68:71], v[224:227]\n"
"v_mfma_f32_16x16x32_bf16 v[232:235], v[0:3], a[72:75], v[232:235]\n"
"v_mfma_f32_16x16x32_bf16 v[232:235], v[4:7], a[76:79], v[232:235]\n"
"buffer_load_dwordx4 v47, s[40:43], s66 offen offset:3072 lds\n"
"v_mfma_f32_16x16x32_bf16 v[240:243], v[0:3], a[80:83], v[240:243]\n"
"v_mfma_f32_16x16x32_bf16 v[240:243], v[4:7], a[84:87], v[240:243]\n"
"v_mfma_f32_16x16x32_bf16 v[248:251], v[0:3], a[88:91], v[248:251]\n"
"v_mfma_f32_16x16x32_bf16 v[248:251], v[4:7], a[92:95], v[248:251]\n"
"v_cvt_pk_bf16_f32 v128, v128, v129\n"
"v_cvt_pk_bf16_f32 v129, v130, v131\n"
"s_waitcnt vmcnt(50)\n"
"s_waitcnt vmcnt(16) lgkmcnt(0)\n"
"s_barrier\n"
"v_mfma_f32_16x16x32_bf16 v[212:215], v[8:11], a[48:51], v[212:215]\n"
"s_cmp_le_u32 s80, 5\n"
"ds_read_b64_tr_b16 v[0:1], v20 offset:0\n"
"v_cvt_pk_bf16_f32 v130, v132, v133\n"
"v_mfma_f32_16x16x32_bf16 v[212:215], v[12:15], a[52:55], v[212:215]\n"
"s_cselect_b32 s66, 9, 5\n"
"ds_read_b64_tr_b16 v[2:3], v20 offset:2048\n"
"v_cvt_pk_bf16_f32 v131, v134, v135\n"
"v_mfma_f32_16x16x32_bf16 v[220:223], v[8:11], a[56:59], v[220:223]\n"
"s_add_u32 s66, s66, s82\n"
"ds_read_b64_tr_b16 v[4:5], v20 offset:4096\n"
"v_mfma_f32_16x16x32_bf16 v[220:223], v[12:15], a[60:63], v[220:223]\n"
"s_lshl_b32 s66, s66, 17\n"
"ds_read_b64_tr_b16 v[6:7], v20 offset:6144\n"
"v_mfma_f32_16x16x32_bf16 v[228:231], v[8:11], a[64:67], v[228:231]\n"
"s_or_b32 s66, s66, s81\n"
"ds_read_b128 a[0:3], v16 offset:0\n"
"v_mfma_f32_16x16x32_bf16 v[228:231], v[12:15], a[68:71], v[228:231]\n"
"s_xor_b32 s66, s66, 128\n"
"ds_read_b128 a[4:7], v16 offset:6144\n"
"v_mfma_f32_16x16x32_bf16 v[236:239], v[8:11], a[72:75], v[236:239]\n"
"s_add_u32 m0, s68, 40960\n"
"ds_read_b128 a[8:11], v16 offset:1024\n"
"v_mfma_f32_16x16x32_bf16 v[236:239], v[12:15], a[76:79], v[236:239]\n"
"buffer_load_dwordx4 v44, s[40:43], s66 offen offset:0 lds\n"
"v_mfma_f32_16x16x32_bf16 v[244:247], v[8:11], a[80:83], v[244:247]\n"
"buffer_load_dwordx4 v45, s[40:43], s66 offen offset:1024 lds\n"
"v_mfma_f32_16x16x32_bf16 v[244:247], v[12:15], a[84:87], v[244:247]\n"
"buffer_load_dwordx4 v46, s[40:43], s66 offen offset:2048 lds\n"
"v_mfma_f32_16x16x32_bf16 v[252:255], v[8:11], a[88:91], v[252:255]\n"
"buffer_load_dwordx4 v47, s[40:43], s66 offen offset:3072 lds\n"
"v_mfma_f32_16x16x32_bf16 v[252:255], v[12:15], a[92:95], v[252:255]\n"
"buffer_store_dwordx4 v[128:131], v56, s[96:99], s65 offen offset:2048 sc1\n"
"s_waitcnt lgkmcnt(3)\n"
"v_mfma_f32_16x16x32_bf16 v[160:163], v[0:3], a[96:99], v[160:163]\n"
"ds_read_b64_tr_b16 v[8:9], v21 offset:0\n"
"v_mfma_f32_16x16x32_bf16 v[160:163], v[4:7], a[100:103], v[160:163]\n"
"ds_read_b64_tr_b16 v[10:11], v21 offset:2048\n"
"v_mfma_f32_16x16x32_bf16 v[168:171], v[0:3], a[104:107], v[168:171]\n"
"ds_read_b64_tr_b16 v[12:13], v21 offset:4096\n"
"v_mfma_f32_16x16x32_bf16 v[168:171], v[4:7], a[108:111], v[168:171]\n"
"ds_read_b64_tr_b16 v[14:15], v21 offset:6144\n"
"v_mfma_f32_16x16x32_bf16 v[176:179], v[0:3], a[112:115], v[176:179]\n"
"ds_read_b128 a[12:15], v16 offset:7168\n"
"v_mfma_f32_16x16x32_bf16 v[176:179], v[4:7], a[116:119], v[176:179]\n"
"ds_read_b128 a[16:19], v16 offset:2048\n"
"v_mfma_f32_16x16x32_bf16 v[184:187], v[0:3], a[120:123], v[184:187]\n"
"ds_read_b128 a[20:23], v16 offset:8192\n"
"v_cvt_pk_bf16_f32 v136, v136, v137\n"
"v_mfma_f32_16x16x32_bf16 v[184:187], v[4:7], a[124:127], v[184:187]\n"
"v_cvt_pk_bf16_f32 v137, v138, v139\n"
"v_cvt_pk_bf16_f32 v138, v140, v141\n"
"v_mfma_f32_16x16x32_bf16 v[192:195], v[0:3], a[128:131], v[192:195]\n"
"v_cvt_pk_bf16_f32 v139, v142, v143\n"
"v_mfma_f32_16x16x32_bf16 v[192:195], v[4:7], a[132:135], v[192:195]\n"
"buffer_store_dwordx4 v[136:139], v56, s[96:99], s65 offen offset:3072 sc1\n"
"v_mfma_f32_16x16x32_bf16 v[200:203], v[0:3], a[136:139], v[200:203]\n"
"v_mfma_f32_16x16x32_bf16 v[200:203], v[4:7], a[140:143], v[200:203]\n"
"s_waitcnt lgkmcnt(3)\n"
"v_mfma_f32_16x16x32_bf16 v[164:167], v[8:11], a[96:99], v[164:167]\n"
"ds_read_b64_tr_b16 v[0:1], v20 offset:512\n"
"v_mfma_f32_16x16x32_bf16 v[164:167], v[12:15], a[100:103], v[164:167]\n"
"ds_read_b64_tr_b16 v[2:3], v20 offset:2560\n"
"v_mfma_f32_16x16x32_bf16 v[172:175], v[8:11], a[104:107], v[172:175]\n"
"ds_read_b64_tr_b16 v[4:5], v20 offset:4608\n"
"v_mfma_f32_16x16x32_bf16 v[172:175], v[12:15], a[108:111], v[172:175]\n"
"ds_read_b64_tr_b16 v[6:7], v20 offset:6656\n"
"v_mfma_f32_16x16x32_bf16 v[180:183], v[8:11], a[112:115], v[180:183]\n"
"ds_read_b128 a[24:27], v16 offset:3072\n"
"v_cvt_pk_bf16_f32 v144, v144, v145\n"
"v_mfma_f32_16x16x32_bf16 v[180:183], v[12:15], a[116:119], v[180:183]\n"
"ds_read_b128 a[28:31], v16 offset:9216\n"
"v_cvt_pk_bf16_f32 v145, v146, v147\n"
"v_mfma_f32_16x16x32_bf16 v[188:191], v[8:11], a[120:123], v[188:191]\n"
"ds_read_b128 a[32:35], v16 offset:4096\n"
"v_cvt_pk_bf16_f32 v146, v148, v149\n"
"v_mfma_f32_16x16x32_bf16 v[188:191], v[12:15], a[124:127], v[188:191]\n"
"v_cvt_pk_bf16_f32 v147, v150, v151\n"
"v_mfma_f32_16x16x32_bf16 v[196:199], v[8:11], a[128:131], v[196:199]\n"
"s_add_u32 s65, s86, 4096\n"
"buffer_store_dwordx4 v[144:147], v56, s[96:99], s65 offen offset:0 sc1\n"
"v_mfma_f32_16x16x32_bf16 v[196:199], v[12:15], a[132:135], v[196:199]\n"
"v_mfma_f32_16x16x32_bf16 v[204:207], v[8:11], a[136:139], v[204:207]\n"
"v_mfma_f32_16x16x32_bf16 v[204:207], v[12:15], a[140:143], v[204:207]\n"
"s_waitcnt lgkmcnt(3)\n"
"v_mfma_f32_16x16x32_bf16 v[208:211], v[0:3], a[96:99], v[208:211]\n"
"ds_read_b64_tr_b16 v[8:9], v21 offset:512\n"
"v_mfma_f32_16x16x32_bf16 v[208:211], v[4:7], a[100:103], v[208:211]\n"
"ds_read_b64_tr_b16 v[10:11], v21 offset:2560\n"
"v_mfma_f32_16x16x32_bf16 v[216:219], v[0:3], a[104:107], v[216:219]\n"
"ds_read_b64_tr_b16 v[12:13], v21 offset:4608\n"
"v_mfma_f32_16x16x32_bf16 v[216:219], v[4:7], a[108:111], v[216:219]\n"
"ds_read_b64_tr_b16 v[14:15], v21 offset:6656\n"
"v_cvt_pk_bf16_f32 v152, v152, v153\n"
"v_mfma_f32_16x16x32_bf16 v[224:227], v[0:3], a[112:115], v[224:227]\n"
"ds_read_b128 a[36:39], v16 offset:10240\n"
"v_cvt_pk_bf16_f32 v153, v154, v155\n"
"v_mfma_f32_16x16x32_bf16 v[224:227], v[4:7], a[116:119], v[224:227]\n"
"ds_read_b128 a[40:43], v16 offset:5120\n"
"v_cvt_pk_bf16_f32 v154, v156, v157\n"
"v_mfma_f32_16x16x32_bf16 v[232:235], v[0:3], a[120:123], v[232:235]\n"
"ds_read_b128 a[44:47], v16 offset:11264\n"
"v_cvt_pk_bf16_f32 v155, v158, v159\n"
"v_mfma_f32_16x16x32_bf16 v[232:235], v[4:7], a[124:127], v[232:235]\n"
"buffer_store_dwordx4 v[152:155], v56, s[96:99], s65 offen offset:1024 sc1\n"
"v_mfma_f32_16x16x32_bf16 v[240:243], v[0:3], a[128:131], v[240:243]\n"
"v_mfma_f32_16x16x32_bf16 v[240:243], v[4:7], a[132:135], v[240:243]\n"
"v_mfma_f32_16x16x32_bf16 v[248:251], v[0:3], a[136:139], v[248:251]\n"
"v_mfma_f32_16x16x32_bf16 v[248:251], v[4:7], a[140:143], v[248:251]\n"
"s_waitcnt vmcnt(48)\n"
"s_waitcnt vmcnt(16) lgkmcnt(0)\n"
"s_barrier\n"
"v_mfma_f32_16x16x32_bf16 v[212:215], v[8:11], a[96:99], v[212:215]\n"
"s_cmp_le_u32 s80, 6\n"
"ds_read_b64_tr_b16 v[0:1], v20 offset:40960\n"
"v_mfma_f32_16x16x32_bf16 v[212:215], v[12:15], a[100:103], v[212:215]\n"
"s_cselect_b32 s66, 10, 6\n"
"ds_read_b64_tr_b16 v[2:3], v20 offset:43008\n"
"v_mfma_f32_16x16x32_bf16 v[220:223], v[8:11], a[104:107], v[220:223]\n"
"s_add_u32 s66, s66, s82\n"
"ds_read_b64_tr_b16 v[4:5], v20 offset:45056\n"
"v_mfma_f32_16x16x32_bf16 v[220:223], v[12:15], a[108:111], v[220:223]\n"
"s_lshl_b32 s66, s66, 17\n"
"ds_read_b64_tr_b16 v[6:7], v20 offset:47104\n"
"v_mfma_f32_16x16x32_bf16 v[228:231], v[8:11], a[112:115], v[228:231]\n"
"s_or_b32 s66, s66, s81\n"
"ds_read_b128 a[48:51], v16 offset:40960\n"
"v_mfma_f32_16x16x32_bf16 v[228:231], v[12:15], a[116:119], v[228:231]\n"
"s_xor_b32 s66, s66, 128\n"
"ds_read_b128 a[52:55], v16 offset:47104\n"
"v_mfma_f32_16x16x32_bf16 v[236:239], v[8:11], a[120:123], v[236:239]\n"
"s_add_u32 m0, s68, 81920\n"
"ds_read_b128 a[56:59], v16 offset:41984\n"
"v_mfma_f32_16x16x32_bf16 v[236:239], v[12:15], a[124:127], v[236:239]\n"
"buffer_load_dwordx4 v44, s[40:43], s66 offen offset:0 lds\n"
"v_mfma_f32_16x16x32_bf16 v[244:247], v[8:11], a[128:131], v[244:247]\n"
"buffer_load_dwordx4 v45, s[40:43], s66 offen offset:1024 lds\n"
"v_mfma_f32_16x16x32_bf16 v[244:247], v[12:15], a[132:135], v[244:247]\n"
"buffer_load_dwordx4 v46, s[40:43], s66 offen offset:2048 lds\n"
"v_mfma_f32_16x16x32_bf16 v[252:255], v[8:11], a[136:139], v[252:255]\n"
"buffer_load_dwordx4 v47, s[40:43], s66 offen offset:3072 lds\n"
"v_mfma_f32_16x16x32_bf16 v[252:255], v[12:15], a[140:143], v[252:255]\n"
"s_waitcnt lgkmcnt(3)\n"
"v_mfma_f32_16x16x32_bf16 v[160:163], v[0:3], a[144:147], v[160:163]\n"
"ds_read_b64_tr_b16 v[8:9], v21 offset:40960\n"
"v_mfma_f32_16x16x32_bf16 v[160:163], v[4:7], a[148:151], v[160:163]\n"
"ds_read_b64_tr_b16 v[10:11], v21 offset:43008\n"
"v_mfma_f32_16x16x32_bf16 v[168:171], v[0:3], a[152:155], v[168:171]\n"
"ds_read_b64_tr_b16 v[12:13], v21 offset:45056\n"
"v_mfma_f32_16x16x32_bf16 v[168:171], v[4:7], a[156:159], v[168:171]\n"
"ds_read_b64_tr_b16 v[14:15], v21 offset:47104\n"
"v_mfma_f32_16x16x32_bf16 v[176:179], v[0:3], a[160:163], v[176:179]\n"
"ds_read_b128 a[60:63], v16 offset:48128\n"
"v_mfma_f32_16x16x32_bf16 v[176:179], v[4:7], a[164:167], v[176:179]\n"
"ds_read_b128 a[64:67], v16 offset:43008\n"
"v_mfma_f32_16x16x32_bf16 v[184:187], v[0:3], a[168:171], v[184:187]\n"
"ds_read_b128 a[68:71], v16 offset:49152\n"
"v_mfma_f32_16x16x32_bf16 v[184:187], v[4:7], a[172:175], v[184:187]\n"
"v_mfma_f32_16x16x32_bf16 v[192:195], v[0:3], a[176:179], v[192:195]\n"
"v_mfma_f32_16x16x32_bf16 v[192:195], v[4:7], a[180:183], v[192:195]\n"
"v_mfma_f32_16x16x32_bf16 v[200:203], v[0:3], a[184:187], v[200:203]\n"
"v_mfma_f32_16x16x32_bf16 v[200:203], v[4:7], a[188:191], v[200:203]\n"
"s_waitcnt lgkmcnt(3)\n"
"v_mfma_f32_16x16x32_bf16 v[164:167], v[8:11], a[144:147], v[164:167]\n"
"ds_read_b64_tr_b16 v[0:1], v20 offset:41472\n"
"v_mfma_f32_16x16x32_bf16 v[164:167], v[12:15], a[148:151], v[164:167]\n"
"ds_read_b64_tr_b16 v[2:3], v20 offset:43520\n"
"v_mfma_f32_16x16x32_bf16 v[172:175], v[8:11], a[152:155], v[172:175]\n"
"ds_read_b64_tr_b16 v[4:5], v20 offset:45568\n"
"v_mfma_f32_16x16x32_bf16 v[172:175], v[12:15], a[156:159], v[172:175]\n"
"ds_read_b64_tr_b16 v[6:7], v20 offset:47616\n"
"v_mfma_f32_16x16x32_bf16 v[180:183], v[8:11], a[160:163], v[180:183]\n"
"ds_read_b128 a[72:75], v16 offset:44032\n"
"v_mfma_f32_16x16x32_bf16 v[180:183], v[12:15], a[164:167], v[180:183]\n"
"ds_read_b128 a[76:79], v16 offset:50176\n"
"v_mfma_f32_16x16x32_bf16 v[188:191], v[8:11], a[168:171], v[188:191]\n"
"ds_read_b128 a[80:83], v16 offset:45056\n"
"v_mfma_f32_16x16x32_bf16 v[188:191], v[12:15], a[172:175], v[188:191]\n"
"v_mfma_f32_16x16x32_bf16 v[196:199], v[8:11], a[176:179], v[196:199]\n"
"v_mfma_f32_16x16x32_bf16 v[196:199], v[12:15], a[180:183], v[196:199]\n"
"v_mfma_f32_16x16x32_bf16 v[204:207], v[8:11], a[184:187], v[204:207]\n"
"v_mfma_f32_16x16x32_bf16 v[204:207], v[12:15], a[188:191], v[204:207]\n"
"s_waitcnt lgkmcnt(3)\n"
"v_mfma_f32_16x16x32_bf16 v[208:211], v[0:3], a[144:147], v[208:211]\n"
"ds_read_b64_tr_b16 v[8:9], v21 offset:41472\n"
"v_mfma_f32_16x16x32_bf16 v[208:211], v[4:7], a[148:151], v[208:211]\n"
"ds_read_b64_tr_b16 v[10:11], v21 offset:43520\n"
"v_mfma_f32_16x16x32_bf16 v[216:219], v[0:3], a[152:155], v[216:219]\n"
"ds_read_b64_tr_b16 v[12:13], v21 offset:45568\n"
"v_mfma_f32_16x16x32_bf16 v[216:219], v[4:7], a[156:159], v[216:219]\n"
"ds_read_b64_tr_b16 v[14:15], v21 offset:47616\n"
"v_mfma_f32_16x16x32_bf16 v[224:227], v[0:3], a[160:163], v[224:227]\n"
"ds_read_b128 a[84:87], v16 offset:51200\n"
"v_mfma_f32_16x16x32_bf16 v[224:227], v[4:7], a[164:167], v[224:227]\n"
"ds_read_b128 a[88:91], v16 offset:46080\n"
"v_mfma_f32_16x16x32_bf16 v[232:235], v[0:3], a[168:171], v[232:235]\n"
"ds_read_b128 a[92:95], v16 offset:52224\n"
"v_mfma_f32_16x16x32_bf16 v[232:235], v[4:7], a[172:175], v[232:235]\n"
"v_mfma_f32_16x16x32_bf16 v[240:243], v[0:3], a[176:179], v[240:243]\n"
"v_mfma_f32_16x16x32_bf16 v[240:243], v[4:7], a[180:183], v[240:243]\n"
"v_mfma_f32_16x16x32_bf16 v[248:251], v[0:3], a[184:187], v[248:251]\n"
"v_mfma_f32_16x16x32_bf16 v[248:251], v[4:7], a[188:191], v[248:251]\n"
"s_waitcnt vmcnt(42)\n"
"s_waitcnt vmcnt(12) lgkmcnt(0)\n"
"s_barrier\n"
"v_mfma_f32_16x16x32_bf16 v[212:215], v[8:11], a[144:147], v[212:215]\n"
"ds_read_b64_tr_b16 v[0:1], v18 offset:0\n"
"s_cmp_le_u32 s80, 7\n"
"s_cselect_b32 s66, 11, 7\n"
"s_add_u32 s66, s66, s82\n"
"s_lshl_b32 s66, s66, 17\n"
"s_or_b32 s66, s66, s81\n"
"s_xor_b32 s66, s66, 128\n"
"s_add_u32 m0, s68, 122880\n"
"s_nop 0\n"
"buffer_load_dwordx4 v44, s[40:43], s66 offen offset:0 lds\n"
"v_mfma_f32_16x16x32_bf16 v[212:215], v[12:15], a[148:151], v[212:215]\n"
"ds_read_b64_tr_b16 v[2:3], v18 offset:2048\n"
"buffer_load_dwordx4 v45, s[40:43], s66 offen offset:1024 lds\n"
"v_mfma_f32_16x16x32_bf16 v[220:223], v[8:11], a[152:155], v[220:223]\n"
"ds_read_b64_tr_b16 v[4:5], v18 offset:4096\n"
"buffer_load_dwordx4 v46, s[40:43], s66 offen offset:2048 lds\n"
"v_mfma_f32_16x16x32_bf16 v[220:223], v[12:15], a[156:159], v[220:223]\n"
"ds_read_b64_tr_b16 v[6:7], v18 offset:6144\n"
"buffer_load_dwordx4 v47, s[40:43], s66 offen offset:3072 lds\n"
"v_mfma_f32_16x16x32_bf16 v[228:231], v[8:11], a[160:163], v[228:231]\n"
"ds_read_b128 a[96:99], v17 offset:0\n"
"v_mfma_f32_16x16x32_bf16 v[228:231], v[12:15], a[164:167], v[228:231]\n"
"ds_read_b128 a[100:103], v17 offset:6144\n"
"v_mfma_f32_16x16x32_bf16 v[236:239], v[8:11], a[168:171], v[236:239]\n"
"ds_read_b128 a[104:107], v17 offset:1024\n"
"v_mfma_f32_16x16x32_bf16 v[236:239], v[12:15], a[172:175], v[236:239]\n"
"ds_read_b128 a[108:111], v17 offset:7168\n"
"v_mfma_f32_16x16x32_bf16 v[244:247], v[8:11], a[176:179], v[244:247]\n"
"ds_read_b128 a[112:115], v17 offset:2048\n"
"v_mfma_f32_16x16x32_bf16 v[244:247], v[12:15], a[180:183], v[244:247]\n"
"ds_read_b128 a[116:119], v17 offset:8192\n"
"v_mfma_f32_16x16x32_bf16 v[252:255], v[8:11], a[184:187], v[252:255]\n"
"v_mfma_f32_16x16x32_bf16 v[252:255], v[12:15], a[188:191], v[252:255]\n"
"s_waitcnt lgkmcnt(6)\n"
"v_mfma_f32_16x16x32_bf16 v[160:163], v[0:3], a[0:3], v[160:163]\n"
"ds_read_b64_tr_b16 v[8:9], v19 offset:0\n"
"v_mfma_f32_16x16x32_bf16 v[160:163], v[4:7], a[4:7], v[160:163]\n"
"ds_read_b64_tr_b16 v[10:11], v19 offset:2048\n"
"v_mfma_f32_16x16x32_bf16 v[168:171], v[0:3], a[8:11], v[168:171]\n"
"ds_read_b64_tr_b16 v[12:13], v19 offset:4096\n"
"v_mfma_f32_16x16x32_bf16 v[168:171], v[4:7], a[12:15], v[168:171]\n"
"ds_read_b64_tr_b16 v[14:15], v19 offset:6144\n"
"v_mfma_f32_16x16x32_bf16 v[176:179], v[0:3], a[16:19], v[176:179]\n"
"ds_read_b128 a[120:123], v17 offset:3072\n"
"v_mfma_f32_16x16x32_bf16 v[176:179], v[4:7], a[20:23], v[176:179]\n"
"ds_read_b128 a[124:127], v17 offset:9216\n"
"v_mfma_f32_16x16x32_bf16 v[184:187], v[0:3], a[24:27], v[184:187]\n"
"ds_read_b128 a[128:131], v17 offset:4096\n"
"v_mfma_f32_16x16x32_bf16 v[184:187], v[4:7], a[28:31], v[184:187]\n"
"ds_read_b128 a[132:135], v17 offset:10240\n"
"v_mfma_f32_16x16x32_bf16 v[192:195], v[0:3], a[32:35], v[192:195]\n"
"ds_read_b128 a[136:139], v17 offset:5120\n"
"v_mfma_f32_16x16x32_bf16 v[192:195], v[4:7], a[36:39], v[192:195]\n"
"ds_read_b128 a[140:143], v17 offset:11264\n"
"v_mfma_f32_16x16x32_bf16 v[200:203], v[0:3], a[40:43], v[200:203]\n"
"s_waitcnt vmcnt(21)\n"
"s_add_u32 s66, s85, 6144\n"
"buffer_store_dwordx2 v[42:43], v23, s[96:99], s66 offen sc1\n"
"v_mfma_f32_16x16x32_bf16 v[200:203], v[4:7], a[44:47], v[200:203]\n"
"s_waitcnt vmcnt(9)\n"
"s_add_u32 s66, s86, 6144\n"
"buffer_store_dwordx2 v[42:43], v23, s[96:99], s66 offen sc1\n"
"s_waitcnt vmcnt(38)\n"
"s_waitcnt vmcnt(14) lgkmcnt(0)\n"
"s_barrier\n"
"v_mfma_f32_16x16x32_bf16 v[164:167], v[8:11], a[0:3], v[164:167]\n"
"ds_read_b64_tr_b16 v[0:1], v18 offset:40960\n"
"v_mfma_f32_16x16x32_bf16 v[164:167], v[12:15], a[4:7], v[164:167]\n"
"ds_read_b64_tr_b16 v[2:3], v18 offset:43008\n"
"v_mfma_f32_16x16x32_bf16 v[172:175], v[8:11], a[8:11], v[172:175]\n"
"ds_read_b64_tr_b16 v[4:5], v18 offset:45056\n"
"v_mfma_f32_16x16x32_bf16 v[172:175], v[12:15], a[12:15], v[172:175]\n"
"ds_read_b64_tr_b16 v[6:7], v18 offset:47104\n"
"v_mfma_f32_16x16x32_bf16 v[180:183], v[8:11], a[16:19], v[180:183]\n"
"ds_read_b128 a[144:147], v17 offset:40960\n"
"v_mfma_f32_16x16x32_bf16 v[180:183], v[12:15], a[20:23], v[180:183]\n"
"ds_read_b128 a[148:151], v17 offset:47104\n"
"v_mfma_f32_16x16x32_bf16 v[188:191], v[8:11], a[24:27], v[188:191]\n"
"ds_read_b128 a[152:155], v17 offset:41984\n"
"v_mfma_f32_16x16x32_bf16 v[188:191], v[12:15], a[28:31], v[188:191]\n"
"ds_read_b128 a[156:159], v17 offset:48128\n"
"v_mfma_f32_16x16x32_bf16 v[196:199], v[8:11], a[32:35], v[196:199]\n"
"ds_read_b128 a[160:163], v17 offset:43008\n"
"v_mfma_f32_16x16x32_bf16 v[196:199], v[12:15], a[36:39], v[196:199]\n"
"ds_read_b128 a[164:167], v17 offset:49152\n"
"v_mfma_f32_16x16x32_bf16 v[204:207], v[8:11], a[40:43], v[204:207]\n"
"v_mfma_f32_16x16x32_bf16 v[204:207], v[12:15], a[44:47], v[204:207]\n"
"s_waitcnt lgkmcnt(6)\n"
"v_mfma_f32_16x16x32_bf16 v[160:163], v[0:3], a[48:51], v[160:163]\n"
"ds_read_b64_tr_b16 v[8:9], v19 offset:40960\n"
"v_mfma_f32_16x16x32_bf16 v[160:163], v[4:7], a[52:55], v[160:163]\n"
"ds_read_b64_tr_b16 v[10:11], v19 offset:43008\n"
"v_mfma_f32_16x16x32_bf16 v[168:171], v[0:3], a[56:59], v[168:171]\n"
"ds_read_b64_tr_b16 v[12:13], v19 offset:45056\n"
"v_mfma_f32_16x16x32_bf16 v[168:171], v[4:7], a[60:63], v[168:171]\n"
"ds_read_b64_tr_b16 v[14:15], v19 offset:47104\n"
"v_mfma_f32_16x16x32_bf16 v[176:179], v[0:3], a[64:67], v[176:179]\n"
"ds_read_b128 a[168:171], v17 offset:44032\n"
"v_mfma_f32_16x16x32_bf16 v[176:179], v[4:7], a[68:71], v[176:179]\n"
"ds_read_b128 a[172:175], v17 offset:50176\n"
"v_mfma_f32_16x16x32_bf16 v[184:187], v[0:3], a[72:75], v[184:187]\n"
"ds_read_b128 a[176:179], v17 offset:45056\n"
"v_mfma_f32_16x16x32_bf16 v[184:187], v[4:7], a[76:79], v[184:187]\n"
"ds_read_b128 a[180:183], v17 offset:51200\n"
"v_mfma_f32_16x16x32_bf16 v[192:195], v[0:3], a[80:83], v[192:195]\n"
"ds_read_b128 a[184:187], v17 offset:46080\n"
"v_mfma_f32_16x16x32_bf16 v[192:195], v[4:7], a[84:87], v[192:195]\n"
"ds_read_b128 a[188:191], v17 offset:52224\n"
"v_mfma_f32_16x16x32_bf16 v[200:203], v[0:3], a[88:91], v[200:203]\n"
"v_mfma_f32_16x16x32_bf16 v[200:203], v[4:7], a[92:95], v[200:203]\n"
"s_waitcnt vmcnt(6) lgkmcnt(0)\n"
"s_barrier\n"
"v_mfma_f32_16x16x32_bf16 v[164:167], v[8:11], a[48:51], v[164:167]\n"
"ds_read_b64_tr_b16 v[0:1], v20 offset:0\n"
"v_mfma_f32_16x16x32_bf16 v[164:167], v[12:15], a[52:55], v[164:167]\n"
"ds_read_b64_tr_b16 v[2:3], v20 offset:2048\n"
"v_mfma_f32_16x16x32_bf16 v[172:175], v[8:11], a[56:59], v[172:175]\n"
"ds_read_b64_tr_b16 v[4:5], v20 offset:4096\n"
"v_mfma_f32_16x16x32_bf16 v[172:175], v[12:15], a[60:63], v[172:175]\n"
"ds_read_b64_tr_b16 v[6:7], v20 offset:6144\n"
"v_mfma_f32_16x16x32_bf16 v[180:183], v[8:11], a[64:67], v[180:183]\n"
"v_mfma_f32_16x16x32_bf16 v[180:183], v[12:15], a[68:71], v[180:183]\n"
"v_mfma_f32_16x16x32_bf16 v[188:191], v[8:11], a[72:75], v[188:191]\n"
"v_mfma_f32_16x16x32_bf16 v[188:191], v[12:15], a[76:79], v[188:191]\n"
"v_mfma_f32_16x16x32_bf16 v[196:199], v[8:11], a[80:83], v[196:199]\n"
"v_mfma_f32_16x16x32_bf16 v[196:199], v[12:15], a[84:87], v[196:199]\n"
"v_mfma_f32_16x16x32_bf16 v[204:207], v[8:11], a[88:91], v[204:207]\n"
"buffer_load_dwordx2 v[40:41], v26, s[96:99], 0 offen sc1\n"
"v_mfma_f32_16x16x32_bf16 v[204:207], v[12:15], a[92:95], v[204:207]\n"
"s_waitcnt lgkmcnt(0)\n"
"v_mfma_f32_16x16x32_bf16 v[160:163], v[0:3], a[96:99], v[160:163]\n"
"ds_read_b64_tr_b16 v[8:9], v21 offset:0\n"
"v_mfma_f32_16x16x32_bf16 v[160:163], v[4:7], a[100:103], v[160:163]\n"
"ds_read_b64_tr_b16 v[10:11], v21 offset:2048\n"
"v_mfma_f32_16x16x32_bf16 v[168:171], v[0:3], a[104:107], v[168:171]\n"
"ds_read_b64_tr_b16 v[12:13], v21 offset:4096\n"
"v_mfma_f32_16x16x32_bf16 v[168:171], v[4:7], a[108:111], v[168:171]\n"
"ds_read_b64_tr_b16 v[14:15], v21 offset:6144\n"
"v_mfma_f32_16x16x32_bf16 v[176:179], v[0:3], a[112:115], v[176:179]\n"
"v_mfma_f32_16x16x32_bf16 v[176:179], v[4:7], a[116:119], v[176:179]\n"
"v_mfma_f32_16x16x32_bf16 v[184:187], v[0:3], a[120:123], v[184:187]\n"
"v_mfma_f32_16x16x32_bf16 v[184:187], v[4:7], a[124:127], v[184:187]\n"
"v_mfma_f32_16x16x32_bf16 v[192:195], v[0:3], a[128:131], v[192:195]\n"
"v_mfma_f32_16x16x32_bf16 v[192:195], v[4:7], a[132:135], v[192:195]\n"
"v_mfma_f32_16x16x32_bf16 v[200:203], v[0:3], a[136:139], v[200:203]\n"
"v_mfma_f32_16x16x32_bf16 v[200:203], v[4:7], a[140:143], v[200:203]\n"
"s_waitcnt vmcnt(3) lgkmcnt(0)\n"
"s_barrier\n"
"v_mfma_f32_16x16x32_bf16 v[164:167], v[8:11], a[96:99], v[164:167]\n"
"ds_read_b64_tr_b16 v[0:1], v20 offset:40960\n"
"v_mfma_f32_16x16x32_bf16 v[164:167], v[12:15], a[100:103], v[164:167]\n"
"ds_read_b64_tr_b16 v[2:3], v20 offset:43008\n"
"v_mfma_f32_16x16x32_bf16 v[172:175], v[8:11], a[104:107], v[172:175]\n"
"ds_read_b64_tr_b16 v[4:5], v20 offset:45056\n"
"v_mfma_f32_16x16x32_bf16 v[172:175], v[12:15], a[108:111], v[172:175]\n"
"ds_read_b64_tr_b16 v[6:7], v20 offset:47104\n"
"v_mfma_f32_16x16x32_bf16 v[180:183], v[8:11], a[112:115], v[180:183]\n"
"v_mfma_f32_16x16x32_bf16 v[180:183], v[12:15], a[116:119], v[180:183]\n"
"v_mfma_f32_16x16x32_bf16 v[188:191], v[8:11], a[120:123], v[188:191]\n"
"v_mfma_f32_16x16x32_bf16 v[188:191], v[12:15], a[124:127], v[188:191]\n"
"v_mfma_f32_16x16x32_bf16 v[196:199], v[8:11], a[128:131], v[196:199]\n"
"v_mfma_f32_16x16x32_bf16 v[196:199], v[12:15], a[132:135], v[196:199]\n"
"v_mfma_f32_16x16x32_bf16 v[204:207], v[8:11], a[136:139], v[204:207]\n"
"buffer_load_dwordx2 v[40:41], v26, s[96:99], 0 offen sc1\n"
"v_mfma_f32_16x16x32_bf16 v[204:207], v[12:15], a[140:143], v[204:207]\n"
"s_waitcnt lgkmcnt(0)\n"
"v_mfma_f32_16x16x32_bf16 v[160:163], v[0:3], a[144:147], v[160:163]\n"
"ds_read_b64_tr_b16 v[8:9], v21 offset:40960\n"
"v_mfma_f32_16x16x32_bf16 v[160:163], v[4:7], a[148:151], v[160:163]\n"
"ds_read_b64_tr_b16 v[10:11], v21 offset:43008\n"
"v_mfma_f32_16x16x32_bf16 v[168:171], v[0:3], a[152:155], v[168:171]\n"
"ds_read_b64_tr_b16 v[12:13], v21 offset:45056\n"
"v_mfma_f32_16x16x32_bf16 v[168:171], v[4:7], a[156:159], v[168:171]\n"
"ds_read_b64_tr_b16 v[14:15], v21 offset:47104\n"
"v_mfma_f32_16x16x32_bf16 v[176:179], v[0:3], a[160:163], v[176:179]\n"
"v_mfma_f32_16x16x32_bf16 v[176:179], v[4:7], a[164:167], v[176:179]\n"
"v_mfma_f32_16x16x32_bf16 v[184:187], v[0:3], a[168:171], v[184:187]\n"
"v_mfma_f32_16x16x32_bf16 v[184:187], v[4:7], a[172:175], v[184:187]\n"
"v_mfma_f32_16x16x32_bf16 v[192:195], v[0:3], a[176:179], v[192:195]\n"
"v_mfma_f32_16x16x32_bf16 v[192:195], v[4:7], a[180:183], v[192:195]\n"
"v_mfma_f32_16x16x32_bf16 v[200:203], v[0:3], a[184:187], v[200:203]\n"
"v_mfma_f32_16x16x32_bf16 v[200:203], v[4:7], a[188:191], v[200:203]\n"
"s_waitcnt lgkmcnt(0)\n"
"v_mfma_f32_16x16x32_bf16 v[164:167], v[8:11], a[144:147], v[164:167]\n"
"ds_read_b64_tr_b16 v[0:1], v18 offset:512\n"
"v_mfma_f32_16x16x32_bf16 v[164:167], v[12:15], a[148:151], v[164:167]\n"
"ds_read_b64_tr_b16 v[2:3], v18 offset:2560\n"
"v_mfma_f32_16x16x32_bf16 v[172:175], v[8:11], a[152:155], v[172:175]\n"
"ds_read_b64_tr_b16 v[4:5], v18 offset:4608\n"
"v_mfma_f32_16x16x32_bf16 v[172:175], v[12:15], a[156:159], v[172:175]\n"
"ds_read_b64_tr_b16 v[6:7], v18 offset:6656\n"
"v_mfma_f32_16x16x32_bf16 v[180:183], v[8:11], a[160:163], v[180:183]\n"
"v_mfma_f32_16x16x32_bf16 v[180:183], v[12:15], a[164:167], v[180:183]\n"
"v_mfma_f32_16x16x32_bf16 v[188:191], v[8:11], a[168:171], v[188:191]\n"
"v_mfma_f32_16x16x32_bf16 v[188:191], v[12:15], a[172:175], v[188:191]\n"
"v_mfma_f32_16x16x32_bf16 v[196:199], v[8:11], a[176:179], v[196:199]\n"
"v_mfma_f32_16x16x32_bf16 v[196:199], v[12:15], a[180:183], v[196:199]\n"
"v_mfma_f32_16x16x32_bf16 v[204:207], v[8:11], a[184:187], v[204:207]\n"
"v_mfma_f32_16x16x32_bf16 v[204:207], v[12:15], a[188:191], v[204:207]\n"
"s_waitcnt lgkmcnt(0)\n"
"v_mfma_f32_16x16x32_bf16 v[208:211], v[0:3], a[0:3], v[208:211]\n"
"ds_read_b64_tr_b16 v[8:9], v19 offset:512\n"
"v_mfma_f32_16x16x32_bf16 v[208:211], v[4:7], a[4:7], v[208:211]\n"
"ds_read_b64_tr_b16 v[10:11], v19 offset:2560\n"
"v_mfma_f32_16x16x32_bf16 v[216:219], v[0:3], a[8:11], v[216:219]\n"
"ds_read_b64_tr_b16 v[12:13], v19 offset:4608\n"
"v_mfma_f32_16x16x32_bf16 v[216:219], v[4:7], a[12:15], v[216:219]\n"
"ds_read_b64_tr_b16 v[14:15], v19 offset:6656\n"
"v_cvt_pk_bf16_f32 v160, v160, v161\n"
"v_mfma_f32_16x16x32_bf16 v[224:227], v[0:3], a[16:19], v[224:227]\n"
"v_cvt_pk_bf16_f32 v161, v162, v163\n"
"v_cvt_pk_bf16_f32 v162, v164, v165\n"
"v_mfma_f32_16x16x32_bf16 v[224:227], v[4:7], a[20:23], v[224:227]\n"
"v_cvt_pk_bf16_f32 v163, v166, v167\n"
"v_mfma_f32_16x16x32_bf16 v[232:235], v[0:3], a[24:27], v[232:235]\n"
"s_add_u32 s65, s95, 0\n"
"buffer_store_dwordx4 v[160:163], v56, s[96:99], s65 offen offset:0 sc1\n"
"v_mfma_f32_16x16x32_bf16 v[232:235], v[4:7], a[28:31], v[232:235]\n"
"v_cvt_pk_bf16_f32 v168, v168, v169\n"
"v_cvt_pk_bf16_f32 v169, v170, v171\n"
"v_mfma_f32_16x16x32_bf16 v[240:243], v[0:3], a[32:35], v[240:243]\n"
"v_cvt_pk_bf16_f32 v170, v172, v173\n"
"v_cvt_pk_bf16_f32 v171, v174, v175\n"
"v_mfma_f32_16x16x32_bf16 v[240:243], v[4:7], a[36:39], v[240:243]\n"
"buffer_store_dwordx4 v[168:171], v56, s[96:99], s65 offen offset:1024 sc1\n"
"v_mfma_f32_16x16x32_bf16 v[248:251], v[0:3], a[40:43], v[248:251]\n"
"v_cvt_pk_bf16_f32 v176, v176, v177\n"
"v_cvt_pk_bf16_f32 v177, v178, v179\n"
"v_mfma_f32_16x16x32_bf16 v[248:251], v[4:7], a[44:47], v[248:251]\n"
"v_cvt_pk_bf16_f32 v178, v180, v181\n"
"v_cvt_pk_bf16_f32 v179, v182, v183\n"
"s_waitcnt lgkmcnt(0)\n"
"v_mfma_f32_16x16x32_bf16 v[212:215], v[8:11], a[0:3], v[212:215]\n"
"ds_read_b64_tr_b16 v[0:1], v18 offset:41472\n"
"v_mfma_f32_16x16x32_bf16 v[212:215], v[12:15], a[4:7], v[212:215]\n"
"ds_read_b64_tr_b16 v[2:3], v18 offset:43520\n"
"v_mfma_f32_16x16x32_bf16 v[220:223], v[8:11], a[8:11], v[220:223]\n"
"ds_read_b64_tr_b16 v[4:5], v18 offset:45568\n"
"v_mfma_f32_16x16x32_bf16 v[220:223], v[12:15], a[12:15], v[220:223]\n"
"ds_read_b64_tr_b16 v[6:7], v18 offset:47616\n"
"v_mfma_f32_16x16x32_bf16 v[228:231], v[8:11], a[16:19], v[228:231]\n"
"buffer_store_dwordx4 v[176:179], v56, s[96:99], s65 offen offset:2048 sc1\n"
"v_mfma_f32_16x16x32_bf16 v[228:231], v[12:15], a[20:23], v[228:231]\n"
"v_cvt_pk_bf16_f32 v184, v184, v185\n"
"v_cvt_pk_bf16_f32 v185, v186, v187\n"
"v_mfma_f32_16x16x32_bf16 v[236:239], v[8:11], a[24:27], v[236:239]\n"
"v_cvt_pk_bf16_f32 v186, v188, v189\n"
"v_cvt_pk_bf16_f32 v187, v190, v191\n"
"v_mfma_f32_16x16x32_bf16 v[236:239], v[12:15], a[28:31], v[236:239]\n"
"buffer_store_dwordx4 v[184:187], v56, s[96:99], s65 offen offset:3072 sc1\n"
"v_mfma_f32_16x16x32_bf16 v[244:247], v[8:11], a[32:35], v[244:247]\n"
"v_cvt_pk_bf16_f32 v192, v192, v193\n"
"v_cvt_pk_bf16_f32 v193, v194, v195\n"
"v_mfma_f32_16x16x32_bf16 v[244:247], v[12:15], a[36:39], v[244:247]\n"
"v_cvt_pk_bf16_f32 v194, v196, v197\n"
"v_cvt_pk_bf16_f32 v195, v198, v199\n"
"v_mfma_f32_16x16x32_bf16 v[252:255], v[8:11], a[40:43], v[252:255]\n"
"s_add_u32 s65, s95, 4096\n"
"buffer_store_dwordx4 v[192:195], v56, s[96:99], s65 offen offset:0 sc1\n"
"v_mfma_f32_16x16x32_bf16 v[252:255], v[12:15], a[44:47], v[252:255]\n"
"v_cvt_pk_bf16_f32 v200, v200, v201\n"
"v_cvt_pk_bf16_f32 v201, v202, v203\n"
"s_waitcnt lgkmcnt(0)\n"
"v_mfma_f32_16x16x32_bf16 v[208:211], v[0:3], a[48:51], v[208:211]\n"
"ds_read_b64_tr_b16 v[8:9], v19 offset:41472\n"
"v_cvt_pk_bf16_f32 v202, v204, v205\n"
"v_mfma_f32_16x16x32_bf16 v[208:211], v[4:7], a[52:55], v[208:211]\n"
"ds_read_b64_tr_b16 v[10:11], v19 offset:43520\n"
"v_cvt_pk_bf16_f32 v203, v206, v207\n"
"v_mfma_f32_16x16x32_bf16 v[216:219], v[0:3], a[56:59], v[216:219]\n"
"ds_read_b64_tr_b16 v[12:13], v19 offset:45568\n"
"v_mfma_f32_16x16x32_bf16 v[216:219], v[4:7], a[60:63], v[216:219]\n"
"ds_read_b64_tr_b16 v[14:15], v19 offset:47616\n"
"v_mfma_f32_16x16x32_bf16 v[224:227], v[0:3], a[64:67], v[224:227]\n"
"buffer_store_dwordx4 v[200:203], v56, s[96:99], s65 offen offset:1024 sc1\n"
"v_cmp_eq_u64 vcc, s[88:89], v[40:41]\n"
"v_mfma_f32_16x16x32_bf16 v[224:227], v[4:7], a[68:71], v[224:227]\n"
"v_mfma_f32_16x16x32_bf16 v[232:235], v[0:3], a[72:75], v[232:235]\n"
"s_mov_b64 s[36:37], s[96:97]\n"
"s_and_b32 s84, vcc_lo, 3\n"
"v_mfma_f32_16x16x32_bf16 v[232:235], v[4:7], a[76:79], v[232:235]\n"
"v_mfma_f32_16x16x32_bf16 v[240:243], v[0:3], a[80:83], v[240:243]\n"
"s_bitcmp1_b32 s84, 0\n"
"s_cselect_b32 s38, s98, 0\n"
"s_add_u32 s66, s58, 0\n"
"buffer_load_dwordx4 v[64:67], v56, s[36:39], s66 offen offset:0 sc1\n"
"v_mfma_f32_16x16x32_bf16 v[240:243], v[4:7], a[84:87], v[240:243]\n"
"v_mfma_f32_16x16x32_bf16 v[248:251], v[0:3], a[88:91], v[248:251]\n"
"buffer_load_dwordx4 v[68:71], v56, s[36:39], s66 offen offset:1024 sc1\n"
"v_mfma_f32_16x16x32_bf16 v[248:251], v[4:7], a[92:95], v[248:251]\n"
"s_waitcnt lgkmcnt(0)\n"
"v_mfma_f32_16x16x32_bf16 v[212:215], v[8:11], a[48:51], v[212:215]\n"
"ds_read_b64_tr_b16 v[0:1], v20 offset:512\n"
"v_mfma_f32_16x16x32_bf16 v[212:215], v[12:15], a[52:55], v[212:215]\n"
"ds_read_b64_tr_b16 v[2:3], v20 offset:2560\n"
"v_mfma_f32_16x16x32_bf16 v[220:223], v[8:11], a[56:59], v[220:223]\n"
"ds_read_b64_tr_b16 v[4:5], v20 offset:4608\n"
"v_mfma_f32_16x16x32_bf16 v[220:223], v[12:15], a[60:63], v[220:223]\n"
"ds_read_b64_tr_b16 v[6:7], v20 offset:6656\n"
"v_mfma_f32_16x16x32_bf16 v[228:231], v[8:11], a[64:67], v[228:231]\n"
"buffer_load_dwordx4 v[72:75], v56, s[36:39], s66 offen offset:2048 sc1\n"
"v_mfma_f32_16x16x32_bf16 v[228:231], v[12:15], a[68:71], v[228:231]\n"
"v_mfma_f32_16x16x32_bf16 v[236:239], v[8:11], a[72:75], v[236:239]\n"
"buffer_load_dwordx4 v[76:79], v56, s[36:39], s66 offen offset:3072 sc1\n"
"v_mfma_f32_16x16x32_bf16 v[236:239], v[12:15], a[76:79], v[236:239]\n"
"v_mfma_f32_16x16x32_bf16 v[244:247], v[8:11], a[80:83], v[244:247]\n"
"s_add_u32 s66, s58, 4096\n"
"buffer_load_dwordx4 v[80:83], v56, s[36:39], s66 offen offset:0 sc1\n"
"v_mfma_f32_16x16x32_bf16 v[244:247], v[12:15], a[84:87], v[244:247]\n"
"v_mfma_f32_16x16x32_bf16 v[252:255], v[8:11], a[88:91], v[252:255]\n"
"buffer_load_dwordx4 v[84:87], v56, s[36:39], s66 offen offset:1024 sc1\n"
"v_mfma_f32_16x16x32_bf16 v[252:255], v[12:15], a[92:95], v[252:255]\n"
"s_waitcnt lgkmcnt(0)\n"
"v_mfma_f32_16x16x32_bf16 v[208:211], v[0:3], a[96:99], v[208:211]\n"
"ds_read_b64_tr_b16 v[8:9], v21 offset:512\n"
"v_mfma_f32_16x16x32_bf16 v[208:211], v[4:7], a[100:103], v[208:211]\n"
"ds_read_b64_tr_b16 v[10:11], v21 offset:2560\n"
"v_mfma_f32_16x16x32_bf16 v[216:219], v[0:3], a[104:107], v[216:219]\n"
"ds_read_b64_tr_b16 v[12:13], v21 offset:4608\n"
"v_mfma_f32_16x16x32_bf16 v[216:219], v[4:7], a[108:111], v[216:219]\n"
"ds_read_b64_tr_b16 v[14:15], v21 offset:6656\n"
"v_mfma_f32_16x16x32_bf16 v[224:227], v[0:3], a[112:115], v[224:227]\n"
"s_bitcmp1_b32 s84, 1\n"
"s_cselect_b32 s38, s98, 0\n"
"s_add_u32 s66, s59, 0\n"
"buffer_load_dwordx4 v[88:91], v56, s[36:39], s66 offen offset:0 sc1\n"
"v_mfma_f32_16x16x32_bf16 v[224:227], v[4:7], a[116:119], v[224:227]\n"
"v_mfma_f32_16x16x32_bf16 v[232:235], v[0:3], a[120:123], v[232:235]\n"
"buffer_load_dwordx4 v[92:95], v56, s[36:39], s66 offen offset:1024 sc1\n"
"v_mfma_f32_16x16x32_bf16 v[232:235], v[4:7], a[124:127], v[232:235]\n"
"v_mfma_f32_16x16x32_bf16 v[240:243], v[0:3], a[128:131], v[240:243]\n"
"buffer_load_dwordx4 v[96:99], v56, s[36:39], s66 offen offset:2048 sc1\n"
"v_mfma_f32_16x16x32_bf16 v[240:243], v[4:7], a[132:135], v[240:243]\n"
"v_mfma_f32_16x16x32_bf16 v[248:251], v[0:3], a[136:139], v[248:251]\n"
"buffer_load_dwordx4 v[100:103], v56, s[36:39], s66 offen offset:3072 sc1\n"
"v_mfma_f32_16x16x32_bf16 v[248:251], v[4:7], a[140:143], v[248:251]\n"
"s_waitcnt lgkmcnt(0)\n"
"v_mfma_f32_16x16x32_bf16 v[212:215], v[8:11], a[96:99], v[212:215]\n"
"ds_read_b64_tr_b16 v[0:1], v20 offset:41472\n"
"v_mfma_f32_16x16x32_bf16 v[212:215], v[12:15], a[100:103], v[212:215]\n"
"ds_read_b64_tr_b16 v[2:3], v20 offset:43520\n"
"v_mfma_f32_16x16x32_bf16 v[220:223], v[8:11], a[104:107], v[220:223]\n"
"ds_read_b64_tr_b16 v[4:5], v20 offset:45568\n"
"v_mfma_f32_16x16x32_bf16 v[220:223], v[12:15], a[108:111], v[220:223]\n"
"ds_read_b64_tr_b16 v[6:7], v20 offset:47616\n"
"v_mfma_f32_16x16x32_bf16 v[228:231], v[8:11], a[112:115], v[228:231]\n"
"s_add_u32 s66, s59, 4096\n"
"buffer_load_dwordx4 v[104:107], v56, s[36:39], s66 offen offset:0 sc1\n"
"v_mfma_f32_16x16x32_bf16 v[228:231], v[12:15], a[116:119], v[228:231]\n"
"v_mfma_f32_16x16x32_bf16 v[236:239], v[8:11], a[120:123], v[236:239]\n"
"buffer_load_dwordx4 v[108:111], v56, s[36:39], s66 offen offset:1024 sc1\n"
"v_mfma_f32_16x16x32_bf16 v[236:239], v[12:15], a[124:127], v[236:239]\n"
"v_mfma_f32_16x16x32_bf16 v[244:247], v[8:11], a[128:131], v[244:247]\n"
"v_mfma_f32_16x16x32_bf16 v[244:247], v[12:15], a[132:135], v[244:247]\n"
"v_mfma_f32_16x16x32_bf16 v[252:255], v[8:11], a[136:139], v[252:255]\n"
"v_mfma_f32_16x16x32_bf16 v[252:255], v[12:15], a[140:143], v[252:255]\n"
"s_waitcnt lgkmcnt(0)\n"
"v_mfma_f32_16x16x32_bf16 v[208:211], v[0:3], a[144:147], v[208:211]\n"
"ds_read_b64_tr_b16 v[8:9], v21 offset:41472\n"
"v_mfma_f32_16x16x32_bf16 v[208:211], v[4:7], a[148:151], v[208:211]\n"
"ds_read_b64_tr_b16 v[10:11], v21 offset:43520\n"
"v_mfma_f32_16x16x32_bf16 v[216:219], v[0:3], a[152:155], v[216:219]\n"
"ds_read_b64_tr_b16 v[12:13], v21 offset:45568\n"
"v_mfma_f32_16x16x32_bf16 v[216:219], v[4:7], a[156:159], v[216:219]\n"
"ds_read_b64_tr_b16 v[14:15], v21 offset:47616\n"
"v_mfma_f32_16x16x32_bf16 v[224:227], v[0:3], a[160:163], v[224:227]\n"
"v_mfma_f32_16x16x32_bf16 v[224:227], v[4:7], a[164:167], v[224:227]\n"
"v_mfma_f32_16x16x32_bf16 v[232:235], v[0:3], a[168:171], v[232:235]\n"
"v_mfma_f32_16x16x32_bf16 v[232:235], v[4:7], a[172:175], v[232:235]\n"
"v_mfma_f32_16x16x32_bf16 v[240:243], v[0:3], a[176:179], v[240:243]\n"
"v_mfma_f32_16x16x32_bf16 v[240:243], v[4:7], a[180:183], v[240:243]\n"
"v_mfma_f32_16x16x32_bf16 v[248:251], v[0:3], a[184:187], v[248:251]\n"
"buffer_load_dwordx2 v[40:41], v26, s[96:99], 0 offen sc1\n"
"v_mfma_f32_16x16x32_bf16 v[248:251], v[4:7], a[188:191], v[248:251]\n"
"s_waitcnt lgkmcnt(0)\n"
"v_mfma_f32_16x16x32_bf16 v[212:215], v[8:11], a[144:147], v[212:215]\n"
"v_mfma_f32_16x16x32_bf16 v[212:215], v[12:15], a[148:151], v[212:215]\n"
"v_mfma_f32_16x16x32_bf16 v[220:223], v[8:11], a[152:155], v[220:223]\n"
"v_mfma_f32_16x16x32_bf16 v[220:223], v[12:15], a[156:159], v[220:223]\n"
"v_mfma_f32_16x16x32_bf16 v[228:231], v[8:11], a[160:163], v[228:231]\n"
"v_mfma_f32_16x16x32_bf16 v[228:231], v[12:15], a[164:167], v[228:231]\n"
"v_mfma_f32_16x16x32_bf16 v[236:239], v[8:11], a[168:171], v[236:239]\n"
"v_mfma_f32_16x16x32_bf16 v[236:239], v[12:15], a[172:175], v[236:239]\n"
"v_mfma_f32_16x16x32_bf16 v[244:247], v[8:11], a[176:179], v[244:247]\n"
"v_mfma_f32_16x16x32_bf16 v[244:247], v[12:15], a[180:183], v[244:247]\n"
"v_mfma_f32_16x16x32_bf16 v[252:255], v[8:11], a[184:187], v[252:255]\n"
"v_mfma_f32_16x16x32_bf16 v[252:255], v[12:15], a[188:191], v[252:255]\n"
"s_waitcnt vmcnt(13)\n"
"s_add_u32 s66, s95, 6144\n"
"buffer_store_dwordx2 v[42:43], v23, s[96:99], s66 offen sc1\n"
"s_mov_b32 s87, 0x100000\n"
"P2_XL:\n"
"s_cmp_eq_u32 s84, 7\n"
"s_cbranch_scc1 P2_XD\n"
"s_waitcnt vmcnt(36)\n"
"buffer_load_dwordx2 v[40:41], v26, s[96:99], 0 offen sc1\n"
"s_sleep 4\n"
"v_cmp_eq_u64 vcc, s[88:89], v[40:41]\n"
"s_sub_u32 s87, s87, 1\n"
"s_cbranch_scc1 P2_XD\n"
"s_andn2_b32 s81, vcc_lo, s84\n"
"s_or_b32 s84, s84, vcc_lo\n"
"s_and_b32 s84, s84, 7\n"
"s_bitcmp1_b32 s81, 0\n"
"s_cbranch_scc0 P2_XS0\n"
"s_add_u32 s65, s58, 0\n"
"buffer_load_dwordx4 v[64:67], v56, s[96:99], s65 offen offset:0 sc1\n"
"buffer_load_dwordx4 v[68:71], v56, s[96:99], s65 offen offset:1024 sc1\n"
"buffer_load_dwordx4 v[72:75], v56, s[96:99], s65 offen offset:2048 sc1\n"
"buffer_load_dwordx4 v[76:79], v56, s[96:99], s65 offen offset:3072 sc1\n"
"s_add_u32 s65, s58, 4096\n"
"buffer_load_dwordx4 v[80:83], v56, s[96:99], s65 offen offset:0 sc1\n"
"buffer_load_dwordx4 v[84:87], v56, s[96:99], s65 offen offset:1024 sc1\n"
"P2_XS0:\n"
"s_bitcmp1_b32 s81, 1\n"
"s_cbranch_scc0 P2_XS1\n"
"s_add_u32 s65, s59, 0\n"
"buffer_load_dwordx4 v[88:91], v56, s[96:99], s65 offen offset:0 sc1\n"
"buffer_load_dwordx4 v[92:95], v56, s[96:99], s65 offen offset:1024 sc1\n"
"buffer_load_dwordx4 v[96:99], v56, s[96:99], s65 offen offset:2048 sc1\n"
"buffer_load_dwordx4 v[100:103], v56, s[96:99], s65 offen offset:3072 sc1\n"
"s_add_u32 s65, s59, 4096\n"
"buffer_load_dwordx4 v[104:107], v56, s[96:99], s65 offen offset:0 sc1\n"
"buffer_load_dwordx4 v[108:111], v56, s[96:99], s65 offen offset:1024 sc1\n"
"P2_XS1:\n"
"s_bitcmp1_b32 s81, 2\n"
"s_cbranch_scc0 P2_XS2\n"
"s_add_u32 s65, s60, 0\n"
"buffer_load_dwordx4 v[112:115], v56, s[96:99], s65 offen offset:0 sc1\n"
"buffer_load_dwordx4 v[116:119], v56, s[96:99], s65 offen offset:1024 sc1\n"
"buffer_load_dwordx4 v[120:123], v56, s[96:99], s65 offen offset:2048 sc1\n"
"buffer_load_dwordx4 v[124:127], v56, s[96:99], s65 offen offset:3072 sc1\n"
"s_add_u32 s65, s60, 4096\n"
"buffer_load_dwordx4 v[128:131], v56, s[96:99], s65 offen offset:0 sc1\n"
"buffer_load_dwordx4 v[132:135], v56, s[96:99], s65 offen offset:1024 sc1\n"
"P2_XS2:\n"
"s_branch P2_XL\n"
"P2_XD:\n"
"s_waitcnt vmcnt(0)\n"
"v_mfma_f32_16x16x32_bf16 v[208:211], v[28:31], v[64:67], v[208:211]\n"
"v_mfma_f32_16x16x32_bf16 v[208:211], v[28:31], v[88:91], v[208:211]\n"
"v_mfma_f32_16x16x32_bf16 v[208:211], v[28:31], v[112:115], v[208:211]\n"
"v_mfma_f32_16x16x32_bf16 v[212:215], v[32:35], v[64:67], v[212:215]\n"
"v_mfma_f32_16x16x32_bf16 v[212:215], v[32:35], v[88:91], v[212:215]\n"
"v_mfma_f32_16x16x32_bf16 v[212:215], v[32:35], v[112:115], v[212:215]\n"
"v_mfma_f32_16x16x32_bf16 v[216:219], v[28:31], v[68:71], v[216:219]\n"
"v_mfma_f32_16x16x32_bf16 v[216:219], v[28:31], v[92:95], v[216:219]\n"
"v_mfma_f32_16x16x32_bf16 v[216:219], v[28:31], v[116:119], v[216:219]\n"
"v_mfma_f32_16x16x32_bf16 v[220:223], v[32:35], v[68:71], v[220:223]\n"
"v_mfma_f32_16x16x32_bf16 v[220:223], v[32:35], v[92:95], v[220:223]\n"
"v_mfma_f32_16x16x32_bf16 v[220:223], v[32:35], v[116:119], v[220:223]\n"
"v_cvt_pk_bf16_f32 v208, v208, v209\n"
"v_cvt_pk_bf16_f32 v209, v210, v211\n"
"v_mfma_f32_16x16x32_bf16 v[224:227], v[28:31], v[72:75], v[224:227]\n"
"v_cvt_pk_bf16_f32 v210, v212, v213\n"
"v_cvt_pk_bf16_f32 v211, v214, v215\n"
"v_mfma_f32_16x16x32_bf16 v[224:227], v[28:31], v[96:99], v[224:227]\n"
"s_nop 1\n"
"v_permlane32_swap_b32 v208, v210\n"
"v_mfma_f32_16x16x32_bf16 v[224:227], v[28:31], v[120:123], v[224:227]\n"
"v_permlane32_swap_b32 v209, v211\n"
"v_mfma_f32_16x16x32_bf16 v[228:231], v[32:35], v[72:75], v[228:231]\n"
"s_nop 1\n"
"v_permlane16_swap_b32 v208, v210\n"
"v_mfma_f32_16x16x32_bf16 v[228:231], v[32:35], v[96:99], v[228:231]\n"
"v_permlane16_swap_b32 v209, v211\n"
"v_mfma_f32_16x16x32_bf16 v[228:231], v[32:35], v[120:123], v[228:231]\n"
"s_mov_b32 s65, 0\n"
"buffer_store_dwordx4 v[208:211], v22, s[48:51], s65 offen sc0 sc1\n"
"v_cvt_pk_bf16_f32 v216, v216, v217\n"
"v_cvt_pk_bf16_f32 v217, v218, v219\n"
"v_mfma_f32_16x16x32_bf16 v[232:235], v[28:31], v[76:79], v[232:235]\n"
"v_cvt_pk_bf16_f32 v218, v220, v221\n"
"v_cvt_pk_bf16_f32 v219, v222, v223\n"
"v_mfma_f32_16x16x32_bf16 v[232:235], v[28:31], v[100:103], v[232:235]\n"
"s_nop 1\n"
"v_permlane32_swap_b32 v216, v218\n"
"v_mfma_f32_16x16x32_bf16 v[232:235], v[28:31], v[124:127], v[232:235]\n"
"v_permlane32_swap_b32 v217, v219\n"
"v_mfma_f32_16x16x32_bf16 v[236:239], v[32:35], v[76:79], v[236:239]\n"
"s_nop 1\n"
"v_permlane16_swap_b32 v216, v218\n"
"v_mfma_f32_16x16x32_bf16 v[236:239], v[32:35], v[100:103], v[236:239]\n"
"v_permlane16_swap_b32 v217, v219\n"
"v_mfma_f32_16x16x32_bf16 v[236:239], v[32:35], v[124:127], v[236:239]\n"
"s_mov_b32 s65, 32768\n"
"buffer_store_dwordx4 v[216:219], v22, s[48:51], s65 offen sc0 sc1\n"
"v_cvt_pk_bf16_f32 v224, v224, v225\n"
"v_cvt_pk_bf16_f32 v225, v226, v227\n"
"v_mfma_f32_16x16x32_bf16 v[240:243], v[28:31], v[80:83], v[240:243]\n"
"v_cvt_pk_bf16_f32 v226, v228, v229\n"
"v_cvt_pk_bf16_f32 v227, v230, v231\n"
"v_mfma_f32_16x16x32_bf16 v[240:243], v[28:31], v[104:107], v[240:243]\n"
"s_nop 1\n"
"v_permlane32_swap_b32 v224, v226\n"
"v_mfma_f32_16x16x32_bf16 v[240:243], v[28:31], v[128:131], v[240:243]\n"
"v_permlane32_swap_b32 v225, v227\n"
"v_mfma_f32_16x16x32_bf16 v[244:247], v[32:35], v[80:83], v[244:247]\n"
"s_nop 1\n"
"v_permlane16_swap_b32 v224, v226\n"
"v_mfma_f32_16x16x32_bf16 v[244:247], v[32:35], v[104:107], v[244:247]\n"
"v_permlane16_swap_b32 v225, v227\n"
"v_mfma_f32_16x16x32_bf16 v[244:247], v[32:35], v[128:131], v[244:247]\n"
"s_mov_b32 s65, 65536\n"
"buffer_store_dwordx4 v[224:227], v22, s[48:51], s65 offen sc0 sc1\n"
"v_cvt_pk_bf16_f32 v232, v232, v233\n"
"v_cvt_pk_bf16_f32 v233, v234, v235\n"
"v_mfma_f32_16x16x32_bf16 v[248:251], v[28:31], v[84:87], v[248:251]\n"
"v_cvt_pk_bf16_f32 v234, v236, v237\n"
"v_cvt_pk_bf16_f32 v235, v238, v239\n"
"v_mfma_f32_16x16x32_bf16 v[248:251], v[28:31], v[108:111], v[248:251]\n"
"s_nop 1\n"
"v_permlane32_swap_b32 v232, v234\n"
"v_mfma_f32_16x16x32_bf16 v[248:251], v[28:31], v[132:135], v[248:251]\n"
"v_permlane32_swap_b32 v233, v235\n"
"v_mfma_f32_16x16x32_bf16 v[252:255], v[32:35], v[84:87], v[252:255]\n"
"s_nop 1\n"
"v_permlane16_swap_b32 v232, v234\n"
"v_mfma_f32_16x16x32_bf16 v[252:255], v[32:35], v[108:111], v[252:255]\n"
"v_permlane16_swap_b32 v233, v235\n"
"v_mfma_f32_16x16x32_bf16 v[252:255], v[32:35], v[132:135], v[252:255]\n"
"s_mov_b32 s65, 98304\n"
"buffer_store_dwordx4 v[232:235], v22, s[48:51], s65 offen sc0 sc1\n"
"v_cvt_pk_bf16_f32 v240, v240, v241\n"
"v_cvt_pk_bf16_f32 v241, v242, v243\n"
"v_cvt_pk_bf16_f32 v242, v244, v245\n"
"v_cvt_pk_bf16_f32 v243, v246, v247\n"
"s_nop 1\n"
"v_permlane32_swap_b32 v240, v242\n"
"v_permlane32_swap_b32 v241, v243\n"
"s_nop 1\n"
"v_permlane16_swap_b32 v240, v242\n"
"v_permlane16_swap_b32 v241, v243\n"
"s_mov_b32 s65, 131072\n"
"buffer_store_dwordx4 v[240:243], v22, s[48:51], s65 offen sc0 sc1\n"
"s_nop 1\n"
"v_cvt_pk_bf16_f32 v248, v248, v249\n"
"v_cvt_pk_bf16_f32 v249, v250, v251\n"
"v_cvt_pk_bf16_f32 v250, v252, v253\n"
"v_cvt_pk_bf16_f32 v251, v254, v255\n"
"s_nop 1\n"
"v_permlane32_swap_b32 v248, v250\n"
"v_permlane32_swap_b32 v249, v251\n"
"s_nop 1\n"
"v_permlane16_swap_b32 v248, v250\n"
"v_permlane16_swap_b32 v249, v251\n"
"s_mov_b32 s65, 163840\n"
"buffer_store_dwordx4 v[248:251], v22, s[48:51], s65 offen sc0 sc1\n"
"s_or_b32 s65, s22, s23\n"
"s_or_b32 s65, s65, s52\n"
"s_cmp_lg_u32 s65, 0\n"
"s_cbranch_scc1 K1_END\n"
"v_lshlrev_b32 v0, 7, v57\n"
"v_add_u32 v0, 4096, v0\n"
"s_mov_b32 s87, 0x20000\n"
"v_add_u32 v1, 8192, v0\n"
"v_add_u32 v2, 16384, v0\n"
"v_add_u32 v3, 24576, v0\n"
"P2_GEN:\n"
"global_load_dwordx2 v[4:5], v0, s[20:21] sc1\n"
"global_load_dwordx2 v[6:7], v1, s[20:21] sc1\n"
"global_load_dwordx2 v[8:9], v2, s[20:21] sc1\n"
"global_load_dwordx2 v[10:11], v3, s[20:21] sc1\n"
"s_waitcnt vmcnt(0)\n"
"v_xor_b32 v4, s88, v4\n"
"v_xor_b32 v5, s89, v5\n"
"v_xor_b32 v6, s88, v6\n"
"v_xor_b32 v7, s89, v7\n"
"v_xor_b32 v8, s88, v8\n"
"v_xor_b32 v9, s89, v9\n"
"v_xor_b32 v10, s88, v10\n"
"v_xor_b32 v11, s89, v11\n"
"v_or3_b32 v4, v4, v5, v6\n"
"v_or3_b32 v7, v7, v8, v9\n"
"v_or3_b32 v4, v4, v10, v11\n"
"v_or_b32 v4, v4, v7\n"
"v_cmp_ne_u32 vcc, 0, v4\n"
"s_sub_u32 s87, s87, 1\n"
"s_cbranch_scc1 P2_GENX\n"
"s_cbranch_vccnz P2_GEN\n"
"P2_GENX:\n"
"global_store_dwordx2 v23, v[42:43], s[20:21] sc1\n"
"s_branch K1_END\n"
"K1_RARE0:\n"
"s_call_b64 s[62:63], K1_RSUB0\n"
"s_branch K1_BACK0\n"
"K1_RARE1:\n"
"s_call_b64 s[62:63], K1_RSUB1\n"
"s_branch K1_BACK1\n"
"K1_RARE2:\n"
"s_call_b64 s[62:63], K1_RSUB2\n"
"s_branch K1_BACK2\n"
"K1_RARE3:\n"
"s_call_b64 s[62:63], K1_RSUB0\n"
"s_branch K1_BACK3\n"
"K1_RARE4:\n"
"s_call_b64 s[62:63], K1_RSUB1\n"
"s_branch K1_BACK4\n"
"K1_RARE5:\n"
"s_call_b64 s[62:63], K1_RSUB2\n"
"s_branch K1_BACK5\n"
"K1_RARE6:\n"
"s_call_b64 s[62:63], K1_RSUB0\n"
"s_branch K1_BACK6\n"
"K1_RARE7:\n"
"s_call_b64 s[62:63], K1_RSUB1\n"
"s_branch K1_BACK7\n"
"K1_RARE8:\n"
"s_call_b64 s[62:63], K1_RSUB2\n"
"s_branch K1_BACK8\n"
"K1_RARE9:\n"
"s_call_b64 s[62:63], K1_RSUB0\n"
"s_branch K1_BACK9\n"
"K1_RARE10:\n"
"s_call_b64 s[62:63], K1_RSUB1\n"
"s_branch K1_BACK10\n"
"K1_RARE11:\n"
"s_call_b64 s[62:63], K1_RSUB2\n"
"s_branch K1_BACK11\n"
"K1_SLP0:\n"
"s_sleep 1\n"
"s_sub_u32 s87, s87, 4\n"
"s_cmp_gt_i32 s87, 0\n"
"s_cbranch_scc1 K1_SLP0\n"
"s_branch K1_SLB0\n"
"K1_SLP1:\n"
"s_sleep 1\n"
"s_sub_u32 s87, s87, 4\n"
"s_cmp_gt_i32 s87, 0\n"
"s_cbranch_scc1 K1_SLP1\n"
"s_branch K1_SLB1\n"
"K1_RSUB0:\n"
"s_nop 3\n"
"v_max3_f32 v48, v160, v161, v162\n"
"v_max3_f32 v48, v48, v163, v164\n"
"v_max3_f32 v48, v48, v165, v166\n"
"v_max_f32 v48, v48, v167\n"
"v_mov_b32 v49, v48\n"
"s_nop 1\n"
"v_permlane32_swap_b32 v48, v49\n"
"s_nop 0\n"
"v_max_f32 v48, v48, v49\n"
"v_mov_b32 v49, v48\n"
"s_nop 1\n"
"v_permlane16_swap_b32 v48, v49\n"
"s_nop 0\n"
"v_max_f32 v48, v48, v49\n"
"v_max_f32 v48, 0, v48\n"
"v_sub_f32 v208, v208, v48\n"
"v_sub_f32 v209, v209, v48\n"
"v_sub_f32 v210, v210, v48\n"
"v_sub_f32 v211, v211, v48\n"
"v_sub_f32 v49, 0, v48\n"
"v_exp_f32 v49, v49\n"
"s_nop 0\n"
"v_mul_f32 v32, v32, v49\n"
"v_accvgpr_read_b32 v50, a0\n"
"v_mul_f32 v50, v50, v49\n"
"v_accvgpr_write_b32 a0, v50\n"
"v_accvgpr_read_b32 v51, a1\n"
"v_mul_f32 v51, v51, v49\n"
"v_accvgpr_write_b32 a1, v51\n"
"v_accvgpr_read_b32 v52, a2\n"
"v_mul_f32 v52, v52, v49\n"
"v_accvgpr_write_b32 a2, v52\n"
"v_accvgpr_read_b32 v53, a3\n"
"v_mul_f32 v53, v53, v49\n"
"v_accvgpr_write_b32 a3, v53\n"
"v_accvgpr_read_b32 v50, a4\n"
"v_mul_f32 v50, v50, v49\n"
"v_accvgpr_write_b32 a4, v50\n"
"v_accvgpr_read_b32 v51, a5\n"
"v_mul_f32 v51, v51, v49\n"
"v_accvgpr_write_b32 a5, v51\n"
"v_accvgpr_read_b32 v52, a6\n"
"v_mul_f32 v52, v52, v49\n"
"v_accvgpr_write_b32 a6, v52\n"
"v_accvgpr_read_b32 v53, a7\n"
"v_mul_f32 v53, v53, v49\n"
"v_accvgpr_write_b32 a7, v53\n"
"v_accvgpr_read_b32 v50, a8\n"
"v_mul_f32 v50, v50, v49\n"
"v_accvgpr_write_b32 a8, v50\n"
"v_accvgpr_read_b32 v51, a9\n"
"v_mul_f32 v51, v51, v49\n"
"v_accvgpr_write_b32 a9, v51\n"
"v_accvgpr_read_b32 v52, a10\n"
"v_mul_f32 v52, v52, v49\n"
"v_accvgpr_write_b32 a10, v52\n"
"v_accvgpr_read_b32 v53, a11\n"
"v_mul_f32 v53, v53, v49\n"
"v_accvgpr_write_b32 a11, v53\n"
"v_accvgpr_read_b32 v50, a12\n"
"v_mul_f32 v50, v50, v49\n"
"v_accvgpr_write_b32 a12, v50\n"
"v_accvgpr_read_b32 v51, a13\n"
"v_mul_f32 v51, v51, v49\n"
"v_accvgpr_write_b32 a13, v51\n"
"v_accvgpr_read_b32 v52, a14\n"
"v_mul_f32 v52, v52, v49\n"
"v_accvgpr_write_b32 a14, v52\n"
"v_accvgpr_read_b32 v53, a15\n"
"v_mul_f32 v53, v53, v49\n"
"v_accvgpr_write_b32 a15, v53\n"
"v_accvgpr_read_b32 v50, a16\n"
"v_mul_f32 v50, v50, v49\n"
"v_accvgpr_write_b32 a16, v50\n"
"v_accvgpr_read_b32 v51, a17\n"
"v_mul_f32 v51, v51, v49\n"
"v_accvgpr_write_b32 a17, v51\n"
"v_accvgpr_read_b32 v52, a18\n"
"v_mul_f32 v52, v52, v49\n"
"v_accvgpr_write_b32 a18, v52\n"
"v_accvgpr_read_b32 v53, a19\n"
"v_mul_f32 v53, v53, v49\n"
"v_accvgpr_write_b32 a19, v53\n"
"v_accvgpr_read_b32 v50, a20\n"
"v_mul_f32 v50, v50, v49\n"
"v_accvgpr_write_b32 a20, v50\n"
"v_accvgpr_read_b32 v51, a21\n"
"v_mul_f32 v51, v51, v49\n"
"v_accvgpr_write_b32 a21, v51\n"
"v_accvgpr_read_b32 v52, a22\n"
"v_mul_f32 v52, v52, v49\n"
"v_accvgpr_write_b32 a22, v52\n"
"v_accvgpr_read_b32 v53, a23\n"
"v_mul_f32 v53, v53, v49\n"
"v_accvgpr_write_b32 a23, v53\n"
"v_accvgpr_read_b32 v50, a24\n"
"v_mul_f32 v50, v50, v49\n"
"v_accvgpr_write_b32 a24, v50\n"
"v_accvgpr_read_b32 v51, a25\n"
"v_mul_f32 v51, v51, v49\n"
"v_accvgpr_write_b32 a25, v51\n"
"v_accvgpr_read_b32 v52, a26\n"
"v_mul_f32 v52, v52, v49\n"
"v_accvgpr_write_b32 a26, v52\n"
"v_accvgpr_read_b32 v53, a27\n"
"v_mul_f32 v53, v53, v49\n"
"v_accvgpr_write_b32 a27, v53\n"
"v_accvgpr_read_b32 v50, a28\n"
"v_mul_f32 v50, v50, v49\n"
"v_accvgpr_write_b32 a28, v50\n"
"v_accvgpr_read_b32 v51, a29\n"
"v_mul_f32 v51, v51, v49\n"
"v_accvgpr_write_b32 a29, v51\n"
"v_accvgpr_read_b32 v52, a30\n"
"v_mul_f32 v52, v52, v49\n"
"v_accvgpr_write_b32 a30, v52\n"
"v_accvgpr_read_b32 v53, a31\n"
"v_mul_f32 v53, v53, v49\n"
"v_accvgpr_write_b32 a31, v53\n"
"v_sub_f32 v160, v160, v48\n"
"v_sub_f32 v161, v161, v48\n"
"v_sub_f32 v162, v162, v48\n"
"v_sub_f32 v163, v163, v48\n"
"v_sub_f32 v164, v164, v48\n"
"v_sub_f32 v165, v165, v48\n"
"v_sub_f32 v166, v166, v48\n"
"v_sub_f32 v167, v167, v48\n"
"v_exp_f32 v160, v160\n"
"v_exp_f32 v161, v161\n"
"v_exp_f32 v162, v162\n"
"v_exp_f32 v163, v163\n"
"v_exp_f32 v164, v164\n"
"v_exp_f32 v165, v165\n"
"v_exp_f32 v166, v166\n"
"v_exp_f32 v167, v167\n"
"s_nop 0\n"
"v_cvt_pk_bf16_f32 v0, v160, v161\n"
"v_cvt_pk_bf16_f32 v1, v162, v163\n"
"v_cvt_pk_bf16_f32 v2, v164, v165\n"
"v_cvt_pk_bf16_f32 v3, v166, v167\n"
"v_max3_f32 v48, v168, v169, v170\n"
"v_max3_f32 v48, v48, v171, v172\n"
"v_max3_f32 v48, v48, v173, v174\n"
"v_max_f32 v48, v48, v175\n"
"v_mov_b32 v49, v48\n"
"s_nop 1\n"
"v_permlane32_swap_b32 v48, v49\n"
"s_nop 0\n"
"v_max_f32 v48, v48, v49\n"
"v_mov_b32 v49, v48\n"
"s_nop 1\n"
"v_permlane16_swap_b32 v48, v49\n"
"s_nop 0\n"
"v_max_f32 v48, v48, v49\n"
"v_max_f32 v48, 0, v48\n"
"v_sub_f32 v212, v212, v48\n"
"v_sub_f32 v213, v213, v48\n"
"v_sub_f32 v214, v214, v48\n"
"v_sub_f32 v215, v215, v48\n"
"v_sub_f32 v49, 0, v48\n"
"v_exp_f32 v49, v49\n"
"s_nop 0\n"
"v_mul_f32 v33, v33, v49\n"
"v_accvgpr_read_b32 v50, a32\n"
"v_mul_f32 v50, v50, v49\n"
"v_accvgpr_write_b32 a32, v50\n"
"v_accvgpr_read_b32 v51, a33\n"
"v_mul_f32 v51, v51, v49\n"
"v_accvgpr_write_b32 a33, v51\n"
"v_accvgpr_read_b32 v52, a34\n"
"v_mul_f32 v52, v52, v49\n"
"v_accvgpr_write_b32 a34, v52\n"
"v_accvgpr_read_b32 v53, a35\n"
"v_mul_f32 v53, v53, v49\n"
"v_accvgpr_write_b32 a35, v53\n"
"v_accvgpr_read_b32 v50, a36\n"
"v_mul_f32 v50, v50, v49\n"
"v_accvgpr_write_b32 a36, v50\n"
"v_accvgpr_read_b32 v51, a37\n"
"v_mul_f32 v51, v51, v49\n"
"v_accvgpr_write_b32 a37, v51\n"
"v_accvgpr_read_b32 v52, a38\n"
"v_mul_f32 v52, v52, v49\n"
"v_accvgpr_write_b32 a38, v52\n"
"v_accvgpr_read_b32 v53, a39\n"
"v_mul_f32 v53, v53, v49\n"
"v_accvgpr_write_b32 a39, v53\n"
"v_accvgpr_read_b32 v50, a40\n"
"v_mul_f32 v50, v50, v49\n"
"v_accvgpr_write_b32 a40, v50\n"
"v_accvgpr_read_b32 v51, a41\n"
"v_mul_f32 v51, v51, v49\n"
"v_accvgpr_write_b32 a41, v51\n"
"v_accvgpr_read_b32 v52, a42\n"
"v_mul_f32 v52, v52, v49\n"
"v_accvgpr_write_b32 a42, v52\n"
"v_accvgpr_read_b32 v53, a43\n"
"v_mul_f32 v53, v53, v49\n"
"v_accvgpr_write_b32 a43, v53\n"
"v_accvgpr_read_b32 v50, a44\n"
"v_mul_f32 v50, v50, v49\n"
"v_accvgpr_write_b32 a44, v50\n"
"v_accvgpr_read_b32 v51, a45\n"
"v_mul_f32 v51, v51, v49\n"
"v_accvgpr_write_b32 a45, v51\n"
"v_accvgpr_read_b32 v52, a46\n"
"v_mul_f32 v52, v52, v49\n"
"v_accvgpr_write_b32 a46, v52\n"
"v_accvgpr_read_b32 v53, a47\n"
"v_mul_f32 v53, v53, v49\n"
"v_accvgpr_write_b32 a47, v53\n"
"v_accvgpr_read_b32 v50, a48\n"
"v_mul_f32 v50, v50, v49\n"
"v_accvgpr_write_b32 a48, v50\n"
"v_accvgpr_read_b32 v51, a49\n"
"v_mul_f32 v51, v51, v49\n"
"v_accvgpr_write_b32 a49, v51\n"
"v_accvgpr_read_b32 v52, a50\n"
"v_mul_f32 v52, v52, v49\n"
"v_accvgpr_write_b32 a50, v52\n"
"v_accvgpr_read_b32 v53, a51\n"
"v_mul_f32 v53, v53, v49\n"
"v_accvgpr_write_b32 a51, v53\n"
"v_accvgpr_read_b32 v50, a52\n"
"v_mul_f32 v50, v50, v49\n"
"v_accvgpr_write_b32 a52, v50\n"
"v_accvgpr_read_b32 v51, a53\n"
"v_mul_f32 v51, v51, v49\n"
"v_accvgpr_write_b32 a53, v51\n"
"v_accvgpr_read_b32 v52, a54\n"
"v_mul_f32 v52, v52, v49\n"
"v_accvgpr_write_b32 a54, v52\n"
"v_accvgpr_read_b32 v53, a55\n"
"v_mul_f32 v53, v53, v49\n"
"v_accvgpr_write_b32 a55, v53\n"
"v_accvgpr_read_b32 v50, a56\n"
"v_mul_f32 v50, v50, v49\n"
"v_accvgpr_write_b32 a56, v50\n"
"v_accvgpr_read_b32 v51, a57\n"
"v_mul_f32 v51, v51, v49\n"
"v_accvgpr_write_b32 a57, v51\n"
"v_accvgpr_read_b32 v52, a58\n"
"v_mul_f32 v52, v52, v49\n"
"v_accvgpr_write_b32 a58, v52\n"
"v_accvgpr_read_b32 v53, a59\n"
"v_mul_f32 v53, v53, v49\n"
"v_accvgpr_write_b32 a59, v53\n"
"v_accvgpr_read_b32 v50, a60\n"
"v_mul_f32 v50, v50, v49\n"
"v_accvgpr_write_b32 a60, v50\n"
"v_accvgpr_read_b32 v51, a61\n"
"v_mul_f32 v51, v51, v49\n"
"v_accvgpr_write_b32 a61, v51\n"
"v_accvgpr_read_b32 v52, a62\n"
"v_mul_f32 v52, v52, v49\n"
"v_accvgpr_write_b32 a62, v52\n"
"v_accvgpr_read_b32 v53, a63\n"
"v_mul_f32 v53, v53, v49\n"
"v_accvgpr_write_b32 a63, v53\n"
"v_sub_f32 v168, v168, v48\n"
"v_sub_f32 v169, v169, v48\n"
"v_sub_f32 v170, v170, v48\n"
"v_sub_f32 v171, v171, v48\n"
"v_sub_f32 v172, v172, v48\n"
"v_sub_f32 v173, v173, v48\n"
"v_sub_f32 v174, v174, v48\n"
"v_sub_f32 v175, v175, v48\n"
"v_exp_f32 v168, v168\n"
"v_exp_f32 v169, v169\n"
"v_exp_f32 v170, v170\n"
"v_exp_f32 v171, v171\n"
"v_exp_f32 v172, v172\n"
"v_exp_f32 v173, v173\n"
"v_exp_f32 v174, v174\n"
"v_exp_f32 v175, v175\n"
"s_nop 0\n"
"v_cvt_pk_bf16_f32 v4, v168, v169\n"
"v_cvt_pk_bf16_f32 v5, v170, v171\n"
"v_cvt_pk_bf16_f32 v6, v172, v173\n"
"v_cvt_pk_bf16_f32 v7, v174, v175\n"
"s_nop 3\n"
"v_mfma_f32_16x16x32_bf16 v[232:235], v[58:61], v[0:3], 0\n"
"v_mfma_f32_16x16x32_bf16 v[236:239], v[58:61], v[4:7], 0\n"
"s_nop 11\n"
"s_setpc_b64 s[62:63]\n"
"K1_RSUB1:\n"
"s_nop 3\n"
"v_max3_f32 v48, v176, v177, v178\n"
"v_max3_f32 v48, v48, v179, v180\n"
"v_max3_f32 v48, v48, v181, v182\n"
"v_max_f32 v48, v48, v183\n"
"v_mov_b32 v49, v48\n"
"s_nop 1\n"
"v_permlane32_swap_b32 v48, v49\n"
"s_nop 0\n"
"v_max_f32 v48, v48, v49\n"
"v_mov_b32 v49, v48\n"
"s_nop 1\n"
"v_permlane16_swap_b32 v48, v49\n"
"s_nop 0\n"
"v_max_f32 v48, v48, v49\n"
"v_max_f32 v48, 0, v48\n"
"v_sub_f32 v216, v216, v48\n"
"v_sub_f32 v217, v217, v48\n"
"v_sub_f32 v218, v218, v48\n"
"v_sub_f32 v219, v219, v48\n"
"v_sub_f32 v49, 0, v48\n"
"v_exp_f32 v49, v49\n"
"s_nop 0\n"
"v_mul_f32 v34, v34, v49\n"
"v_accvgpr_read_b32 v50, a64\n"
"v_mul_f32 v50, v50, v49\n"
"v_accvgpr_write_b32 a64, v50\n"
"v_accvgpr_read_b32 v51, a65\n"
"v_mul_f32 v51, v51, v49\n"
"v_accvgpr_write_b32 a65, v51\n"
"v_accvgpr_read_b32 v52, a66\n"
"v_mul_f32 v52, v52, v49\n"
"v_accvgpr_write_b32 a66, v52\n"
"v_accvgpr_read_b32 v53, a67\n"
"v_mul_f32 v53, v53, v49\n"
"v_accvgpr_write_b32 a67, v53\n"
"v_accvgpr_read_b32 v50, a68\n"
"v_mul_f32 v50, v50, v49\n"
"v_accvgpr_write_b32 a68, v50\n"
"v_accvgpr_read_b32 v51, a69\n"
"v_mul_f32 v51, v51, v49\n"
"v_accvgpr_write_b32 a69, v51\n"
"v_accvgpr_read_b32 v52, a70\n"
"v_mul_f32 v52, v52, v49\n"
"v_accvgpr_write_b32 a70, v52\n"
"v_accvgpr_read_b32 v53, a71\n"
"v_mul_f32 v53, v53, v49\n"
"v_accvgpr_write_b32 a71, v53\n"
"v_accvgpr_read_b32 v50, a72\n"
"v_mul_f32 v50, v50, v49\n"
"v_accvgpr_write_b32 a72, v50\n"
"v_accvgpr_read_b32 v51, a73\n"
"v_mul_f32 v51, v51, v49\n"
"v_accvgpr_write_b32 a73, v51\n"
"v_accvgpr_read_b32 v52, a74\n"
"v_mul_f32 v52, v52, v49\n"
"v_accvgpr_write_b32 a74, v52\n"
"v_accvgpr_read_b32 v53, a75\n"
"v_mul_f32 v53, v53, v49\n"
"v_accvgpr_write_b32 a75, v53\n"
"v_accvgpr_read_b32 v50, a76\n"
"v_mul_f32 v50, v50, v49\n"
"v_accvgpr_write_b32 a76, v50\n"
"v_accvgpr_read_b32 v51, a77\n"
"v_mul_f32 v51, v51, v49\n"
"v_accvgpr_write_b32 a77, v51\n"
"v_accvgpr_read_b32 v52, a78\n"
"v_mul_f32 v52, v52, v49\n"
"v_accvgpr_write_b32 a78, v52\n"
"v_accvgpr_read_b32 v53, a79\n"
"v_mul_f32 v53, v53, v49\n"
"v_accvgpr_write_b32 a79, v53\n"
"v_accvgpr_read_b32 v50, a80\n"
"v_mul_f32 v50, v50, v49\n"
"v_accvgpr_write_b32 a80, v50\n"
"v_accvgpr_read_b32 v51, a81\n"
"v_mul_f32 v51, v51, v49\n"
"v_accvgpr_write_b32 a81, v51\n"
"v_accvgpr_read_b32 v52, a82\n"
"v_mul_f32 v52, v52, v49\n"
"v_accvgpr_write_b32 a82, v52\n"
"v_accvgpr_read_b32 v53, a83\n"
"v_mul_f32 v53, v53, v49\n"
"v_accvgpr_write_b32 a83, v53\n"
"v_accvgpr_read_b32 v50, a84\n"
"v_mul_f32 v50, v50, v49\n"
"v_accvgpr_write_b32 a84, v50\n"
"v_accvgpr_read_b32 v51, a85\n"
"v_mul_f32 v51, v51, v49\n"
"v_accvgpr_write_b32 a85, v51\n"
"v_accvgpr_read_b32 v52, a86\n"
"v_mul_f32 v52, v52, v49\n"
"v_accvgpr_write_b32 a86, v52\n"
"v_accvgpr_read_b32 v53, a87\n"
"v_mul_f32 v53, v53, v49\n"
"v_accvgpr_write_b32 a87, v53\n"
"v_accvgpr_read_b32 v50, a88\n"
"v_mul_f32 v50, v50, v49\n"
"v_accvgpr_write_b32 a88, v50\n"
"v_accvgpr_read_b32 v51, a89\n"
"v_mul_f32 v51, v51, v49\n"
"v_accvgpr_write_b32 a89, v51\n"
"v_accvgpr_read_b32 v52, a90\n"
"v_mul_f32 v52, v52, v49\n"
"v_accvgpr_write_b32 a90, v52\n"
"v_accvgpr_read_b32 v53, a91\n"
"v_mul_f32 v53, v53, v49\n"
"v_accvgpr_write_b32 a91, v53\n"
"v_accvgpr_read_b32 v50, a92\n"
"v_mul_f32 v50, v50, v49\n"
"v_accvgpr_write_b32 a92, v50\n"
"v_accvgpr_read_b32 v51, a93\n"
"v_mul_f32 v51, v51, v49\n"
"v_accvgpr_write_b32 a93, v51\n"
"v_accvgpr_read_b32 v52, a94\n"
"v_mul_f32 v52, v52, v49\n"
"v_accvgpr_write_b32 a94, v52\n"
"v_accvgpr_read_b32 v53, a95\n"
"v_mul_f32 v53, v53, v49\n"
"v_accvgpr_write_b32 a95, v53\n"
"v_sub_f32 v176, v176, v48\n"
"v_sub_f32 v177, v177, v48\n"
"v_sub_f32 v178, v178, v48\n"
"v_sub_f32 v179, v179, v48\n"
"v_sub_f32 v180, v180, v48\n"
"v_sub_f32 v181, v181, v48\n"
"v_sub_f32 v182, v182, v48\n"
"v_sub_f32 v183, v183, v48\n"
"v_exp_f32 v176, v176\n"
"v_exp_f32 v177, v177\n"
"v_exp_f32 v178, v178\n"
"v_exp_f32 v179, v179\n"
"v_exp_f32 v180, v180\n"
"v_exp_f32 v181, v181\n"
"v_exp_f32 v182, v182\n"
"v_exp_f32 v183, v183\n"
"s_nop 0\n"
"v_cvt_pk_bf16_f32 v8, v176, v177\n"
"v_cvt_pk_bf16_f32 v9, v178, v179\n"
"v_cvt_pk_bf16_f32 v10, v180, v181\n"
"v_cvt_pk_bf16_f32 v11, v182, v183\n"
"v_max3_f32 v48, v184, v185, v186\n"
"v_max3_f32 v48, v48, v187, v188\n"
"v_max3_f32 v48, v48, v189, v190\n"
"v_max_f32 v48, v48, v191\n"
"v_mov_b32 v49, v48\n"
"s_nop 1\n"
"v_permlane32_swap_b32 v48, v49\n"
"s_nop 0\n"
"v_max_f32 v48, v48, v49\n"
"v_mov_b32 v49, v48\n"
"s_nop 1\n"
"v_permlane16_swap_b32 v48, v49\n"
"s_nop 0\n"
"v_max_f32 v48, v48, v49\n"
"v_max_f32 v48, 0, v48\n"
"v_sub_f32 v220, v220, v48\n"
"v_sub_f32 v221, v221, v48\n"
"v_sub_f32 v222, v222, v48\n"
"v_sub_f32 v223, v223, v48\n"
"v_sub_f32 v49, 0, v48\n"
"v_exp_f32 v49, v49\n"
"s_nop 0\n"
"v_mul_f32 v35, v35, v49\n"
"v_accvgpr_read_b32 v50, a96\n"
"v_mul_f32 v50, v50, v49\n"
"v_accvgpr_write_b32 a96, v50\n"
"v_accvgpr_read_b32 v51, a97\n"
"v_mul_f32 v51, v51, v49\n"
"v_accvgpr_write_b32 a97, v51\n"
"v_accvgpr_read_b32 v52, a98\n"
"v_mul_f32 v52, v52, v49\n"
"v_accvgpr_write_b32 a98, v52\n"
"v_accvgpr_read_b32 v53, a99\n"
"v_mul_f32 v53, v53, v49\n"
"v_accvgpr_write_b32 a99, v53\n"
"v_accvgpr_read_b32 v50, a100\n"
"v_mul_f32 v50, v50, v49\n"
"v_accvgpr_write_b32 a100, v50\n"
"v_accvgpr_read_b32 v51, a101\n"
"v_mul_f32 v51, v51, v49\n"
"v_accvgpr_write_b32 a101, v51\n"
"v_accvgpr_read_b32 v52, a102\n"
"v_mul_f32 v52, v52, v49\n"
"v_accvgpr_write_b32 a102, v52\n"
"v_accvgpr_read_b32 v53, a103\n"
"v_mul_f32 v53, v53, v49\n"
"v_accvgpr_write_b32 a103, v53\n"
"v_accvgpr_read_b32 v50, a104\n"
"v_mul_f32 v50, v50, v49\n"
"v_accvgpr_write_b32 a104, v50\n"
"v_accvgpr_read_b32 v51, a105\n"
"v_mul_f32 v51, v51, v49\n"
"v_accvgpr_write_b32 a105, v51\n"
"v_accvgpr_read_b32 v52, a106\n"
"v_mul_f32 v52, v52, v49\n"
"v_accvgpr_write_b32 a106, v52\n"
"v_accvgpr_read_b32 v53, a107\n"
"v_mul_f32 v53, v53, v49\n"
"v_accvgpr_write_b32 a107, v53\n"
"v_accvgpr_read_b32 v50, a108\n"
"v_mul_f32 v50, v50, v49\n"
"v_accvgpr_write_b32 a108, v50\n"
"v_accvgpr_read_b32 v51, a109\n"
"v_mul_f32 v51, v51, v49\n"
"v_accvgpr_write_b32 a109, v51\n"
"v_accvgpr_read_b32 v52, a110\n"
"v_mul_f32 v52, v52, v49\n"
"v_accvgpr_write_b32 a110, v52\n"
"v_accvgpr_read_b32 v53, a111\n"
"v_mul_f32 v53, v53, v49\n"
"v_accvgpr_write_b32 a111, v53\n"
"v_accvgpr_read_b32 v50, a112\n"
"v_mul_f32 v50, v50, v49\n"
"v_accvgpr_write_b32 a112, v50\n"
"v_accvgpr_read_b32 v51, a113\n"
"v_mul_f32 v51, v51, v49\n"
"v_accvgpr_write_b32 a113, v51\n"
"v_accvgpr_read_b32 v52, a114\n"
"v_mul_f32 v52, v52, v49\n"
"v_accvgpr_write_b32 a114, v52\n"
"v_accvgpr_read_b32 v53, a115\n"
"v_mul_f32 v53, v53, v49\n"
"v_accvgpr_write_b32 a115, v53\n"
"v_accvgpr_read_b32 v50, a116\n"
"v_mul_f32 v50, v50, v49\n"
"v_accvgpr_write_b32 a116, v50\n"
"v_accvgpr_read_b32 v51, a117\n"
"v_mul_f32 v51, v51, v49\n"
"v_accvgpr_write_b32 a117, v51\n"
"v_accvgpr_read_b32 v52, a118\n"
"v_mul_f32 v52, v52, v49\n"
"v_accvgpr_write_b32 a118, v52\n"
"v_accvgpr_read_b32 v53, a119\n"
"v_mul_f32 v53, v53, v49\n"
"v_accvgpr_write_b32 a119, v53\n"
"v_accvgpr_read_b32 v50, a120\n"
"v_mul_f32 v50, v50, v49\n"
"v_accvgpr_write_b32 a120, v50\n"
"v_accvgpr_read_b32 v51, a121\n"
"v_mul_f32 v51, v51, v49\n"
"v_accvgpr_write_b32 a121, v51\n"
"v_accvgpr_read_b32 v52, a122\n"
"v_mul_f32 v52, v52, v49\n"
"v_accvgpr_write_b32 a122, v52\n"
"v_accvgpr_read_b32 v53, a123\n"
"v_mul_f32 v53, v53, v49\n"
"v_accvgpr_write_b32 a123, v53\n"
"v_accvgpr_read_b32 v50, a124\n"
"v_mul_f32 v50, v50, v49\n"
"v_accvgpr_write_b32 a124, v50\n"
"v_accvgpr_read_b32 v51, a125\n"
"v_mul_f32 v51, v51, v49\n"
"v_accvgpr_write_b32 a125, v51\n"
"v_accvgpr_read_b32 v52, a126\n"
"v_mul_f32 v52, v52, v49\n"
"v_accvgpr_write_b32 a126, v52\n"
"v_accvgpr_read_b32 v53, a127\n"
"v_mul_f32 v53, v53, v49\n"
"v_accvgpr_write_b32 a127, v53\n"
"v_sub_f32 v184, v184, v48\n"
"v_sub_f32 v185, v185, v48\n"
"v_sub_f32 v186, v186, v48\n"
"v_sub_f32 v187, v187, v48\n"
"v_sub_f32 v188, v188, v48\n"
"v_sub_f32 v189, v189, v48\n"
"v_sub_f32 v190, v190, v48\n"
"v_sub_f32 v191, v191, v48\n"
"v_exp_f32 v184, v184\n"
"v_exp_f32 v185, v185\n"
"v_exp_f32 v186, v186\n"
"v_exp_f32 v187, v187\n"
"v_exp_f32 v188, v188\n"
"v_exp_f32 v189, v189\n"
"v_exp_f32 v190, v190\n"
"v_exp_f32 v191, v191\n"
"s_nop 0\n"
"v_cvt_pk_bf16_f32 v12, v184, v185\n"
"v_cvt_pk_bf16_f32 v13, v186, v187\n"
"v_cvt_pk_bf16_f32 v14, v188, v189\n"
"v_cvt_pk_bf16_f32 v15, v190, v191\n"
"s_nop 3\n"
"v_mfma_f32_16x16x32_bf16 v[240:243], v[58:61], v[8:11], 0\n"
"v_mfma_f32_16x16x32_bf16 v[244:247], v[58:61], v[12:15], 0\n"
"s_nop 11\n"
"s_setpc_b64 s[62:63]\n"
"K1_RSUB2:\n"
"s_nop 3\n"
"v_max3_f32 v48, v192, v193, v194\n"
"v_max3_f32 v48, v48, v195, v196\n"
"v_max3_f32 v48, v48, v197, v198\n"
"v_max_f32 v48, v48, v199\n"
"v_mov_b32 v49, v48\n"
"s_nop 1\n"
"v_permlane32_swap_b32 v48, v49\n"
"s_nop 0\n"
"v_max_f32 v48, v48, v49\n"
"v_mov_b32 v49, v48\n"
"s_nop 1\n"
"v_permlane16_swap_b32 v48, v49\n"
"s_nop 0\n"
"v_max_f32 v48, v48, v49\n"
"v_max_f32 v48, 0, v48\n"
"v_sub_f32 v224, v224, v48\n"
"v_sub_f32 v225, v225, v48\n"
"v_sub_f32 v226, v226, v48\n"
"v_sub_f32 v227, v227, v48\n"
"v_sub_f32 v49, 0, v48\n"
"v_exp_f32 v49, v49\n"
"s_nop 0\n"
"v_mul_f32 v36, v36, v49\n"
"v_accvgpr_read_b32 v50, a128\n"
"v_mul_f32 v50, v50, v49\n"
"v_accvgpr_write_b32 a128, v50\n"
"v_accvgpr_read_b32 v51, a129\n"
"v_mul_f32 v51, v51, v49\n"
"v_accvgpr_write_b32 a129, v51\n"
"v_accvgpr_read_b32 v52, a130\n"
"v_mul_f32 v52, v52, v49\n"
"v_accvgpr_write_b32 a130, v52\n"
"v_accvgpr_read_b32 v53, a131\n"
"v_mul_f32 v53, v53, v49\n"
"v_accvgpr_write_b32 a131, v53\n"
"v_accvgpr_read_b32 v50, a132\n"
"v_mul_f32 v50, v50, v49\n"
"v_accvgpr_write_b32 a132, v50\n"
"v_accvgpr_read_b32 v51, a133\n"
"v_mul_f32 v51, v51, v49\n"
"v_accvgpr_write_b32 a133, v51\n"
"v_accvgpr_read_b32 v52, a134\n"
"v_mul_f32 v52, v52, v49\n"
"v_accvgpr_write_b32 a134, v52\n"
"v_accvgpr_read_b32 v53, a135\n"
"v_mul_f32 v53, v53, v49\n"
"v_accvgpr_write_b32 a135, v53\n"
"v_accvgpr_read_b32 v50, a136\n"
"v_mul_f32 v50, v50, v49\n"
"v_accvgpr_write_b32 a136, v50\n"
"v_accvgpr_read_b32 v51, a137\n"
"v_mul_f32 v51, v51, v49\n"
"v_accvgpr_write_b32 a137, v51\n"
"v_accvgpr_read_b32 v52, a138\n"
"v_mul_f32 v52, v52, v49\n"
"v_accvgpr_write_b32 a138, v52\n"
"v_accvgpr_read_b32 v53, a139\n"
"v_mul_f32 v53, v53, v49\n"
"v_accvgpr_write_b32 a139, v53\n"
"v_accvgpr_read_b32 v50, a140\n"
"v_mul_f32 v50, v50, v49\n"
"v_accvgpr_write_b32 a140, v50\n"
"v_accvgpr_read_b32 v51, a141\n"
"v_mul_f32 v51, v51, v49\n"
"v_accvgpr_write_b32 a141, v51\n"
"v_accvgpr_read_b32 v52, a142\n"
"v_mul_f32 v52, v52, v49\n"
"v_accvgpr_write_b32 a142, v52\n"
"v_accvgpr_read_b32 v53, a143\n"
"v_mul_f32 v53, v53, v49\n"
"v_accvgpr_write_b32 a143, v53\n"
"v_accvgpr_read_b32 v50, a144\n"
"v_mul_f32 v50, v50, v49\n"
"v_accvgpr_write_b32 a144, v50\n"
"v_accvgpr_read_b32 v51, a145\n"
"v_mul_f32 v51, v51, v49\n"
"v_accvgpr_write_b32 a145, v51\n"
"v_accvgpr_read_b32 v52, a146\n"
"v_mul_f32 v52, v52, v49\n"
"v_accvgpr_write_b32 a146, v52\n"
"v_accvgpr_read_b32 v53, a147\n"
"v_mul_f32 v53, v53, v49\n"
"v_accvgpr_write_b32 a147, v53\n"
"v_accvgpr_read_b32 v50, a148\n"
"v_mul_f32 v50, v50, v49\n"
"v_accvgpr_write_b32 a148, v50\n"
"v_accvgpr_read_b32 v51, a149\n"
"v_mul_f32 v51, v51, v49\n"
"v_accvgpr_write_b32 a149, v51\n"
"v_accvgpr_read_b32 v52, a150\n"
"v_mul_f32 v52, v52, v49\n"
"v_accvgpr_write_b32 a150, v52\n"
"v_accvgpr_read_b32 v53, a151\n"
"v_mul_f32 v53, v53, v49\n"
"v_accvgpr_write_b32 a151, v53\n"
"v_accvgpr_read_b32 v50, a152\n"
"v_mul_f32 v50, v50, v49\n"
"v_accvgpr_write_b32 a152, v50\n"
"v_accvgpr_read_b32 v51, a153\n"
"v_mul_f32 v51, v51, v49\n"
"v_accvgpr_write_b32 a153, v51\n"
"v_accvgpr_read_b32 v52, a154\n"
"v_mul_f32 v52, v52, v49\n"
"v_accvgpr_write_b32 a154, v52\n"
"v_accvgpr_read_b32 v53, a155\n"
"v_mul_f32 v53, v53, v49\n"
"v_accvgpr_write_b32 a155, v53\n"
"v_accvgpr_read_b32 v50, a156\n"
"v_mul_f32 v50, v50, v49\n"
"v_accvgpr_write_b32 a156, v50\n"
"v_accvgpr_read_b32 v51, a157\n"
"v_mul_f32 v51, v51, v49\n"
"v_accvgpr_write_b32 a157, v51\n"
"v_accvgpr_read_b32 v52, a158\n"
"v_mul_f32 v52, v52, v49\n"
"v_accvgpr_write_b32 a158, v52\n"
"v_accvgpr_read_b32 v53, a159\n"
"v_mul_f32 v53, v53, v49\n"
"v_accvgpr_write_b32 a159, v53\n"
"v_sub_f32 v192, v192, v48\n"
"v_sub_f32 v193, v193, v48\n"
"v_sub_f32 v194, v194, v48\n"
"v_sub_f32 v195, v195, v48\n"
"v_sub_f32 v196, v196, v48\n"
"v_sub_f32 v197, v197, v48\n"
"v_sub_f32 v198, v198, v48\n"
"v_sub_f32 v199, v199, v48\n"
"v_exp_f32 v192, v192\n"
"v_exp_f32 v193, v193\n"
"v_exp_f32 v194, v194\n"
"v_exp_f32 v195, v195\n"
"v_exp_f32 v196, v196\n"
"v_exp_f32 v197, v197\n"
"v_exp_f32 v198, v198\n"
"v_exp_f32 v199, v199\n"
"s_nop 0\n"
"v_cvt_pk_bf16_f32 v16, v192, v193\n"
"v_cvt_pk_bf16_f32 v17, v194, v195\n"
"v_cvt_pk_bf16_f32 v18, v196, v197\n"
"v_cvt_pk_bf16_f32 v19, v198, v199\n"
"v_max3_f32 v48, v200, v201, v202\n"
"v_max3_f32 v48, v48, v203, v204\n"
"v_max3_f32 v48, v48, v205, v206\n"
"v_max_f32 v48, v48, v207\n"
"v_mov_b32 v49, v48\n"
"s_nop 1\n"
"v_permlane32_swap_b32 v48, v49\n"
"s_nop 0\n"
"v_max_f32 v48, v48, v49\n"
"v_mov_b32 v49, v48\n"
"s_nop 1\n"
"v_permlane16_swap_b32 v48, v49\n"
"s_nop 0\n"
"v_max_f32 v48, v48, v49\n"
"v_max_f32 v48, 0, v48\n"
"v_sub_f32 v228, v228, v48\n"
"v_sub_f32 v229, v229, v48\n"
"v_sub_f32 v230, v230, v48\n"
"v_sub_f32 v231, v231, v48\n"
"v_sub_f32 v49, 0, v48\n"
"v_exp_f32 v49, v49\n"
"s_nop 0\n"
"v_mul_f32 v37, v37, v49\n"
"v_accvgpr_read_b32 v50, a160\n"
"v_mul_f32 v50, v50, v49\n"
"v_accvgpr_write_b32 a160, v50\n"
"v_accvgpr_read_b32 v51, a161\n"
"v_mul_f32 v51, v51, v49\n"
"v_accvgpr_write_b32 a161, v51\n"
"v_accvgpr_read_b32 v52, a162\n"
"v_mul_f32 v52, v52, v49\n"
"v_accvgpr_write_b32 a162, v52\n"
"v_accvgpr_read_b32 v53, a163\n"
"v_mul_f32 v53, v53, v49\n"
"v_accvgpr_write_b32 a163, v53\n"
"v_accvgpr_read_b32 v50, a164\n"
"v_mul_f32 v50, v50, v49\n"
"v_accvgpr_write_b32 a164, v50\n"
"v_accvgpr_read_b32 v51, a165\n"
"v_mul_f32 v51, v51, v49\n"
"v_accvgpr_write_b32 a165, v51\n"
"v_accvgpr_read_b32 v52, a166\n"
"v_mul_f32 v52, v52, v49\n"
"v_accvgpr_write_b32 a166, v52\n"
"v_accvgpr_read_b32 v53, a167\n"
"v_mul_f32 v53, v53, v49\n"
"v_accvgpr_write_b32 a167, v53\n"
"v_accvgpr_read_b32 v50, a168\n"
"v_mul_f32 v50, v50, v49\n"
"v_accvgpr_write_b32 a168, v50\n"
"v_accvgpr_read_b32 v51, a169\n"
"v_mul_f32 v51, v51, v49\n"
"v_accvgpr_write_b32 a169, v51\n"
"v_accvgpr_read_b32 v52, a170\n"
"v_mul_f32 v52, v52, v49\n"
"v_accvgpr_write_b32 a170, v52\n"
"v_accvgpr_read_b32 v53, a171\n"
"v_mul_f32 v53, v53, v49\n"
"v_accvgpr_write_b32 a171, v53\n"
"v_accvgpr_read_b32 v50, a172\n"
"v_mul_f32 v50, v50, v49\n"
"v_accvgpr_write_b32 a172, v50\n"
"v_accvgpr_read_b32 v51, a173\n"
"v_mul_f32 v51, v51, v49\n"
"v_accvgpr_write_b32 a173, v51\n"
"v_accvgpr_read_b32 v52, a174\n"
"v_mul_f32 v52, v52, v49\n"
"v_accvgpr_write_b32 a174, v52\n"
"v_accvgpr_read_b32 v53, a175\n"
"v_mul_f32 v53, v53, v49\n"
"v_accvgpr_write_b32 a175, v53\n"
"v_accvgpr_read_b32 v50, a176\n"
"v_mul_f32 v50, v50, v49\n"
"v_accvgpr_write_b32 a176, v50\n"
"v_accvgpr_read_b32 v51, a177\n"
"v_mul_f32 v51, v51, v49\n"
"v_accvgpr_write_b32 a177, v51\n"
"v_accvgpr_read_b32 v52, a178\n"
"v_mul_f32 v52, v52, v49\n"
"v_accvgpr_write_b32 a178, v52\n"
"v_accvgpr_read_b32 v53, a179\n"
"v_mul_f32 v53, v53, v49\n"
"v_accvgpr_write_b32 a179, v53\n"
"v_accvgpr_read_b32 v50, a180\n"
"v_mul_f32 v50, v50, v49\n"
"v_accvgpr_write_b32 a180, v50\n"
"v_accvgpr_read_b32 v51, a181\n"
"v_mul_f32 v51, v51, v49\n"
"v_accvgpr_write_b32 a181, v51\n"
"v_accvgpr_read_b32 v52, a182\n"
"v_mul_f32 v52, v52, v49\n"
"v_accvgpr_write_b32 a182, v52\n"
"v_accvgpr_read_b32 v53, a183\n"
"v_mul_f32 v53, v53, v49\n"
"v_accvgpr_write_b32 a183, v53\n"
"v_accvgpr_read_b32 v50, a184\n"
"v_mul_f32 v50, v50, v49\n"
"v_accvgpr_write_b32 a184, v50\n"
"v_accvgpr_read_b32 v51, a185\n"
"v_mul_f32 v51, v51, v49\n"
"v_accvgpr_write_b32 a185, v51\n"
"v_accvgpr_read_b32 v52, a186\n"
"v_mul_f32 v52, v52, v49\n"
"v_accvgpr_write_b32 a186, v52\n"
"v_accvgpr_read_b32 v53, a187\n"
"v_mul_f32 v53, v53, v49\n"
"v_accvgpr_write_b32 a187, v53\n"
"v_accvgpr_read_b32 v50, a188\n"
"v_mul_f32 v50, v50, v49\n"
"v_accvgpr_write_b32 a188, v50\n"
"v_accvgpr_read_b32 v51, a189\n"
"v_mul_f32 v51, v51, v49\n"
"v_accvgpr_write_b32 a189, v51\n"
"v_accvgpr_read_b32 v52, a190\n"
"v_mul_f32 v52, v52, v49\n"
"v_accvgpr_write_b32 a190, v52\n"
"v_accvgpr_read_b32 v53, a191\n"
"v_mul_f32 v53, v53, v49\n"
"v_accvgpr_write_b32 a191, v53\n"
"v_sub_f32 v200, v200, v48\n"
"v_sub_f32 v201, v201, v48\n"
"v_sub_f32 v202, v202, v48\n"
"v_sub_f32 v203, v203, v48\n"
"v_sub_f32 v204, v204, v48\n"
"v_sub_f32 v205, v205, v48\n"
"v_sub_f32 v206, v206, v48\n"
"v_sub_f32 v207, v207, v48\n"
"v_exp_f32 v200, v200\n"
"v_exp_f32 v201, v201\n"
"v_exp_f32 v202, v202\n"
"v_exp_f32 v203, v203\n"
"v_exp_f32 v204, v204\n"
"v_exp_f32 v205, v205\n"
"v_exp_f32 v206, v206\n"
"v_exp_f32 v207, v207\n"
"s_nop 0\n"
"v_cvt_pk_bf16_f32 v20, v200, v201\n"
"v_cvt_pk_bf16_f32 v21, v202, v203\n"
"v_cvt_pk_bf16_f32 v22, v204, v205\n"
"v_cvt_pk_bf16_f32 v23, v206, v207\n"
"s_nop 3\n"
"v_mfma_f32_16x16x32_bf16 v[248:251], v[58:61], v[16:19], 0\n"
"v_mfma_f32_16x16x32_bf16 v[252:255], v[58:61], v[20:23], 0\n"
"s_nop 11\n"
"s_setpc_b64 s[62:63]\n"
"K1_END:\n"
  :: [ka] "s"(__builtin_amdgcn_kernarg_segment_ptr()), [wg] "s"(blockIdx.x), [w] "s"(w), [lds] "s"((unsigned)(size_t)lds)
  : "v0","v1","v2","v3","v4","v5","v6","v7","v8","v9","v10","v11","v12","v13","v14","v15","v16","v17","v18","v19","v20","v21","v22","v23","v24","v25","v26","v27","v28","v29","v30","v31","v32","v33","v34","v35","v36","v37","v38","v39","v40","v41","v42","v43","v44","v45","v46","v47","v48","v49","v50","v51","v52","v53","v54","v55","v56","v57","v58","v59","v60","v61","v62","v63","v64","v65","v66","v67","v68","v69","v70","v71","v72","v73","v74","v75","v76","v77","v78","v79","v80","v81","v82","v83","v84","v85","v86","v87","v88","v89","v90","v91","v92","v93","v94","v95","v96","v97","v98","v99","v100","v101","v102","v103","v104","v105","v106","v107","v108","v109","v110","v111","v112","v113","v114","v115","v116","v117","v118","v119","v120","v121","v122","v123","v124","v125","v126","v127","v128","v129","v130","v131","v132","v133","v134","v135","v136","v137","v138","v139","v140","v141","v142","v143","v144","v145","v146","v147","v148","v149","v150","v151","v152","v153","v154","v155","v156","v157","v158","v159","v160","v161","v162","v163","v164","v165","v166","v167","v168","v169","v170","v171","v172","v173","v174","v175","v176","v177","v178","v179","v180","v181","v182","v183","v184","v185","v186","v187","v188","v189","v190","v191","v192","v193","v194","v195","v196","v197","v198","v199","v200","v201","v202","v203","v204","v205","v206","v207","v208","v209","v210","v211","v212","v213","v214","v215","v216","v217","v218","v219","v220","v221","v222","v223","v224","v225","v226","v227","v228","v229","v230","v231","v232","v233","v234","v235","v236","v237","v238","v239","v240","v241","v242","v243","v244","v245","v246","v247","v248","v249","v250","v251","v252","v253","v254","v255","a0","a1","a2","a3","a4","a5","a6","a7","a8","a9","a10","a11","a12","a13","a14","a15","a16","a17","a18","a19","a20","a21","a22","a23","a24","a25","a26","a27","a28","a29","a30","a31","a32","a33","a34","a35","a36","a37","a38","a39","a40","a41","a42","a43","a44","a45","a46","a47","a48","a49","a50","a51","a52","a53","a54","a55","a56","a57","a58","a59","a60","a61","a62","a63","a64","a65","a66","a67","a68","a69","a70","a71","a72","a73","a74","a75","a76","a77","a78","a79","a80","a81","a82","a83","a84","a85","a86","a87","a88","a89","a90","a91","a92","a93","a94","a95","a96","a97","a98","a99","a100","a101","a102","a103","a104","a105","a106","a107","a108","a109","a110","a111","a112","a113","a114","a115","a116","a117","a118","a119","a120","a121","a122","a123","a124","a125","a126","a127","a128","a129","a130","a131","a132","a133","a134","a135","a136","a137","a138","a139","a140","a141","a142","a143","a144","a145","a146","a147","a148","a149","a150","a151","a152","a153","a154","a155","a156","a157","a158","a159","a160","a161","a162","a163","a164","a165","a166","a167","a168","a169","a170","a171","a172","a173","a174","a175","a176","a177","a178","a179","a180","a181","a182","a183","a184","a185","a186","a187","a188","a189","a190","a191","a192","a193","a194","a195","a196","a197","a198","a199","a200","a201","a202","a203","a204","a205","a206","a207","a208","a209","a210","a211","a212","a213","a214","a215","a216","a217","a218","a219","a220","a221","a222","a223","a224","a225","a226","a227","a228","a229","a230","a231","a232","a233","a234","a235","a236","a237","a238","a239","a240","a241","a242","a243","a244","a245","a246","a247","a248","a249","a250","a251","a252","a253","a254","a255","s8","s9","s10","s11","s12","s13","s14","s15","s16","s17","s18","s19","s20","s21","s22","s23","s24","s25","s26","s27","s28","s29","s30","s31","s33","s34","s35","s36","s37","s38","s39","s40","s41","s42","s43","s44","s45","s46","s47","s48","s49","s50","s51","s52","s53","s54","s55","s56","s57","s58","s59","s60","s61","s62","s63","s64","s65","s66","s67","s68","s69","s70","s71","s72","s73","s74","s75","s76","s77","s78","s79","s80","s81","s82","s83","s84","s85","s86","s87","s88","s89","s90","s91","s92","s93","s94","s95","s96","s97","s98","s99","vcc","scc","memory");
}

extern "C" void kernel_launch(void* const* d_in, const int* in_sizes, int n_in, void* d_out, int out_size, void* d_ws, size_t ws_size, hipStream_t stream) {
  mha_fused<<<256, 256, 0, stream>>>((const char*)d_in[0], (const char*)d_in[1], (const char*)d_in[2], (const char*)d_in[3], (const char*)d_in[4],
                                     (char*)d_out, (char*)d_ws);
}
```
